```python
import torch

def _softmax(x, axis=-1):
    return torch.softmax(x, dim=axis)

def _gelu(x):
    return torch.nn.functional.gelu(x, approximate='tanh')

def _silu(x):
    return torch.nn.functional.silu(x)

def _sigmoid(x):
    return torch.sigmoid(x)

def _layernorm(x, axis=-1, eps=1e-05):
    m = x.mean(dim=axis, keepdim=True)
    v = x.var(dim=axis, keepdim=True, unbiased=False)
    return (x - m) / torch.sqrt(v + eps)

def _scatter_add(tgt, idx, src, axis=0):
    return tgt.clone().index_add_(axis, idx, src)

def _segment_sum(src, seg, n):
    out = torch.zeros((n,) + src.shape[1:], dtype=src.dtype)
    return out.index_add_(0, seg, src)

def _pad(x, pw):
    flat = tuple((v for pair in reversed(pw) for v in pair))
    return torch.nn.functional.pad(x, flat)
kernel_name = 'sequential_preLN__memory_aug_recurrent_mem_tr__mixer_channel_mlp__prompt'

def setup_inputs():

    _w = lambda a: (lambda t: t.to(torch.bfloat16) if t.is_floating_point() else t)(torch.as_tensor(a))

    def _A_setup():
        B, S, Dm = (1, 2048, 448)
        H, Dh = (16, 256)
        M, seg = (8, 32)
        seg = min(seg, S)
        S = S // seg * seg
        return {'x': _w((lambda *s: torch.randn(*s, dtype=torch.bfloat16) * 0.05)(B, S, Dm)), 'mem0': _w((lambda *s: torch.randn(*s, dtype=torch.bfloat16) * 0.2)(B, M, Dm)), 'Wq': _w((lambda *s: torch.randn(*s, dtype=torch.bfloat16) * 1.6)(Dm, H, Dh) * Dm ** (-0.5)), 'Wk': _w((lambda *s: torch.randn(*s, dtype=torch.bfloat16) * 1.6)(Dm, H, Dh) * Dm ** (-0.5)), 'Wv': _w((lambda *s: torch.randn(*s, dtype=torch.bfloat16) * 0.2)(Dm, H, Dh) * Dm ** (-0.5)), 'Wo': _w((lambda *s: torch.randn(*s, dtype=torch.bfloat16) * 0.2)(H, Dh, Dm) * (H * Dh) ** (-0.5)), 'seg_len': seg}
    _A_in = _A_setup()

    def _B_setup():
        B, S, D, F = (1, 2048, 448, 1792)
        return {'x': _w((lambda *s: torch.randn(*s, dtype=torch.bfloat16) * 0.05)(B, S, D)), 'w1': _w((lambda *s: torch.randn(*s, dtype=torch.bfloat16) * 0.8)(D, F) * D ** (-0.5)), 'b1': _w((lambda *s: torch.randn(*s, dtype=torch.bfloat16) * 0.4)(F)), 'w2': _w((lambda *s: torch.randn(*s, dtype=torch.bfloat16) * 0.05)(F, D) * F ** (-0.5)), 'b2': _w((lambda *s: torch.randn(*s, dtype=torch.bfloat16) * 0.05)(D))}
    _B_in = _B_setup()
    return {'A_x': _A_in['x'], 'A_mem0': _A_in['mem0'], 'A_Wq': _A_in['Wq'], 'A_Wk': _A_in['Wk'], 'A_Wv': _A_in['Wv'], 'A_Wo': _A_in['Wo'], 'A_seg_len': _A_in['seg_len'], 'B_x': _B_in['x'], 'B_w1': _B_in['w1'], 'B_b1': _B_in['b1'], 'B_w2': _B_in['w2'], 'B_b2': _B_in['b2']}

def reference(A_x, A_mem0, A_Wq, A_Wk, A_Wv, A_Wo, A_seg_len, B_x, B_w1, B_b1, B_w2, B_b2):

    def _A_ref(x, mem0, Wq, Wk, Wv, Wo, seg_len):
        B, S, Dm = x.shape
        H, Dh = (Wq.shape[1], Wq.shape[2])
        M = mem0.shape[1]
        scale = Dh ** (-0.5)
        n_seg = S // seg_len
        L = M + seg_len
        idx = torch.arange(L)
        mask = (idx[:, None] < M) | (idx[:, None] >= idx[None, :])
        mem = mem0
        outs = []
        for s in range(n_seg):
            xs = x[:, s * seg_len:(s + 1) * seg_len, :].clone()
            h = torch.concatenate([mem, xs], axis=1)
            q = torch.einsum('btd,dhe->bhte', h, Wq)
            k = torch.einsum('btd,dhe->bhte', h, Wk)
            v = torch.einsum('btd,dhe->bhte', h, Wv)
            scores = torch.einsum('bhqe,bhke->bhqk', q, k) * scale
            scores = torch.where(mask[None, None], scores, -1000000000.0)
            p = _softmax(scores, axis=-1)
            o = torch.einsum('bhqk,bhke->bhqe', p, v)
            o = torch.einsum('bhte,hed->btd', o, Wo)
            mem = o[:, :M, :].clone()
            outs.append(o[:, M:, :])
        return torch.concatenate(outs, axis=1)

    def _B_ref(x, w1, b1, w2, b2):
        y = _layernorm(x, axis=-1)
        h = _gelu(torch.einsum('bsd,df->bsf', y, w1) + b1)
        return torch.einsum('bsf,fd->bsd', h, w2) + b2
    x = A_x
    h = x + _A_ref(_layernorm(x, axis=-1), A_mem0, A_Wq, A_Wk, A_Wv, A_Wo, A_seg_len)
    h2 = h + _B_ref(_layernorm(h, axis=-1), B_w1, B_b1, B_w2, B_b2)
    return h2
```

```cpp
#include <hip/hip_runtime.h>
#include <hip/hip_bf16.h>

typedef __bf16 bf16x2 __attribute__((ext_vector_type(2)));
typedef __bf16 bf16x4 __attribute__((ext_vector_type(4)));
typedef __bf16 bf16x8 __attribute__((ext_vector_type(8)));
typedef __bf16 bf16x16 __attribute__((ext_vector_type(16)));
typedef float f32x4 __attribute__((ext_vector_type(4)));
typedef unsigned u32x2 __attribute__((ext_vector_type(2)));
typedef unsigned u32x4 __attribute__((ext_vector_type(4)));
typedef unsigned u32x8 __attribute__((ext_vector_type(8)));
typedef __amdgpu_buffer_rsrc_t rsrc_t;
#define GAS __attribute__((address_space(1)))
#define LAS __attribute__((address_space(3)))
#define DEV __device__ __forceinline__
#define RLX_AGENT __ATOMIC_RELAXED, __HIP_MEMORY_SCOPE_AGENT

constexpr int D = 448, NH = 16, DH = 256, NSEG = 64, FF = 1792;
constexpr int SC1 = 16;
constexpr int R = 3;
constexpr int CPS = 2 * R + 1;
constexpr unsigned SENT = 0xFFFFFFFFu;

constexpr int OFF_WF = 0;
constexpr int OFF_XN = 13 << 20;
constexpr int OFF_G = 17 << 20;
constexpr int OFF_U = 45 << 20;
constexpr int OFF_GM = 73 << 20;
constexpr int OFF_UM = 80 << 20;
constexpr int OFF_PXX = 87 << 20;
constexpr int OFF_PXM = 89 << 20;
constexpr int OFF_H = 90 << 20;
constexpr int OFF_Z = 92 << 20;
constexpr int OFF_CTL = 100 << 20;
constexpr int OFF_PROF = 102 << 20;
constexpr int OFF_EX = 104 << 20;
constexpr int WS_BYTES = 128 << 20;
constexpr int C_TMO = 0;
constexpr int FL = 128;
constexpr int C_FI = 4096;
constexpr int C_FG = C_FI + 64 * FL;
constexpr int C_FU = C_FG + 112 * FL;
constexpr int C_FGM = C_FU + 112 * FL;
constexpr int C_FUM = C_FGM + 16 * FL;
constexpr int C_FP = C_FUM + 112 * FL;
constexpr int C_FH = C_FP + 64 * FL;
constexpr int C_FZ = C_FH + 64 * FL;
constexpr int C_END = C_FZ + 224 * FL;
constexpr int C_EPOCH = C_END + 256;
constexpr int C_SLOT = C_EPOCH + 256;
constexpr int C_REC = C_SLOT + 256 * FL;
constexpr int N_INIT = 56;
#define POLL_SLEEP 4
#define QUIET 3000
constexpr unsigned SPIN_CAP = 1u << 17;
constexpr int LDS_BYTES = 160 * 1024;

DEV unsigned pk(float a, float b) { bf16x2 v = {(__bf16)a, (__bf16)b}; return __builtin_bit_cast(unsigned, v); }
DEV float bf_lo(unsigned u) { return __builtin_bit_cast(float, u << 16); }
DEV float bf_hi(unsigned u) { return __builtin_bit_cast(float, u & 0xFFFF0000u); }
DEV bf16x8 as8(u32x4 v) { return __builtin_bit_cast(bf16x8, v); }
DEV f32x4 mfma(u32x4 a, u32x4 b, f32x4 c) { return __builtin_amdgcn_mfma_f32_16x16x32_bf16(as8(a), as8(b), c, 0, 0, 0); }
DEV f32x4 smfmac(u32x4 a, u32x8 b, f32x4 c, int idx) {
  return __builtin_amdgcn_smfmac_f32_16x16x64_bf16(as8(a), __builtin_bit_cast(bf16x16, b), c, idx, 0, 0);
}
DEV u32x8 cat8(u32x4 a, u32x4 b) { return __builtin_shufflevector(a, b, 0, 1, 2, 3, 4, 5, 6, 7); }
DEV u32x4 ldg(rsrc_t r, int voff, int soff) { return __builtin_amdgcn_raw_buffer_load_b128(r, voff, soff, 0); }
DEV u32x4 ldd(rsrc_t r, int voff, int soff) { return __builtin_amdgcn_raw_buffer_load_b128(r, voff, soff, SC1); }
DEV void std16(u32x4 v, rsrc_t r, int voff, int soff) { __builtin_amdgcn_raw_buffer_store_b128(v, r, voff, soff, SC1); asm volatile("s_nop 1" :: "v"(v) : "memory"); }
DEV void std8(u32x2 v, rsrc_t r, int voff, int soff) { __builtin_amdgcn_raw_buffer_store_b64(v, r, voff, soff, SC1); }
DEV void std4(unsigned v, rsrc_t r, int voff, int soff) { __builtin_amdgcn_raw_buffer_store_b32(v, r, voff, soff, SC1); }
DEV void fence_c() { asm volatile("" ::: "memory"); }
DEV void drain() { asm volatile("s_waitcnt vmcnt(0)" ::: "memory"); }
DEV void bar() { asm volatile("s_waitcnt lgkmcnt(0)\n\ts_barrier" ::: "memory"); }
DEV unsigned xcc_id() { return (unsigned)__builtin_amdgcn_s_getreg(20 | (3 << 11)) & 0xfu; }
DEV unsigned clk() { return (unsigned)__builtin_amdgcn_s_memtime(); }
DEV float fbits(unsigned u) { return __builtin_bit_cast(float, u); }
DEV unsigned ubits(float f) { return __builtin_bit_cast(unsigned, f); }
DEV float ror8(float x) { return fbits((unsigned)__builtin_amdgcn_update_dpp(0, (int)ubits(x), 0x128, 0xf, 0xf, true)); }
DEV float swp1(float x) { return fbits((unsigned)__builtin_amdgcn_update_dpp(0, (int)ubits(x), 0xB1, 0xf, 0xf, true)); }
DEV void swap32(float& a, float& b) { auto r = __builtin_amdgcn_permlane32_swap(ubits(a), ubits(b), false, false); a = fbits(r[0]); b = fbits(r[1]); }
DEV void swap16(float& a, float& b) { auto r = __builtin_amdgcn_permlane16_swap(ubits(a), ubits(b), false, false); a = fbits(r[0]); b = fbits(r[1]); }
DEV float sum_x16(float x) { float y = x; swap16(x, y); return x + y; }
DEV float sum_x32(float x) { float y = x; swap32(x, y); return x + y; }
DEV float max_x16(float x) { float y = x; swap16(x, y); return fmaxf(x, y); }
DEV float max_x32(float x) { float y = x; swap32(x, y); return fmaxf(x, y); }
DEV unsigned umax3(unsigned a, unsigned b, unsigned c) { return max(max(a, b), c); }
DEV unsigned umax4(u32x4 v, unsigned m) { return umax3(umax3(v.x, v.y, m), v.z, v.w); }
DEV void dma16(unsigned lds_addr, int voff, rsrc_t r, int soff) {
  asm volatile("s_mov_b32 m0, %0\n\ts_nop 0\n\tbuffer_load_dwordx4 %1, %2, %3 offen lds" :: "s"(lds_addr), "v"(voff), "s"(r), "s"(soff) : "m0", "memory");
}
DEV void dma16d(unsigned lds_addr, int voff, rsrc_t r, int soff) {
  asm volatile("s_mov_b32 m0, %0\n\ts_nop 0\n\tbuffer_load_dwordx4 %1, %2, %3 offen sc1 lds" :: "s"(lds_addr), "v"(voff), "s"(r), "s"(soff) : "m0", "memory");
}
DEV u32x4 lds16(const char* smem, int off) { return *(const u32x4*)(smem + off); }
DEV u32x2 lds_tr(const char* smem, int off) {
  return __builtin_bit_cast(u32x2, __builtin_amdgcn_ds_read_tr16_b64_v4bf16((LAS bf16x4*)(smem + off)));
}
DEV bool give_up(unsigned& spins, rsrc_t ws, int lane) {
  if (++spins > SPIN_CAP) { if (lane == 0) std4(1u, ws, C_TMO, OFF_CTL); return true; }
  if ((spins & 255u) == 0u) return __builtin_amdgcn_readfirstlane(__builtin_amdgcn_raw_buffer_load_b32(ws, C_TMO, OFF_CTL, SC1)) != 0;
  return false;
}
DEV bool wait_words(rsrc_t ws, int voff, unsigned seg, int n, int lane) {
  for (unsigned spins = 0;;) {
    fence_c();
    unsigned v = ~0u;
    if (lane < n) v = __builtin_amdgcn_raw_buffer_load_b32(ws, voff, OFF_CTL, SC1);
    if (__all(v > seg)) break;
    if (give_up(spins, ws, lane)) return false;
    __builtin_amdgcn_s_sleep(3);
  }
  fence_c();
  return true;
}
DEV bool wait_cached2(rsrc_t ws, int voff, int need, int n, int lane, unsigned& seen, bool tight = false) {
  for (unsigned spins = 0; !__all(lane >= n || (int)seen > need);) {
    if (spins) { if (give_up(spins, ws, lane)) return false; if (!tight) __builtin_amdgcn_s_sleep(POLL_SLEEP); } else spins = 1;
    fence_c();
    if (lane < n) seen = __builtin_amdgcn_raw_buffer_load_b32(ws, voff, OFF_CTL, SC1);
  }
  fence_c();
  return true;
}
DEV bool wait_paced(rsrc_t ws, int voff, int need, int n, int lane, unsigned& seen, unsigned& last, unsigned quiet, bool tight) {
  bool waited = false;
  if (!__all(lane >= n || (int)seen > need)) {
    for (int i = 0; i < 64 && clk() - last < quiet; ++i) __builtin_amdgcn_s_sleep(4);
    fence_c();
    if (lane < n) seen = __builtin_amdgcn_raw_buffer_load_b32(ws, voff, OFF_CTL, SC1);
    waited = !__all(lane >= n || (int)seen > need);
  }
  const bool ok = wait_cached2(ws, voff, need, n, lane, seen, tight);
  last = clk() - (waited ? 0u : 1u << 20);
  return ok;
}
DEV bool wait_cached(rsrc_t ws, int voff, unsigned seg, int n, int lane, unsigned& seen) { return wait_cached2(ws, voff, (int)seg, n, lane, seen); }
DEV bool wait_flags(rsrc_t ws, int coff, int n, int seg, int lane) { return wait_words(ws, coff + lane * FL, (unsigned)seg, n, lane); }
DEV void put_flag(rsrc_t ws, int coff, int seg, int lane) { if (lane == 0) std4((unsigned)seg + 1u, ws, coff, OFF_CTL); }
DEV void set_flag(rsrc_t ws, int coff, int seg, int lane) { drain(); put_flag(ws, coff, seg, lane); }
DEV void wait_clk(unsigned t0, unsigned dt) { for (int i = 0; i < 4096 && clk() - t0 < dt; ++i) {} }

__device__ __attribute__((noinline, used, visibility("hidden"))) void text_end_marker();
DEV unsigned warm_text(int slice, int lane) {
  unsigned long long pc;
  asm volatile("s_getpc_b64 %0" : "=s"(pc));
  pc &= ~127ull;
  const unsigned long long len = (unsigned long long)(size_t)&text_end_marker - pc;
  const unsigned off = (unsigned)(slice * 64 + lane) * 128u;
  return len < (1ull << 20) && off < len ? *(const GAS unsigned*)(pc + off) : 0u;
}

struct Ex { int base; unsigned w0, par; };
DEV int ex_chunk(const Ex& e, int n) {
  const unsigned o = (unsigned)n * 16384u;
  const unsigned w = ((e.w0 & 0x1FFFFFu) + o) >> 21;
  return e.base + (int)o + (int)(((e.par >> w) & 1u) << 13);
}
DEV int ex_base(const void* wsp) { return OFF_EX + (int)((0u - (unsigned)(size_t)wsp - (unsigned)OFF_EX) & 0x7FFFu); }

#define K1STAMP(i) do {} while (0)
#define CHAIN_CLOBBERS "v0", "v1", "v2", "v3", "v4", "v5", "v6", "v7", "v8", "v9", "v10", "v11", "v12", "v13", "v14", "v15", "v16", "v17", "v18", "v19", "v20", "v21", "v22", "v23", "v24", "v25", "v26", "v27", "v28", "v29", "v30", "v31", "v32", "v33", "v34", "v35", "v36", "v37", "v38", "v39", "v40", "v41", "v42", "v43", "v44", "v45", "v46", "v47", "v48", "v49", "v50", "v51", "v52", "v53", "v54", "v55", "v56", "v57", "v58", "v59", "v60", "v61", "v62", "v63", "v64", "v65", "v66", "v67", "v68", "v69", "v70", "v71", "v72", "v73", "v74", "v75", "v76", "v77", "v78", "v79", "v80", "v81", "v82", "v83", "v84", "v85", "v86", "v87", "v88", "v89", "v90", "v91", "v92", "v93", "v94", "v95", "v96", "v97", "v98", "v99", "v100", "v101", "v102", "v103", "v104", "v105", "v106", "v107", "v108", "v109", "v110", "v111", "v112", "v113", "v114", "v115", "v116", "v117", "v118", "v119", "v120", "v121", "v122", "v123", "v124", "v125", "v126", "v127", "v128", "v129", "v130", "v131", "v132", "v133", "v134", "v135", "v136", "v137", "v138", "v139", "v140", "v141", "v142", "v143", "v144", "v145", "v146", "v147", "v148", "v149", "v150", "v151", "v152", "v153", "v154", "v155", "v156", "v157", "v158", "v159", "v160", "v161", "v162", "v163", "v164", "v165", "v166", "v167", "v168", "v169", "v170", "v171", "v172", "v173", "v174", "v175", "v176", "v177", "v178", "v179", "v180", "v181", "v182", "v183", "v184", "v185", "v186", "v187", "v188", "v189", "v190", "v191", "v192", "v193", "v194", "v195", "v196", "v197", "v198", "v199", "v200", "v201", "v202", "v203", "v204", "v205", "v206", "v207", "v208", "v209", "v210", "v211", "v212", "v213", "v214", "v215", "v216", "v217", "v218", "v219", "v220", "v221", "v222", "v223", "v224", "v225", "v226", "v227", "v228", "v229", "v230", "v231", "v232", "v233", "v234", "v235", "v236", "v237", "v238", "v239", "v240", "v241", "v242", "v243", "v244", "v245", "v246", "v247", "v248", "v249", "v250", "v251", "v252", "v253", "v254", "v255", "a0", "a1", "a2", "a3", "a4", "a5", "a6", "a7", "a8", "a9", "a10", "a11", "a12", "a13", "a14", "a15", "a16", "a17", "a18", "a19", "a20", "a21", "a22", "a23", "a24", "a25", "a26", "a27", "a28", "a29", "a30", "a31", "a32", "a33", "a34", "a35", "a36", "a37", "a38", "a39", "a40", "a41", "a42", "a43", "a44", "a45", "a46", "a47", "a48", "a49", "a50", "a51", "a52", "a53", "a54", "a55", "a56", "a57", "a58", "a59", "a60", "a61", "a62", "a63", "a64", "a65", "a66", "a67", "a68", "a69", "a70", "a71", "a72", "a73", "a74", "a75", "a76", "a77", "a78", "a79", "a80", "a81", "a82", "a83", "a84", "a85", "a86", "a87", "a88", "a89", "a90", "a91", "a92", "a93", "a94", "a95", "a96", "a97", "a98", "a99", "a100", "a101", "a102", "a103", "a104", "a105", "a106", "a107", "a108", "a109", "a110", "a111", "a112", "a113", "a114", "a115", "a116", "a117", "a118", "a119", "a120", "a121", "a122", "a123", "a124", "a125", "a126", "a127", "a128", "a129", "a130", "a131", "a132", "a133", "a134", "a135", "a136", "a137", "a138", "a139", "a140", "a141", "a142", "a143", "a144", "a145", "a146", "a147", "a148", "a149", "a150", "a151", "a152", "a153", "a154", "a155", "a156", "a157", "a158", "a159", "a160", "a161", "a162", "a163", "a164", "a165", "a166", "a167", "a168", "a169", "a170", "a171", "a172", "a173", "a174", "a175", "a176", "a177", "a178", "a179", "a180", "a181", "a182", "a183", "a184", "a185", "a186", "a187", "a188", "a189", "a190", "a191", "a192", "a193", "a194", "a195", "a196", "a197", "a198", "a199", "a200", "a201", "a202", "a203", "a204", "a205", "a206", "a207", "a208", "a209", "a210", "a211", "a212", "a213", "a214", "a215", "a216", "a217", "a218", "a219", "a220", "a221", "a222", "a223", "a224", "a225", "a226", "a227", "a228", "a229", "a230", "a231", "a232", "a233", "a234", "a235", "a236", "a237", "a238", "a239", "a240", "a241", "a242", "a243", "a244", "a245", "a246", "a247", "a248", "a249", "a250", "a251", "a252", "a253", "a254", "a255", "s40", "s41", "s42", "s43", "s44", "s45", "s46", "s47", "s48", "s49", "s50", "s51", "s52", "s53", "s54", "s55", "s56", "s57", "s58", "s59", "s60", "s61", "s62", "s63", "s64", "s65", "s66", "s67", "s68", "s69", "s70", "s71", "s72", "s73", "s74", "s75", "s76", "s77", "s78", "s79", "s80", "s81", "s82", "s83", "s84", "s85", "s86", "s87", "s88", "s89", "s90", "s91", "s92", "s93", "s94", "s95", "s96", "s97", "s98", "s99", "s100", "s101", "vcc", "scc", "m0", "memory"
#define FOLD_CLOBBERS "v12", "v13", "v14", "v15", "v16", "v17", "v18", "v19", "v20", "v21", "v22", "v23", "v24", "v25", "v26", "v27", "v28", "v29", "v30", "v31", "v32", "v33", "v34", "v35", "v36", "v37", "v38", "v39", "v40", "v41", "v42", "v43", "v44", "v45", "v46", "v47", "v48", "v49", "v50", "v51", "v52", "v53", "v54", "v55", "v56", "v57", "v58", "v59", "v60", "v61", "v62", "v63", "v64", "v65", "v66", "v67", "v68", "v69", "v70", "v71", "v72", "v73", "v74", "v75", "v76", "v77", "v78", "v79", "v80", "v81", "v82", "v83", "v84", "v85", "v86", "v87", "v88", "v89", "v90", "v91", "v92", "v93", "v94", "v95", "v96", "v97", "v98", "v99", "v100", "v101", "v102", "v103", "v104", "v105", "v106", "v107", "v108", "v109", "v110", "v111", "v112", "v113", "v114", "v115", "v116", "v117", "v118", "v119", "v120", "v121", "v122", "v123", "v124", "v125", "v126", "v127", "v128", "v129", "v130", "v131", "v132", "v133", "v134", "v135", "v136", "v137", "v138", "v139", "v140", "v141", "v142", "v143", "v144", "v145", "v146", "v147", "v148", "v149", "v150", "v151", "v152", "v153", "v154", "v155", "v156", "v157", "v158", "v159", "v160", "v161", "v162", "v163", "v164", "v165", "v166", "v167", "v168", "v169", "v170", "v171", "v172", "v173", "v174", "v175", "v176", "v177", "v178", "v179", "v180", "v181", "v182", "v183", "v184", "v185", "v186", "v187", "v188", "v189", "v190", "v191", "v192", "v193", "v194", "v195", "v196", "v197", "v198", "v199", "v200", "v201", "v202", "v203", "v204", "v205", "v206", "v207", "v208", "v209", "v210", "v211", "v212", "v213", "v214", "v215", "v216", "v217", "v218", "v219", "v220", "v221", "v222", "v223", "v224", "v225", "v226", "v227", "v228", "v229", "v230", "v231", "v232", "v233", "v234", "v235", "v236", "v237", "v238", "v239", "v240", "v241", "v242", "v243", "v244", "v245", "v246", "v247", "v248", "v249", "v250", "v251", "v252", "s40", "s41", "s42", "s43", "s44", "s45", "s46", "s47", "s48", "s49", "s50", "s51", "s52", "s53", "scc", "m0", "memory"
#define ASM_FOLDG \
  "s_mov_b32 s44, 0x3db8aa3b\n" \
  "s_mov_b32 s45, 0x3db8aa3b\n" \
  "s_add_i32 s46, %5, 0\n" \
  "s_sub_i32 s40, s46, 7\n" \
  "s_cmp_lt_i32 s40, 0\n" \
  "s_cselect_b32 s46, s46, s40\n" \
  "s_add_i32 s47, %5, 1\n" \
  "s_sub_i32 s40, s47, 7\n" \
  "s_cmp_lt_i32 s40, 0\n" \
  "s_cselect_b32 s47, s47, s40\n" \
  "s_add_i32 s48, %5, 2\n" \
  "s_sub_i32 s40, s48, 7\n" \
  "s_cmp_lt_i32 s40, 0\n" \
  "s_cselect_b32 s48, s48, s40\n" \
  "s_add_i32 s49, %5, 3\n" \
  "s_sub_i32 s40, s49, 7\n" \
  "s_cmp_lt_i32 s40, 0\n" \
  "s_cselect_b32 s49, s49, s40\n" \
  "s_add_i32 s50, %5, 4\n" \
  "s_sub_i32 s40, s50, 7\n" \
  "s_cmp_lt_i32 s40, 0\n" \
  "s_cselect_b32 s50, s50, s40\n" \
  "s_add_i32 s51, %5, 5\n" \
  "s_sub_i32 s40, s51, 7\n" \
  "s_cmp_lt_i32 s40, 0\n" \
  "s_cselect_b32 s51, s51, s40\n" \
  "s_add_i32 s52, %5, 6\n" \
  "s_sub_i32 s40, s52, 7\n" \
  "s_cmp_lt_i32 s40, 0\n" \
  "s_cselect_b32 s52, s52, s40\n" \
  "v_xor_b32 v248, 0, %6\n" \
  "v_xor_b32 v249, 64, %6\n" \
  "v_xor_b32 v250, 128, %6\n" \
  "v_xor_b32 v251, 192, %6\n" \
  "s_sub_i32 s53, %14, %4\n" \
  "v_add_u32 v12, s53, v248\n" \
  "v_add_u32 v13, s53, v249\n" \
  "v_add_u32 v14, s53, v250\n" \
  "v_add_u32 v15, s53, v251\n" \
  "ds_read_b128 v[16:19], %7 offset:0\n" \
  "ds_read_b128 v[20:23], %7 offset:8192\n" \
  "ds_read_b128 v[24:27], %7 offset:16384\n" \
  "ds_read_b128 v[28:31], %7 offset:24576\n" \
  "ds_read_b128 v[144:147], v248 offset:0\n" \
  "s_waitcnt lgkmcnt(0)\n" \
  "v_mfma_f32_16x16x32_bf16 v[208:211], v[144:147], v[16:19], 0\n" \
  "ds_read_b128 v[32:35], %8 offset:0\n" \
  "ds_read_b128 v[36:39], %8 offset:8192\n" \
  "ds_read_b128 v[40:43], %8 offset:16384\n" \
  "v_mfma_f32_16x16x32_bf16 v[212:215], v[144:147], v[20:23], 0\n" \
  "ds_read_b128 v[44:47], %8 offset:24576\n" \
  "ds_read_b128 v[148:151], v249 offset:0\n" \
  "v_mfma_f32_16x16x32_bf16 v[216:219], v[144:147], v[24:27], 0\n" \
  "v_mfma_f32_16x16x32_bf16 v[220:223], v[144:147], v[28:31], 0\n" \
  "s_waitcnt lgkmcnt(0)\n" \
  "v_mfma_f32_16x16x32_bf16 v[208:211], v[148:151], v[32:35], v[208:211]\n" \
  "ds_read_b128 v[48:51], %9 offset:0\n" \
  "ds_read_b128 v[52:55], %9 offset:8192\n" \
  "ds_read_b128 v[56:59], %9 offset:16384\n" \
  "v_mfma_f32_16x16x32_bf16 v[212:215], v[148:151], v[36:39], v[212:215]\n" \
  "ds_read_b128 v[60:63], %9 offset:24576\n" \
  "ds_read_b128 v[152:155], v250 offset:0\n" \
  "v_mfma_f32_16x16x32_bf16 v[216:219], v[148:151], v[40:43], v[216:219]\n" \
  "v_mfma_f32_16x16x32_bf16 v[220:223], v[148:151], v[44:47], v[220:223]\n" \
  "s_waitcnt lgkmcnt(0)\n" \
  "v_mfma_f32_16x16x32_bf16 v[208:211], v[152:155], v[48:51], v[208:211]\n" \
  "ds_read_b128 v[64:67], %10 offset:0\n" \
  "ds_read_b128 v[68:71], %10 offset:8192\n" \
  "ds_read_b128 v[72:75], %10 offset:16384\n" \
  "v_mfma_f32_16x16x32_bf16 v[212:215], v[152:155], v[52:55], v[212:215]\n" \
  "ds_read_b128 v[76:79], %10 offset:24576\n" \
  "ds_read_b128 v[156:159], v251 offset:0\n" \
  "v_mfma_f32_16x16x32_bf16 v[216:219], v[152:155], v[56:59], v[216:219]\n" \
  "v_mfma_f32_16x16x32_bf16 v[220:223], v[152:155], v[60:63], v[220:223]\n" \
  "s_waitcnt lgkmcnt(0)\n" \
  "v_mfma_f32_16x16x32_bf16 v[208:211], v[156:159], v[64:67], v[208:211]\n" \
  "ds_read_b128 v[80:83], %7 offset:256\n" \
  "ds_read_b128 v[84:87], %7 offset:8448\n" \
  "ds_read_b128 v[88:91], %7 offset:16640\n" \
  "v_mfma_f32_16x16x32_bf16 v[212:215], v[156:159], v[68:71], v[212:215]\n" \
  "ds_read_b128 v[92:95], %7 offset:24832\n" \
  "ds_read_b128 v[160:163], v248 offset:256\n" \
  "v_mfma_f32_16x16x32_bf16 v[216:219], v[156:159], v[72:75], v[216:219]\n" \
  "v_mfma_f32_16x16x32_bf16 v[220:223], v[156:159], v[76:79], v[220:223]\n" \
  "s_waitcnt lgkmcnt(0)\n" \
  "v_mfma_f32_16x16x32_bf16 v[208:211], v[160:163], v[80:83], v[208:211]\n" \
  "ds_read_b128 v[96:99], %8 offset:256\n" \
  "ds_read_b128 v[100:103], %8 offset:8448\n" \
  "ds_read_b128 v[104:107], %8 offset:16640\n" \
  "v_mfma_f32_16x16x32_bf16 v[212:215], v[160:163], v[84:87], v[212:215]\n" \
  "ds_read_b128 v[108:111], %8 offset:24832\n" \
  "ds_read_b128 v[164:167], v249 offset:256\n" \
  "v_mfma_f32_16x16x32_bf16 v[216:219], v[160:163], v[88:91], v[216:219]\n" \
  "v_mfma_f32_16x16x32_bf16 v[220:223], v[160:163], v[92:95], v[220:223]\n" \
  "s_waitcnt lgkmcnt(0)\n" \
  "v_mfma_f32_16x16x32_bf16 v[208:211], v[164:167], v[96:99], v[208:211]\n" \
  "ds_read_b128 v[112:115], %9 offset:256\n" \
  "ds_read_b128 v[116:119], %9 offset:8448\n" \
  "ds_read_b128 v[120:123], %9 offset:16640\n" \
  "v_mfma_f32_16x16x32_bf16 v[212:215], v[164:167], v[100:103], v[212:215]\n" \
  "ds_read_b128 v[124:127], %9 offset:24832\n" \
  "ds_read_b128 v[168:171], v250 offset:256\n" \
  "v_mfma_f32_16x16x32_bf16 v[216:219], v[164:167], v[104:107], v[216:219]\n" \
  "v_mfma_f32_16x16x32_bf16 v[220:223], v[164:167], v[108:111], v[220:223]\n" \
  "s_waitcnt lgkmcnt(0)\n" \
  "v_mfma_f32_16x16x32_bf16 v[208:211], v[168:171], v[112:115], v[208:211]\n" \
  "ds_read_b128 v[128:131], %10 offset:256\n" \
  "ds_read_b128 v[132:135], %10 offset:8448\n" \
  "ds_read_b128 v[136:139], %10 offset:16640\n" \
  "v_mfma_f32_16x16x32_bf16 v[212:215], v[168:171], v[116:119], v[212:215]\n" \
  "ds_read_b128 v[140:143], %10 offset:24832\n" \
  "ds_read_b128 v[172:175], v251 offset:256\n" \
  "v_mfma_f32_16x16x32_bf16 v[216:219], v[168:171], v[120:123], v[216:219]\n" \
  "v_mfma_f32_16x16x32_bf16 v[220:223], v[168:171], v[124:127], v[220:223]\n" \
  "s_waitcnt lgkmcnt(0)\n" \
  "v_mfma_f32_16x16x32_bf16 v[208:211], v[172:175], v[128:131], v[208:211]\n" \
  "s_waitcnt vmcnt(16)\n" \
  "ds_read_b128 v[176:179], v248 offset:8192\n" \
  "ds_read_b128 v[180:183], v249 offset:8192\n" \
  "v_mfma_f32_16x16x32_bf16 v[212:215], v[172:175], v[132:135], v[212:215]\n" \
  "ds_read_b128 v[184:187], v250 offset:8192\n" \
  "ds_read_b128 v[188:191], v251 offset:8192\n" \
  "ds_read_b128 v[192:195], v248 offset:8448\n" \
  "v_mfma_f32_16x16x32_bf16 v[216:219], v[172:175], v[136:139], v[216:219]\n" \
  "ds_read_b128 v[196:199], v249 offset:8448\n" \
  "ds_read_b128 v[200:203], v250 offset:8448\n" \
  "ds_read_b128 v[204:207], v251 offset:8448\n" \
  "v_mfma_f32_16x16x32_bf16 v[220:223], v[172:175], v[140:143], v[220:223]\n" \
  "s_waitcnt lgkmcnt(7)\n" \
  "v_mfma_f32_16x16x32_bf16 v[224:227], v[176:179], v[16:19], 0\n" \
  "s_barrier\n" \
  "s_lshl_b32 s43, s51, 17\n" \
  "s_add_i32 s43, s43, %3\n" \
  "v_mfma_f32_16x16x32_bf16 v[228:231], v[176:179], v[20:23], 0\n" \
  "s_add_i32 m0, %4, 0\n" \
  "v_xor_b32 v252, 0, %2\n" \
  "s_add_i32 s40, s43, 0\n" \
  "buffer_load_dwordx4 v252, %1, s40 offen lds\n" \
  "s_add_i32 m0, %4, 1024\n" \
  "v_xor_b32 v252, 32, %2\n" \
  "s_add_i32 s40, s43, 16384\n" \
  "buffer_load_dwordx4 v252, %1, s40 offen lds\n" \
  "v_mfma_f32_16x16x32_bf16 v[232:235], v[176:179], v[24:27], 0\n" \
  "s_add_i32 m0, %4, 2048\n" \
  "v_xor_b32 v252, 64, %2\n" \
  "s_add_i32 s40, s43, 32768\n" \
  "buffer_load_dwordx4 v252, %1, s40 offen lds\n" \
  "s_add_i32 m0, %4, 3072\n" \
  "v_xor_b32 v252, 96, %2\n" \
  "s_add_i32 s40, s43, 49152\n" \
  "buffer_load_dwordx4 v252, %1, s40 offen lds\n" \
  "v_mfma_f32_16x16x32_bf16 v[236:239], v[176:179], v[28:31], 0\n" \
  "s_add_i32 m0, %4, 4096\n" \
  "v_xor_b32 v252, 128, %2\n" \
  "s_add_i32 s40, s43, 65536\n" \
  "buffer_load_dwordx4 v252, %1, s40 offen lds\n" \
  "s_add_i32 m0, %4, 5120\n" \
  "v_xor_b32 v252, 160, %2\n" \
  "s_add_i32 s40, s43, 81920\n" \
  "buffer_load_dwordx4 v252, %1, s40 offen lds\n" \
  "s_waitcnt lgkmcnt(6)\n" \
  "v_mfma_f32_16x16x32_bf16 v[224:227], v[180:183], v[32:35], v[224:227]\n" \
  "s_add_i32 m0, %4, 6144\n" \
  "v_xor_b32 v252, 192, %2\n" \
  "s_add_i32 s40, s43, 98304\n" \
  "buffer_load_dwordx4 v252, %1, s40 offen lds\n" \
  "s_add_i32 m0, %4, 7168\n" \
  "v_xor_b32 v252, 224, %2\n" \
  "s_add_i32 s40, s43, 114688\n" \
  "buffer_load_dwordx4 v252, %1, s40 offen lds\n" \
  "v_mfma_f32_16x16x32_bf16 v[228:231], v[180:183], v[36:39], v[228:231]\n" \
  "s_lshl_b32 s43, s50, 17\n" \
  "s_add_i32 s43, s43, %3\n" \
  "s_add_i32 m0, %14, 0\n" \
  "v_xor_b32 v252, 0, %2\n" \
  "s_add_i32 s40, s43, 0\n" \
  "buffer_load_dwordx4 v252, %1, s40 offen lds\n" \
  "v_mfma_f32_16x16x32_bf16 v[232:235], v[180:183], v[40:43], v[232:235]\n" \
  "s_add_i32 m0, %14, 1024\n" \
  "v_xor_b32 v252, 32, %2\n" \
  "s_add_i32 s40, s43, 16384\n" \
  "buffer_load_dwordx4 v252, %1, s40 offen lds\n" \
  "s_add_i32 m0, %14, 2048\n" \
  "v_xor_b32 v252, 64, %2\n" \
  "s_add_i32 s40, s43, 32768\n" \
  "buffer_load_dwordx4 v252, %1, s40 offen lds\n" \
  "v_mfma_f32_16x16x32_bf16 v[236:239], v[180:183], v[44:47], v[236:239]\n" \
  "s_add_i32 m0, %14, 3072\n" \
  "v_xor_b32 v252, 96, %2\n" \
  "s_add_i32 s40, s43, 49152\n" \
  "buffer_load_dwordx4 v252, %1, s40 offen lds\n" \
  "s_add_i32 m0, %14, 4096\n" \
  "v_xor_b32 v252, 128, %2\n" \
  "s_add_i32 s40, s43, 65536\n" \
  "buffer_load_dwordx4 v252, %1, s40 offen lds\n" \
  "s_waitcnt lgkmcnt(5)\n" \
  "v_mfma_f32_16x16x32_bf16 v[224:227], v[184:187], v[48:51], v[224:227]\n" \
  "s_add_i32 m0, %14, 5120\n" \
  "v_xor_b32 v252, 160, %2\n" \
  "s_add_i32 s40, s43, 81920\n" \
  "buffer_load_dwordx4 v252, %1, s40 offen lds\n" \
  "s_add_i32 m0, %14, 6144\n" \
  "v_xor_b32 v252, 192, %2\n" \
  "s_add_i32 s40, s43, 98304\n" \
  "buffer_load_dwordx4 v252, %1, s40 offen lds\n" \
  "v_mfma_f32_16x16x32_bf16 v[228:231], v[184:187], v[52:55], v[228:231]\n" \
  "s_add_i32 m0, %14, 7168\n" \
  "v_xor_b32 v252, 224, %2\n" \
  "s_add_i32 s40, s43, 114688\n" \
  "buffer_load_dwordx4 v252, %1, s40 offen lds\n" \
  "s_waitcnt lgkmcnt(0)\n" \
  "v_mfma_f32_16x16x32_bf16 v[232:235], v[184:187], v[56:59], v[232:235]\n" \
  "s_lshl_b32 s43, s52, 17\n" \
  "s_add_i32 s43, s43, %3\n" \
  "s_add_i32 m0, %4, 8192\n" \
  "v_xor_b32 v252, 0, %2\n" \
  "s_add_i32 s40, s43, 0\n" \
  "buffer_load_dwordx4 v252, %1, s40 offen lds\n" \
  "v_mfma_f32_16x16x32_bf16 v[236:239], v[184:187], v[60:63], v[236:239]\n" \
  "s_add_i32 m0, %4, 9216\n" \
  "v_xor_b32 v252, 32, %2\n" \
  "s_add_i32 s40, s43, 16384\n" \
  "buffer_load_dwordx4 v252, %1, s40 offen lds\n" \
  "s_add_i32 m0, %4, 10240\n" \
  "v_xor_b32 v252, 64, %2\n" \
  "s_add_i32 s40, s43, 32768\n" \
  "buffer_load_dwordx4 v252, %1, s40 offen lds\n" \
  "s_waitcnt lgkmcnt(4)\n" \
  "v_mfma_f32_16x16x32_bf16 v[224:227], v[188:191], v[64:67], v[224:227]\n" \
  "s_add_i32 m0, %4, 11264\n" \
  "v_xor_b32 v252, 96, %2\n" \
  "s_add_i32 s40, s43, 49152\n" \
  "buffer_load_dwordx4 v252, %1, s40 offen lds\n" \
  "s_add_i32 m0, %4, 12288\n" \
  "v_xor_b32 v252, 128, %2\n" \
  "s_add_i32 s40, s43, 65536\n" \
  "buffer_load_dwordx4 v252, %1, s40 offen lds\n" \
  "v_mfma_f32_16x16x32_bf16 v[228:231], v[188:191], v[68:71], v[228:231]\n" \
  "s_add_i32 m0, %4, 13312\n" \
  "v_xor_b32 v252, 160, %2\n" \
  "s_add_i32 s40, s43, 81920\n" \
  "buffer_load_dwordx4 v252, %1, s40 offen lds\n" \
  "s_add_i32 m0, %4, 14336\n" \
  "v_xor_b32 v252, 192, %2\n" \
  "s_add_i32 s40, s43, 98304\n" \
  "buffer_load_dwordx4 v252, %1, s40 offen lds\n" \
  "v_mfma_f32_16x16x32_bf16 v[232:235], v[188:191], v[72:75], v[232:235]\n" \
  "s_add_i32 m0, %4, 15360\n" \
  "v_xor_b32 v252, 224, %2\n" \
  "s_add_i32 s40, s43, 114688\n" \
  "buffer_load_dwordx4 v252, %1, s40 offen lds\n" \
  "v_mfma_f32_16x16x32_bf16 v[236:239], v[188:191], v[76:79], v[236:239]\n" \
  "s_waitcnt lgkmcnt(3)\n" \
  "v_mfma_f32_16x16x32_bf16 v[224:227], v[192:195], v[80:83], v[224:227]\n" \
  "s_add_i32 s41, %13, s46\n" \
  "s_lshr_b32 s42, s41, 1\n" \
  "s_and_b32 s41, s41, 1\n" \
  "s_lshl_b32 s42, s42, 11\n" \
  "s_lshl_b32 s41, s41, 9\n" \
  "s_add_i32 s41, s41, s42\n" \
  "s_add_i32 s41, s41, %12\n" \
  "s_add_i32 s42, s41, 28672\n" \
  "v_mfma_f32_16x16x32_bf16 v[228:231], v[192:195], v[84:87], v[228:231]\n" \
  "v_mul_f32 v208, s44, v208\n" \
  "v_mul_f32 v209, s44, v209\n" \
  "v_mfma_f32_16x16x32_bf16 v[232:235], v[192:195], v[88:91], v[232:235]\n" \
  "v_mul_f32 v210, s44, v210\n" \
  "v_mul_f32 v211, s44, v211\n" \
  "v_mfma_f32_16x16x32_bf16 v[236:239], v[192:195], v[92:95], v[236:239]\n" \
  "v_mul_f32 v212, s44, v212\n" \
  "v_mul_f32 v213, s44, v213\n" \
  "s_waitcnt lgkmcnt(2)\n" \
  "v_mfma_f32_16x16x32_bf16 v[224:227], v[196:199], v[96:99], v[224:227]\n" \
  "v_mul_f32 v214, s44, v214\n" \
  "v_mul_f32 v215, s44, v215\n" \
  "s_waitcnt vmcnt(32)\n" \
  "v_mfma_f32_16x16x32_bf16 v[228:231], v[196:199], v[100:103], v[228:231]\n" \
  "v_cvt_pk_bf16_f32 v240, v208, v209\n" \
  "v_cvt_pk_bf16_f32 v241, v212, v213\n" \
  "ds_read_b128 v[144:147], v248 offset:16384\n" \
  "v_mfma_f32_16x16x32_bf16 v[232:235], v[196:199], v[104:107], v[232:235]\n" \
  "v_cvt_pk_bf16_f32 v242, v210, v211\n" \
  "v_cvt_pk_bf16_f32 v243, v214, v215\n" \
  "ds_read_b128 v[148:151], v249 offset:16384\n" \
  "v_mfma_f32_16x16x32_bf16 v[236:239], v[196:199], v[108:111], v[236:239]\n" \
  "buffer_store_dwordx4 v[240:243], %11, %0, s41 offen sc1\n" \
  "v_mul_f32 v216, s44, v216\n" \
  "ds_read_b128 v[152:155], v250 offset:16384\n" \
  "s_waitcnt lgkmcnt(4)\n" \
  "v_mfma_f32_16x16x32_bf16 v[224:227], v[200:203], v[112:115], v[224:227]\n" \
  "v_mul_f32 v217, s44, v217\n" \
  "v_mul_f32 v218, s44, v218\n" \
  "ds_read_b128 v[156:159], v251 offset:16384\n" \
  "v_mfma_f32_16x16x32_bf16 v[228:231], v[200:203], v[116:119], v[228:231]\n" \
  "v_mul_f32 v219, s44, v219\n" \
  "v_mul_f32 v220, s44, v220\n" \
  "ds_read_b128 v[160:163], v248 offset:16640\n" \
  "v_mfma_f32_16x16x32_bf16 v[232:235], v[200:203], v[120:123], v[232:235]\n" \
  "v_mul_f32 v221, s44, v221\n" \
  "v_mul_f32 v222, s44, v222\n" \
  "ds_read_b128 v[164:167], v249 offset:16640\n" \
  "v_mfma_f32_16x16x32_bf16 v[236:239], v[200:203], v[124:127], v[236:239]\n" \
  "v_mul_f32 v223, s44, v223\n" \
  "v_cvt_pk_bf16_f32 v244, v216, v217\n" \
  "ds_read_b128 v[168:171], v250 offset:16640\n" \
  "s_waitcnt lgkmcnt(7)\n" \
  "v_mfma_f32_16x16x32_bf16 v[224:227], v[204:207], v[128:131], v[224:227]\n" \
  "v_cvt_pk_bf16_f32 v245, v220, v221\n" \
  "v_cvt_pk_bf16_f32 v246, v218, v219\n" \
  "ds_read_b128 v[172:175], v251 offset:16640\n" \
  "v_mfma_f32_16x16x32_bf16 v[228:231], v[204:207], v[132:135], v[228:231]\n" \
  "v_cvt_pk_bf16_f32 v247, v222, v223\n" \
  "buffer_store_dwordx4 v[244:247], %11, %0, s42 offen sc1\n" \
  "v_mfma_f32_16x16x32_bf16 v[232:235], v[204:207], v[136:139], v[232:235]\n" \
  "v_mfma_f32_16x16x32_bf16 v[236:239], v[204:207], v[140:143], v[236:239]\n" \
  "s_waitcnt lgkmcnt(7)\n" \
  "v_mfma_f32_16x16x32_bf16 v[208:211], v[144:147], v[16:19], 0\n" \
  "v_mfma_f32_16x16x32_bf16 v[212:215], v[144:147], v[20:23], 0\n" \
  "v_mfma_f32_16x16x32_bf16 v[216:219], v[144:147], v[24:27], 0\n" \
  "v_mfma_f32_16x16x32_bf16 v[220:223], v[144:147], v[28:31], 0\n" \
  "s_add_i32 s41, %13, s47\n" \
  "s_lshr_b32 s42, s41, 1\n" \
  "s_and_b32 s41, s41, 1\n" \
  "s_lshl_b32 s42, s42, 11\n" \
  "s_waitcnt lgkmcnt(6)\n" \
  "v_mfma_f32_16x16x32_bf16 v[208:211], v[148:151], v[32:35], v[208:211]\n" \
  "s_lshl_b32 s41, s41, 9\n" \
  "s_add_i32 s41, s41, s42\n" \
  "s_add_i32 s41, s41, %12\n" \
  "s_add_i32 s42, s41, 28672\n" \
  "v_mfma_f32_16x16x32_bf16 v[212:215], v[148:151], v[36:39], v[212:215]\n" \
  "v_mul_f32 v224, s44, v224\n" \
  "v_mfma_f32_16x16x32_bf16 v[216:219], v[148:151], v[40:43], v[216:219]\n" \
  "v_mul_f32 v225, s44, v225\n" \
  "v_mfma_f32_16x16x32_bf16 v[220:223], v[148:151], v[44:47], v[220:223]\n" \
  "v_mul_f32 v226, s44, v226\n" \
  "s_waitcnt lgkmcnt(5)\n" \
  "v_mfma_f32_16x16x32_bf16 v[208:211], v[152:155], v[48:51], v[208:211]\n" \
  "v_mul_f32 v227, s44, v227\n" \
  "v_mfma_f32_16x16x32_bf16 v[212:215], v[152:155], v[52:55], v[212:215]\n" \
  "v_mul_f32 v228, s44, v228\n" \
  "v_mfma_f32_16x16x32_bf16 v[216:219], v[152:155], v[56:59], v[216:219]\n" \
  "v_mul_f32 v229, s44, v229\n" \
  "v_mfma_f32_16x16x32_bf16 v[220:223], v[152:155], v[60:63], v[220:223]\n" \
  "v_mul_f32 v230, s44, v230\n" \
  "s_waitcnt lgkmcnt(4)\n" \
  "v_mfma_f32_16x16x32_bf16 v[208:211], v[156:159], v[64:67], v[208:211]\n" \
  "v_mul_f32 v231, s44, v231\n" \
  "v_mfma_f32_16x16x32_bf16 v[212:215], v[156:159], v[68:71], v[212:215]\n" \
  "v_cvt_pk_bf16_f32 v240, v224, v225\n" \
  "v_mfma_f32_16x16x32_bf16 v[216:219], v[156:159], v[72:75], v[216:219]\n" \
  "v_cvt_pk_bf16_f32 v241, v228, v229\n" \
  "v_mfma_f32_16x16x32_bf16 v[220:223], v[156:159], v[76:79], v[220:223]\n" \
  "v_cvt_pk_bf16_f32 v242, v226, v227\n" \
  "s_waitcnt lgkmcnt(3)\n" \
  "v_mfma_f32_16x16x32_bf16 v[208:211], v[160:163], v[80:83], v[208:211]\n" \
  "v_cvt_pk_bf16_f32 v243, v230, v231\n" \
  "v_mfma_f32_16x16x32_bf16 v[212:215], v[160:163], v[84:87], v[212:215]\n" \
  "buffer_store_dwordx4 v[240:243], %11, %0, s41 offen sc1\n" \
  "v_mfma_f32_16x16x32_bf16 v[216:219], v[160:163], v[88:91], v[216:219]\n" \
  "v_mul_f32 v232, s44, v232\n" \
  "v_mfma_f32_16x16x32_bf16 v[220:223], v[160:163], v[92:95], v[220:223]\n" \
  "v_mul_f32 v233, s44, v233\n" \
  "s_waitcnt lgkmcnt(2)\n" \
  "v_mfma_f32_16x16x32_bf16 v[208:211], v[164:167], v[96:99], v[208:211]\n" \
  "v_mul_f32 v234, s44, v234\n" \
  "s_waitcnt vmcnt(27)\n" \
  "v_mfma_f32_16x16x32_bf16 v[212:215], v[164:167], v[100:103], v[212:215]\n" \
  "v_mul_f32 v235, s44, v235\n" \
  "ds_read_b128 v[176:179], v248 offset:24576\n" \
  "v_mfma_f32_16x16x32_bf16 v[216:219], v[164:167], v[104:107], v[216:219]\n" \
  "v_mul_f32 v236, s44, v236\n" \
  "ds_read_b128 v[180:183], v249 offset:24576\n" \
  "v_mfma_f32_16x16x32_bf16 v[220:223], v[164:167], v[108:111], v[220:223]\n" \
  "v_mul_f32 v237, s44, v237\n" \
  "ds_read_b128 v[184:187], v250 offset:24576\n" \
  "s_waitcnt lgkmcnt(4)\n" \
  "v_mfma_f32_16x16x32_bf16 v[208:211], v[168:171], v[112:115], v[208:211]\n" \
  "v_mul_f32 v238, s44, v238\n" \
  "ds_read_b128 v[188:191], v251 offset:24576\n" \
  "v_mfma_f32_16x16x32_bf16 v[212:215], v[168:171], v[116:119], v[212:215]\n" \
  "v_mul_f32 v239, s44, v239\n" \
  "ds_read_b128 v[192:195], v248 offset:24832\n" \
  "v_mfma_f32_16x16x32_bf16 v[216:219], v[168:171], v[120:123], v[216:219]\n" \
  "v_cvt_pk_bf16_f32 v244, v232, v233\n" \
  "ds_read_b128 v[196:199], v249 offset:24832\n" \
  "v_mfma_f32_16x16x32_bf16 v[220:223], v[168:171], v[124:127], v[220:223]\n" \
  "v_cvt_pk_bf16_f32 v245, v236, v237\n" \
  "ds_read_b128 v[200:203], v250 offset:24832\n" \
  "s_waitcnt lgkmcnt(7)\n" \
  "v_mfma_f32_16x16x32_bf16 v[208:211], v[172:175], v[128:131], v[208:211]\n" \
  "v_cvt_pk_bf16_f32 v246, v234, v235\n" \
  "ds_read_b128 v[204:207], v251 offset:24832\n" \
  "v_mfma_f32_16x16x32_bf16 v[212:215], v[172:175], v[132:135], v[212:215]\n" \
  "v_cvt_pk_bf16_f32 v247, v238, v239\n" \
  "v_mfma_f32_16x16x32_bf16 v[216:219], v[172:175], v[136:139], v[216:219]\n" \
  "buffer_store_dwordx4 v[244:247], %11, %0, s42 offen sc1\n" \
  "v_mfma_f32_16x16x32_bf16 v[220:223], v[172:175], v[140:143], v[220:223]\n" \
  "s_waitcnt lgkmcnt(7)\n" \
  "v_mfma_f32_16x16x32_bf16 v[224:227], v[176:179], v[16:19], 0\n" \
  "v_mfma_f32_16x16x32_bf16 v[228:231], v[176:179], v[20:23], 0\n" \
  "v_mfma_f32_16x16x32_bf16 v[232:235], v[176:179], v[24:27], 0\n" \
  "v_mfma_f32_16x16x32_bf16 v[236:239], v[176:179], v[28:31], 0\n" \
  "s_add_i32 s41, %13, s48\n" \
  "s_lshr_b32 s42, s41, 1\n" \
  "s_and_b32 s41, s41, 1\n" \
  "s_lshl_b32 s42, s42, 11\n" \
  "s_waitcnt lgkmcnt(6)\n" \
  "v_mfma_f32_16x16x32_bf16 v[224:227], v[180:183], v[32:35], v[224:227]\n" \
  "s_lshl_b32 s41, s41, 9\n" \
  "s_add_i32 s41, s41, s42\n" \
  "s_add_i32 s41, s41, %12\n" \
  "s_add_i32 s42, s41, 28672\n" \
  "v_mfma_f32_16x16x32_bf16 v[228:231], v[180:183], v[36:39], v[228:231]\n" \
  "v_mul_f32 v208, s44, v208\n" \
  "v_mfma_f32_16x16x32_bf16 v[232:235], v[180:183], v[40:43], v[232:235]\n" \
  "v_mul_f32 v209, s44, v209\n" \
  "v_mfma_f32_16x16x32_bf16 v[236:239], v[180:183], v[44:47], v[236:239]\n" \
  "v_mul_f32 v210, s44, v210\n" \
  "s_waitcnt lgkmcnt(5)\n" \
  "v_mfma_f32_16x16x32_bf16 v[224:227], v[184:187], v[48:51], v[224:227]\n" \
  "v_mul_f32 v211, s44, v211\n" \
  "v_mfma_f32_16x16x32_bf16 v[228:231], v[184:187], v[52:55], v[228:231]\n" \
  "v_mul_f32 v212, s44, v212\n" \
  "v_mfma_f32_16x16x32_bf16 v[232:235], v[184:187], v[56:59], v[232:235]\n" \
  "v_mul_f32 v213, s44, v213\n" \
  "v_mfma_f32_16x16x32_bf16 v[236:239], v[184:187], v[60:63], v[236:239]\n" \
  "v_mul_f32 v214, s44, v214\n" \
  "s_waitcnt lgkmcnt(4)\n" \
  "v_mfma_f32_16x16x32_bf16 v[224:227], v[188:191], v[64:67], v[224:227]\n" \
  "v_mul_f32 v215, s44, v215\n" \
  "v_mfma_f32_16x16x32_bf16 v[228:231], v[188:191], v[68:71], v[228:231]\n" \
  "v_cvt_pk_bf16_f32 v240, v208, v209\n" \
  "v_mfma_f32_16x16x32_bf16 v[232:235], v[188:191], v[72:75], v[232:235]\n" \
  "v_cvt_pk_bf16_f32 v241, v212, v213\n" \
  "v_mfma_f32_16x16x32_bf16 v[236:239], v[188:191], v[76:79], v[236:239]\n" \
  "v_cvt_pk_bf16_f32 v242, v210, v211\n" \
  "s_waitcnt lgkmcnt(3)\n" \
  "v_mfma_f32_16x16x32_bf16 v[224:227], v[192:195], v[80:83], v[224:227]\n" \
  "v_cvt_pk_bf16_f32 v243, v214, v215\n" \
  "v_mfma_f32_16x16x32_bf16 v[228:231], v[192:195], v[84:87], v[228:231]\n" \
  "buffer_store_dwordx4 v[240:243], %11, %0, s41 offen sc1\n" \
  "v_mfma_f32_16x16x32_bf16 v[232:235], v[192:195], v[88:91], v[232:235]\n" \
  "v_mul_f32 v216, s44, v216\n" \
  "v_mfma_f32_16x16x32_bf16 v[236:239], v[192:195], v[92:95], v[236:239]\n" \
  "v_mul_f32 v217, s44, v217\n" \
  "s_waitcnt lgkmcnt(2)\n" \
  "v_mfma_f32_16x16x32_bf16 v[224:227], v[196:199], v[96:99], v[224:227]\n" \
  "v_mul_f32 v218, s44, v218\n" \
  "s_waitcnt vmcnt(13)\n" \
  "v_mfma_f32_16x16x32_bf16 v[228:231], v[196:199], v[100:103], v[228:231]\n" \
  "v_mul_f32 v219, s44, v219\n" \
  "ds_read_b128 v[144:147], v12 offset:0\n" \
  "v_mfma_f32_16x16x32_bf16 v[232:235], v[196:199], v[104:107], v[232:235]\n" \
  "v_mul_f32 v220, s44, v220\n" \
  "ds_read_b128 v[148:151], v13 offset:0\n" \
  "v_mfma_f32_16x16x32_bf16 v[236:239], v[196:199], v[108:111], v[236:239]\n" \
  "v_mul_f32 v221, s44, v221\n" \
  "ds_read_b128 v[152:155], v14 offset:0\n" \
  "s_waitcnt lgkmcnt(4)\n" \
  "v_mfma_f32_16x16x32_bf16 v[224:227], v[200:203], v[112:115], v[224:227]\n" \
  "v_mul_f32 v222, s44, v222\n" \
  "ds_read_b128 v[156:159], v15 offset:0\n" \
  "v_mfma_f32_16x16x32_bf16 v[228:231], v[200:203], v[116:119], v[228:231]\n" \
  "v_mul_f32 v223, s44, v223\n" \
  "ds_read_b128 v[160:163], v12 offset:256\n" \
  "v_mfma_f32_16x16x32_bf16 v[232:235], v[200:203], v[120:123], v[232:235]\n" \
  "v_cvt_pk_bf16_f32 v244, v216, v217\n" \
  "ds_read_b128 v[164:167], v13 offset:256\n" \
  "v_mfma_f32_16x16x32_bf16 v[236:239], v[200:203], v[124:127], v[236:239]\n" \
  "v_cvt_pk_bf16_f32 v245, v220, v221\n" \
  "ds_read_b128 v[168:171], v14 offset:256\n" \
  "s_waitcnt lgkmcnt(7)\n" \
  "v_mfma_f32_16x16x32_bf16 v[224:227], v[204:207], v[128:131], v[224:227]\n" \
  "v_cvt_pk_bf16_f32 v246, v218, v219\n" \
  "ds_read_b128 v[172:175], v15 offset:256\n" \
  "v_mfma_f32_16x16x32_bf16 v[228:231], v[204:207], v[132:135], v[228:231]\n" \
  "v_cvt_pk_bf16_f32 v247, v222, v223\n" \
  "v_mfma_f32_16x16x32_bf16 v[232:235], v[204:207], v[136:139], v[232:235]\n" \
  "buffer_store_dwordx4 v[244:247], %11, %0, s42 offen sc1\n" \
  "v_mfma_f32_16x16x32_bf16 v[236:239], v[204:207], v[140:143], v[236:239]\n" \
  "s_waitcnt lgkmcnt(7)\n" \
  "v_mfma_f32_16x16x32_bf16 v[208:211], v[144:147], v[16:19], 0\n" \
  "v_mfma_f32_16x16x32_bf16 v[212:215], v[144:147], v[20:23], 0\n" \
  "v_mfma_f32_16x16x32_bf16 v[216:219], v[144:147], v[24:27], 0\n" \
  "v_mfma_f32_16x16x32_bf16 v[220:223], v[144:147], v[28:31], 0\n" \
  "s_add_i32 s41, %13, s49\n" \
  "s_lshr_b32 s42, s41, 1\n" \
  "s_and_b32 s41, s41, 1\n" \
  "s_lshl_b32 s42, s42, 11\n" \
  "s_waitcnt lgkmcnt(6)\n" \
  "v_mfma_f32_16x16x32_bf16 v[208:211], v[148:151], v[32:35], v[208:211]\n" \
  "s_lshl_b32 s41, s41, 9\n" \
  "s_add_i32 s41, s41, s42\n" \
  "s_add_i32 s41, s41, %12\n" \
  "s_add_i32 s42, s41, 28672\n" \
  "v_mfma_f32_16x16x32_bf16 v[212:215], v[148:151], v[36:39], v[212:215]\n" \
  "v_mul_f32 v224, s44, v224\n" \
  "v_mfma_f32_16x16x32_bf16 v[216:219], v[148:151], v[40:43], v[216:219]\n" \
  "v_mul_f32 v225, s44, v225\n" \
  "v_mfma_f32_16x16x32_bf16 v[220:223], v[148:151], v[44:47], v[220:223]\n" \
  "v_mul_f32 v226, s44, v226\n" \
  "s_waitcnt lgkmcnt(5)\n" \
  "v_mfma_f32_16x16x32_bf16 v[208:211], v[152:155], v[48:51], v[208:211]\n" \
  "v_mul_f32 v227, s44, v227\n" \
  "v_mfma_f32_16x16x32_bf16 v[212:215], v[152:155], v[52:55], v[212:215]\n" \
  "v_mul_f32 v228, s44, v228\n" \
  "v_mfma_f32_16x16x32_bf16 v[216:219], v[152:155], v[56:59], v[216:219]\n" \
  "v_mul_f32 v229, s44, v229\n" \
  "v_mfma_f32_16x16x32_bf16 v[220:223], v[152:155], v[60:63], v[220:223]\n" \
  "v_mul_f32 v230, s44, v230\n" \
  "s_waitcnt lgkmcnt(4)\n" \
  "v_mfma_f32_16x16x32_bf16 v[208:211], v[156:159], v[64:67], v[208:211]\n" \
  "v_mul_f32 v231, s44, v231\n" \
  "v_mfma_f32_16x16x32_bf16 v[212:215], v[156:159], v[68:71], v[212:215]\n" \
  "v_cvt_pk_bf16_f32 v240, v224, v225\n" \
  "v_mfma_f32_16x16x32_bf16 v[216:219], v[156:159], v[72:75], v[216:219]\n" \
  "v_cvt_pk_bf16_f32 v241, v228, v229\n" \
  "v_mfma_f32_16x16x32_bf16 v[220:223], v[156:159], v[76:79], v[220:223]\n" \
  "v_cvt_pk_bf16_f32 v242, v226, v227\n" \
  "s_waitcnt lgkmcnt(3)\n" \
  "v_mfma_f32_16x16x32_bf16 v[208:211], v[160:163], v[80:83], v[208:211]\n" \
  "v_cvt_pk_bf16_f32 v243, v230, v231\n" \
  "v_mfma_f32_16x16x32_bf16 v[212:215], v[160:163], v[84:87], v[212:215]\n" \
  "buffer_store_dwordx4 v[240:243], %11, %0, s41 offen sc1\n" \
  "v_mfma_f32_16x16x32_bf16 v[216:219], v[160:163], v[88:91], v[216:219]\n" \
  "v_mul_f32 v232, s44, v232\n" \
  "v_mfma_f32_16x16x32_bf16 v[220:223], v[160:163], v[92:95], v[220:223]\n" \
  "v_mul_f32 v233, s44, v233\n" \
  "s_waitcnt lgkmcnt(2)\n" \
  "v_mfma_f32_16x16x32_bf16 v[208:211], v[164:167], v[96:99], v[208:211]\n" \
  "v_mul_f32 v234, s44, v234\n" \
  "s_waitcnt vmcnt(23)\n" \
  "v_mfma_f32_16x16x32_bf16 v[212:215], v[164:167], v[100:103], v[212:215]\n" \
  "v_mul_f32 v235, s44, v235\n" \
  "ds_read_b128 v[176:179], v248 offset:0\n" \
  "v_mfma_f32_16x16x32_bf16 v[216:219], v[164:167], v[104:107], v[216:219]\n" \
  "v_mul_f32 v236, s44, v236\n" \
  "ds_read_b128 v[180:183], v249 offset:0\n" \
  "v_mfma_f32_16x16x32_bf16 v[220:223], v[164:167], v[108:111], v[220:223]\n" \
  "v_mul_f32 v237, s44, v237\n" \
  "ds_read_b128 v[184:187], v250 offset:0\n" \
  "s_waitcnt lgkmcnt(4)\n" \
  "v_mfma_f32_16x16x32_bf16 v[208:211], v[168:171], v[112:115], v[208:211]\n" \
  "v_mul_f32 v238, s44, v238\n" \
  "ds_read_b128 v[188:191], v251 offset:0\n" \
  "v_mfma_f32_16x16x32_bf16 v[212:215], v[168:171], v[116:119], v[212:215]\n" \
  "v_mul_f32 v239, s44, v239\n" \
  "ds_read_b128 v[192:195], v248 offset:256\n" \
  "v_mfma_f32_16x16x32_bf16 v[216:219], v[168:171], v[120:123], v[216:219]\n" \
  "v_cvt_pk_bf16_f32 v244, v232, v233\n" \
  "ds_read_b128 v[196:199], v249 offset:256\n" \
  "v_mfma_f32_16x16x32_bf16 v[220:223], v[168:171], v[124:127], v[220:223]\n" \
  "v_cvt_pk_bf16_f32 v245, v236, v237\n" \
  "ds_read_b128 v[200:203], v250 offset:256\n" \
  "s_waitcnt lgkmcnt(7)\n" \
  "v_mfma_f32_16x16x32_bf16 v[208:211], v[172:175], v[128:131], v[208:211]\n" \
  "v_cvt_pk_bf16_f32 v246, v234, v235\n" \
  "ds_read_b128 v[204:207], v251 offset:256\n" \
  "v_mfma_f32_16x16x32_bf16 v[212:215], v[172:175], v[132:135], v[212:215]\n" \
  "v_cvt_pk_bf16_f32 v247, v238, v239\n" \
  "v_mfma_f32_16x16x32_bf16 v[216:219], v[172:175], v[136:139], v[216:219]\n" \
  "buffer_store_dwordx4 v[244:247], %11, %0, s42 offen sc1\n" \
  "v_mfma_f32_16x16x32_bf16 v[220:223], v[172:175], v[140:143], v[220:223]\n" \
  "s_waitcnt lgkmcnt(7)\n" \
  "v_mfma_f32_16x16x32_bf16 v[224:227], v[176:179], v[16:19], 0\n" \
  "v_mfma_f32_16x16x32_bf16 v[228:231], v[176:179], v[20:23], 0\n" \
  "v_mfma_f32_16x16x32_bf16 v[232:235], v[176:179], v[24:27], 0\n" \
  "v_mfma_f32_16x16x32_bf16 v[236:239], v[176:179], v[28:31], 0\n" \
  "s_add_i32 s41, %13, s50\n" \
  "s_lshr_b32 s42, s41, 1\n" \
  "s_and_b32 s41, s41, 1\n" \
  "s_lshl_b32 s42, s42, 11\n" \
  "s_waitcnt lgkmcnt(6)\n" \
  "v_mfma_f32_16x16x32_bf16 v[224:227], v[180:183], v[32:35], v[224:227]\n" \
  "s_lshl_b32 s41, s41, 9\n" \
  "s_add_i32 s41, s41, s42\n" \
  "s_add_i32 s41, s41, %12\n" \
  "s_add_i32 s42, s41, 28672\n" \
  "v_mfma_f32_16x16x32_bf16 v[228:231], v[180:183], v[36:39], v[228:231]\n" \
  "v_mul_f32 v208, s44, v208\n" \
  "v_mfma_f32_16x16x32_bf16 v[232:235], v[180:183], v[40:43], v[232:235]\n" \
  "v_mul_f32 v209, s44, v209\n" \
  "v_mfma_f32_16x16x32_bf16 v[236:239], v[180:183], v[44:47], v[236:239]\n" \
  "v_mul_f32 v210, s44, v210\n" \
  "s_waitcnt lgkmcnt(5)\n" \
  "v_mfma_f32_16x16x32_bf16 v[224:227], v[184:187], v[48:51], v[224:227]\n" \
  "v_mul_f32 v211, s44, v211\n" \
  "v_mfma_f32_16x16x32_bf16 v[228:231], v[184:187], v[52:55], v[228:231]\n" \
  "v_mul_f32 v212, s44, v212\n" \
  "v_mfma_f32_16x16x32_bf16 v[232:235], v[184:187], v[56:59], v[232:235]\n" \
  "v_mul_f32 v213, s44, v213\n" \
  "v_mfma_f32_16x16x32_bf16 v[236:239], v[184:187], v[60:63], v[236:239]\n" \
  "v_mul_f32 v214, s44, v214\n" \
  "s_waitcnt lgkmcnt(4)\n" \
  "v_mfma_f32_16x16x32_bf16 v[224:227], v[188:191], v[64:67], v[224:227]\n" \
  "v_mul_f32 v215, s44, v215\n" \
  "v_mfma_f32_16x16x32_bf16 v[228:231], v[188:191], v[68:71], v[228:231]\n" \
  "v_cvt_pk_bf16_f32 v240, v208, v209\n" \
  "v_mfma_f32_16x16x32_bf16 v[232:235], v[188:191], v[72:75], v[232:235]\n" \
  "v_cvt_pk_bf16_f32 v241, v212, v213\n" \
  "v_mfma_f32_16x16x32_bf16 v[236:239], v[188:191], v[76:79], v[236:239]\n" \
  "v_cvt_pk_bf16_f32 v242, v210, v211\n" \
  "s_waitcnt lgkmcnt(3)\n" \
  "v_mfma_f32_16x16x32_bf16 v[224:227], v[192:195], v[80:83], v[224:227]\n" \
  "v_cvt_pk_bf16_f32 v243, v214, v215\n" \
  "v_mfma_f32_16x16x32_bf16 v[228:231], v[192:195], v[84:87], v[228:231]\n" \
  "buffer_store_dwordx4 v[240:243], %11, %0, s41 offen sc1\n" \
  "v_mfma_f32_16x16x32_bf16 v[232:235], v[192:195], v[88:91], v[232:235]\n" \
  "v_mul_f32 v216, s44, v216\n" \
  "v_mfma_f32_16x16x32_bf16 v[236:239], v[192:195], v[92:95], v[236:239]\n" \
  "v_mul_f32 v217, s44, v217\n" \
  "s_waitcnt lgkmcnt(2)\n" \
  "v_mfma_f32_16x16x32_bf16 v[224:227], v[196:199], v[96:99], v[224:227]\n" \
  "v_mul_f32 v218, s44, v218\n" \
  "s_waitcnt vmcnt(9)\n" \
  "v_mfma_f32_16x16x32_bf16 v[228:231], v[196:199], v[100:103], v[228:231]\n" \
  "v_mul_f32 v219, s44, v219\n" \
  "ds_read_b128 v[144:147], v248 offset:8192\n" \
  "v_mfma_f32_16x16x32_bf16 v[232:235], v[196:199], v[104:107], v[232:235]\n" \
  "v_mul_f32 v220, s44, v220\n" \
  "ds_read_b128 v[148:151], v249 offset:8192\n" \
  "v_mfma_f32_16x16x32_bf16 v[236:239], v[196:199], v[108:111], v[236:239]\n" \
  "v_mul_f32 v221, s44, v221\n" \
  "ds_read_b128 v[152:155], v250 offset:8192\n" \
  "s_waitcnt lgkmcnt(4)\n" \
  "v_mfma_f32_16x16x32_bf16 v[224:227], v[200:203], v[112:115], v[224:227]\n" \
  "v_mul_f32 v222, s44, v222\n" \
  "ds_read_b128 v[156:159], v251 offset:8192\n" \
  "v_mfma_f32_16x16x32_bf16 v[228:231], v[200:203], v[116:119], v[228:231]\n" \
  "v_mul_f32 v223, s44, v223\n" \
  "ds_read_b128 v[160:163], v248 offset:8448\n" \
  "v_mfma_f32_16x16x32_bf16 v[232:235], v[200:203], v[120:123], v[232:235]\n" \
  "v_cvt_pk_bf16_f32 v244, v216, v217\n" \
  "ds_read_b128 v[164:167], v249 offset:8448\n" \
  "v_mfma_f32_16x16x32_bf16 v[236:239], v[200:203], v[124:127], v[236:239]\n" \
  "v_cvt_pk_bf16_f32 v245, v220, v221\n" \
  "ds_read_b128 v[168:171], v250 offset:8448\n" \
  "s_waitcnt lgkmcnt(7)\n" \
  "v_mfma_f32_16x16x32_bf16 v[224:227], v[204:207], v[128:131], v[224:227]\n" \
  "v_cvt_pk_bf16_f32 v246, v218, v219\n" \
  "ds_read_b128 v[172:175], v251 offset:8448\n" \
  "v_mfma_f32_16x16x32_bf16 v[228:231], v[204:207], v[132:135], v[228:231]\n" \
  "v_cvt_pk_bf16_f32 v247, v222, v223\n" \
  "v_mfma_f32_16x16x32_bf16 v[232:235], v[204:207], v[136:139], v[232:235]\n" \
  "buffer_store_dwordx4 v[244:247], %11, %0, s42 offen sc1\n" \
  "v_mfma_f32_16x16x32_bf16 v[236:239], v[204:207], v[140:143], v[236:239]\n" \
  "s_waitcnt lgkmcnt(7)\n" \
  "v_mfma_f32_16x16x32_bf16 v[208:211], v[144:147], v[16:19], 0\n" \
  "v_mfma_f32_16x16x32_bf16 v[212:215], v[144:147], v[20:23], 0\n" \
  "v_mfma_f32_16x16x32_bf16 v[216:219], v[144:147], v[24:27], 0\n" \
  "v_mfma_f32_16x16x32_bf16 v[220:223], v[144:147], v[28:31], 0\n" \
  "s_add_i32 s41, %13, s51\n" \
  "s_lshr_b32 s42, s41, 1\n" \
  "s_and_b32 s41, s41, 1\n" \
  "s_lshl_b32 s42, s42, 11\n" \
  "s_waitcnt lgkmcnt(6)\n" \
  "v_mfma_f32_16x16x32_bf16 v[208:211], v[148:151], v[32:35], v[208:211]\n" \
  "s_lshl_b32 s41, s41, 9\n" \
  "s_add_i32 s41, s41, s42\n" \
  "s_add_i32 s41, s41, %12\n" \
  "s_add_i32 s42, s41, 28672\n" \
  "v_mfma_f32_16x16x32_bf16 v[212:215], v[148:151], v[36:39], v[212:215]\n" \
  "v_mul_f32 v224, s44, v224\n" \
  "v_mfma_f32_16x16x32_bf16 v[216:219], v[148:151], v[40:43], v[216:219]\n" \
  "v_mul_f32 v225, s44, v225\n" \
  "v_mfma_f32_16x16x32_bf16 v[220:223], v[148:151], v[44:47], v[220:223]\n" \
  "v_mul_f32 v226, s44, v226\n" \
  "s_waitcnt lgkmcnt(5)\n" \
  "v_mfma_f32_16x16x32_bf16 v[208:211], v[152:155], v[48:51], v[208:211]\n" \
  "v_mul_f32 v227, s44, v227\n" \
  "v_mfma_f32_16x16x32_bf16 v[212:215], v[152:155], v[52:55], v[212:215]\n" \
  "v_mul_f32 v228, s44, v228\n" \
  "v_mfma_f32_16x16x32_bf16 v[216:219], v[152:155], v[56:59], v[216:219]\n" \
  "v_mul_f32 v229, s44, v229\n" \
  "v_mfma_f32_16x16x32_bf16 v[220:223], v[152:155], v[60:63], v[220:223]\n" \
  "v_mul_f32 v230, s44, v230\n" \
  "s_waitcnt lgkmcnt(4)\n" \
  "v_mfma_f32_16x16x32_bf16 v[208:211], v[156:159], v[64:67], v[208:211]\n" \
  "v_mul_f32 v231, s44, v231\n" \
  "v_mfma_f32_16x16x32_bf16 v[212:215], v[156:159], v[68:71], v[212:215]\n" \
  "v_cvt_pk_bf16_f32 v240, v224, v225\n" \
  "v_mfma_f32_16x16x32_bf16 v[216:219], v[156:159], v[72:75], v[216:219]\n" \
  "v_cvt_pk_bf16_f32 v241, v228, v229\n" \
  "v_mfma_f32_16x16x32_bf16 v[220:223], v[156:159], v[76:79], v[220:223]\n" \
  "v_cvt_pk_bf16_f32 v242, v226, v227\n" \
  "s_waitcnt lgkmcnt(3)\n" \
  "v_mfma_f32_16x16x32_bf16 v[208:211], v[160:163], v[80:83], v[208:211]\n" \
  "v_cvt_pk_bf16_f32 v243, v230, v231\n" \
  "v_mfma_f32_16x16x32_bf16 v[212:215], v[160:163], v[84:87], v[212:215]\n" \
  "buffer_store_dwordx4 v[240:243], %11, %0, s41 offen sc1\n" \
  "v_mfma_f32_16x16x32_bf16 v[216:219], v[160:163], v[88:91], v[216:219]\n" \
  "v_mul_f32 v232, s44, v232\n" \
  "v_mfma_f32_16x16x32_bf16 v[220:223], v[160:163], v[92:95], v[220:223]\n" \
  "v_mul_f32 v233, s44, v233\n" \
  "s_waitcnt lgkmcnt(2)\n" \
  "v_mfma_f32_16x16x32_bf16 v[208:211], v[164:167], v[96:99], v[208:211]\n" \
  "v_mul_f32 v234, s44, v234\n" \
  "v_mfma_f32_16x16x32_bf16 v[212:215], v[164:167], v[100:103], v[212:215]\n" \
  "v_mul_f32 v235, s44, v235\n" \
  "v_mfma_f32_16x16x32_bf16 v[216:219], v[164:167], v[104:107], v[216:219]\n" \
  "v_mul_f32 v236, s44, v236\n" \
  "v_mfma_f32_16x16x32_bf16 v[220:223], v[164:167], v[108:111], v[220:223]\n" \
  "v_mul_f32 v237, s44, v237\n" \
  "s_waitcnt lgkmcnt(1)\n" \
  "v_mfma_f32_16x16x32_bf16 v[208:211], v[168:171], v[112:115], v[208:211]\n" \
  "v_mul_f32 v238, s44, v238\n" \
  "v_mfma_f32_16x16x32_bf16 v[212:215], v[168:171], v[116:119], v[212:215]\n" \
  "v_mul_f32 v239, s44, v239\n" \
  "v_mfma_f32_16x16x32_bf16 v[216:219], v[168:171], v[120:123], v[216:219]\n" \
  "v_cvt_pk_bf16_f32 v244, v232, v233\n" \
  "v_mfma_f32_16x16x32_bf16 v[220:223], v[168:171], v[124:127], v[220:223]\n" \
  "v_cvt_pk_bf16_f32 v245, v236, v237\n" \
  "s_waitcnt lgkmcnt(0)\n" \
  "v_mfma_f32_16x16x32_bf16 v[208:211], v[172:175], v[128:131], v[208:211]\n" \
  "v_cvt_pk_bf16_f32 v246, v234, v235\n" \
  "v_mfma_f32_16x16x32_bf16 v[212:215], v[172:175], v[132:135], v[212:215]\n" \
  "v_cvt_pk_bf16_f32 v247, v238, v239\n" \
  "v_mfma_f32_16x16x32_bf16 v[216:219], v[172:175], v[136:139], v[216:219]\n" \
  "buffer_store_dwordx4 v[244:247], %11, %0, s42 offen sc1\n" \
  "v_mfma_f32_16x16x32_bf16 v[220:223], v[172:175], v[140:143], v[220:223]\n" \
  "s_nop 7\n" \
  "s_add_i32 s41, %13, s52\n" \
  "s_lshr_b32 s42, s41, 1\n" \
  "s_and_b32 s41, s41, 1\n" \
  "s_lshl_b32 s42, s42, 11\n" \
  "s_lshl_b32 s41, s41, 9\n" \
  "s_add_i32 s41, s41, s42\n" \
  "s_add_i32 s41, s41, %12\n" \
  "s_add_i32 s42, s41, 28672\n" \
  "v_mul_f32 v208, s44, v208\n" \
  "v_mul_f32 v209, s44, v209\n" \
  "v_mul_f32 v210, s44, v210\n" \
  "v_mul_f32 v211, s44, v211\n" \
  "v_mul_f32 v212, s44, v212\n" \
  "v_mul_f32 v213, s44, v213\n" \
  "v_mul_f32 v214, s44, v214\n" \
  "v_mul_f32 v215, s44, v215\n" \
  "v_cvt_pk_bf16_f32 v240, v208, v209\n" \
  "v_cvt_pk_bf16_f32 v241, v212, v213\n" \
  "v_cvt_pk_bf16_f32 v242, v210, v211\n" \
  "v_cvt_pk_bf16_f32 v243, v214, v215\n" \
  "buffer_store_dwordx4 v[240:243], %11, %0, s41 offen sc1\n" \
  "v_mul_f32 v216, s44, v216\n" \
  "v_mul_f32 v217, s44, v217\n" \
  "v_mul_f32 v218, s44, v218\n" \
  "v_mul_f32 v219, s44, v219\n" \
  "v_mul_f32 v220, s44, v220\n" \
  "v_mul_f32 v221, s44, v221\n" \
  "v_mul_f32 v222, s44, v222\n" \
  "v_mul_f32 v223, s44, v223\n" \
  "v_cvt_pk_bf16_f32 v244, v216, v217\n" \
  "v_cvt_pk_bf16_f32 v245, v220, v221\n" \
  "v_cvt_pk_bf16_f32 v246, v218, v219\n" \
  "v_cvt_pk_bf16_f32 v247, v222, v223\n" \
  "buffer_store_dwordx4 v[244:247], %11, %0, s42 offen sc1\n"

#define ASM_FOLDU \
  "s_mov_b32 s44, 0x3db8aa3b\n" \
  "s_mov_b32 s45, 0x3db8aa3b\n" \
  "s_add_i32 s46, %5, 0\n" \
  "s_sub_i32 s40, s46, 7\n" \
  "s_cmp_lt_i32 s40, 0\n" \
  "s_cselect_b32 s46, s46, s40\n" \
  "s_add_i32 s47, %5, 1\n" \
  "s_sub_i32 s40, s47, 7\n" \
  "s_cmp_lt_i32 s40, 0\n" \
  "s_cselect_b32 s47, s47, s40\n" \
  "s_add_i32 s48, %5, 2\n" \
  "s_sub_i32 s40, s48, 7\n" \
  "s_cmp_lt_i32 s40, 0\n" \
  "s_cselect_b32 s48, s48, s40\n" \
  "s_add_i32 s49, %5, 3\n" \
  "s_sub_i32 s40, s49, 7\n" \
  "s_cmp_lt_i32 s40, 0\n" \
  "s_cselect_b32 s49, s49, s40\n" \
  "s_add_i32 s50, %5, 4\n" \
  "s_sub_i32 s40, s50, 7\n" \
  "s_cmp_lt_i32 s40, 0\n" \
  "s_cselect_b32 s50, s50, s40\n" \
  "s_add_i32 s51, %5, 5\n" \
  "s_sub_i32 s40, s51, 7\n" \
  "s_cmp_lt_i32 s40, 0\n" \
  "s_cselect_b32 s51, s51, s40\n" \
  "s_add_i32 s52, %5, 6\n" \
  "s_sub_i32 s40, s52, 7\n" \
  "s_cmp_lt_i32 s40, 0\n" \
  "s_cselect_b32 s52, s52, s40\n" \
  "v_xor_b32 v248, 0, %6\n" \
  "v_xor_b32 v249, 64, %6\n" \
  "v_xor_b32 v250, 128, %6\n" \
  "v_xor_b32 v251, 192, %6\n" \
  "s_sub_i32 s53, %14, %4\n" \
  "v_add_u32 v12, s53, v248\n" \
  "v_add_u32 v13, s53, v249\n" \
  "v_add_u32 v14, s53, v250\n" \
  "v_add_u32 v15, s53, v251\n" \
  "ds_read_b64_tr_b16 v[16:17], %7 offset:0\n" \
  "ds_read_b64_tr_b16 v[18:19], %7 offset:512\n" \
  "ds_read_b64_tr_b16 v[20:21], %8 offset:0\n" \
  "ds_read_b64_tr_b16 v[22:23], %8 offset:512\n" \
  "ds_read_b64_tr_b16 v[24:25], %9 offset:0\n" \
  "ds_read_b64_tr_b16 v[26:27], %9 offset:512\n" \
  "ds_read_b64_tr_b16 v[28:29], %10 offset:0\n" \
  "ds_read_b64_tr_b16 v[30:31], %10 offset:512\n" \
  "ds_read_b128 v[144:147], v248 offset:0\n" \
  "s_waitcnt lgkmcnt(0)\n" \
  "v_mfma_f32_16x16x32_bf16 v[208:211], v[144:147], v[16:19], 0\n" \
  "ds_read_b64_tr_b16 v[32:33], %7 offset:4096\n" \
  "ds_read_b64_tr_b16 v[34:35], %7 offset:4608\n" \
  "ds_read_b64_tr_b16 v[36:37], %8 offset:4096\n" \
  "v_mfma_f32_16x16x32_bf16 v[212:215], v[144:147], v[20:23], 0\n" \
  "ds_read_b64_tr_b16 v[38:39], %8 offset:4608\n" \
  "ds_read_b64_tr_b16 v[40:41], %9 offset:4096\n" \
  "ds_read_b64_tr_b16 v[42:43], %9 offset:4608\n" \
  "v_mfma_f32_16x16x32_bf16 v[216:219], v[144:147], v[24:27], 0\n" \
  "ds_read_b64_tr_b16 v[44:45], %10 offset:4096\n" \
  "ds_read_b64_tr_b16 v[46:47], %10 offset:4608\n" \
  "ds_read_b128 v[148:151], v249 offset:0\n" \
  "v_mfma_f32_16x16x32_bf16 v[220:223], v[144:147], v[28:31], 0\n" \
  "s_waitcnt lgkmcnt(0)\n" \
  "v_mfma_f32_16x16x32_bf16 v[208:211], v[148:151], v[32:35], v[208:211]\n" \
  "ds_read_b64_tr_b16 v[48:49], %7 offset:8192\n" \
  "ds_read_b64_tr_b16 v[50:51], %7 offset:8704\n" \
  "ds_read_b64_tr_b16 v[52:53], %8 offset:8192\n" \
  "v_mfma_f32_16x16x32_bf16 v[212:215], v[148:151], v[36:39], v[212:215]\n" \
  "ds_read_b64_tr_b16 v[54:55], %8 offset:8704\n" \
  "ds_read_b64_tr_b16 v[56:57], %9 offset:8192\n" \
  "ds_read_b64_tr_b16 v[58:59], %9 offset:8704\n" \
  "v_mfma_f32_16x16x32_bf16 v[216:219], v[148:151], v[40:43], v[216:219]\n" \
  "ds_read_b64_tr_b16 v[60:61], %10 offset:8192\n" \
  "ds_read_b64_tr_b16 v[62:63], %10 offset:8704\n" \
  "ds_read_b128 v[152:155], v250 offset:0\n" \
  "v_mfma_f32_16x16x32_bf16 v[220:223], v[148:151], v[44:47], v[220:223]\n" \
  "s_waitcnt lgkmcnt(0)\n" \
  "v_mfma_f32_16x16x32_bf16 v[208:211], v[152:155], v[48:51], v[208:211]\n" \
  "ds_read_b64_tr_b16 v[64:65], %7 offset:12288\n" \
  "ds_read_b64_tr_b16 v[66:67], %7 offset:12800\n" \
  "ds_read_b64_tr_b16 v[68:69], %8 offset:12288\n" \
  "v_mfma_f32_16x16x32_bf16 v[212:215], v[152:155], v[52:55], v[212:215]\n" \
  "ds_read_b64_tr_b16 v[70:71], %8 offset:12800\n" \
  "ds_read_b64_tr_b16 v[72:73], %9 offset:12288\n" \
  "ds_read_b64_tr_b16 v[74:75], %9 offset:12800\n" \
  "v_mfma_f32_16x16x32_bf16 v[216:219], v[152:155], v[56:59], v[216:219]\n" \
  "ds_read_b64_tr_b16 v[76:77], %10 offset:12288\n" \
  "ds_read_b64_tr_b16 v[78:79], %10 offset:12800\n" \
  "ds_read_b128 v[156:159], v251 offset:0\n" \
  "v_mfma_f32_16x16x32_bf16 v[220:223], v[152:155], v[60:63], v[220:223]\n" \
  "s_waitcnt lgkmcnt(0)\n" \
  "v_mfma_f32_16x16x32_bf16 v[208:211], v[156:159], v[64:67], v[208:211]\n" \
  "ds_read_b64_tr_b16 v[80:81], %7 offset:16384\n" \
  "ds_read_b64_tr_b16 v[82:83], %7 offset:16896\n" \
  "ds_read_b64_tr_b16 v[84:85], %8 offset:16384\n" \
  "v_mfma_f32_16x16x32_bf16 v[212:215], v[156:159], v[68:71], v[212:215]\n" \
  "ds_read_b64_tr_b16 v[86:87], %8 offset:16896\n" \
  "ds_read_b64_tr_b16 v[88:89], %9 offset:16384\n" \
  "ds_read_b64_tr_b16 v[90:91], %9 offset:16896\n" \
  "v_mfma_f32_16x16x32_bf16 v[216:219], v[156:159], v[72:75], v[216:219]\n" \
  "ds_read_b64_tr_b16 v[92:93], %10 offset:16384\n" \
  "ds_read_b64_tr_b16 v[94:95], %10 offset:16896\n" \
  "ds_read_b128 v[160:163], v248 offset:256\n" \
  "v_mfma_f32_16x16x32_bf16 v[220:223], v[156:159], v[76:79], v[220:223]\n" \
  "s_waitcnt lgkmcnt(0)\n" \
  "v_mfma_f32_16x16x32_bf16 v[208:211], v[160:163], v[80:83], v[208:211]\n" \
  "ds_read_b64_tr_b16 v[96:97], %7 offset:20480\n" \
  "ds_read_b64_tr_b16 v[98:99], %7 offset:20992\n" \
  "ds_read_b64_tr_b16 v[100:101], %8 offset:20480\n" \
  "v_mfma_f32_16x16x32_bf16 v[212:215], v[160:163], v[84:87], v[212:215]\n" \
  "ds_read_b64_tr_b16 v[102:103], %8 offset:20992\n" \
  "ds_read_b64_tr_b16 v[104:105], %9 offset:20480\n" \
  "ds_read_b64_tr_b16 v[106:107], %9 offset:20992\n" \
  "v_mfma_f32_16x16x32_bf16 v[216:219], v[160:163], v[88:91], v[216:219]\n" \
  "ds_read_b64_tr_b16 v[108:109], %10 offset:20480\n" \
  "ds_read_b64_tr_b16 v[110:111], %10 offset:20992\n" \
  "ds_read_b128 v[164:167], v249 offset:256\n" \
  "v_mfma_f32_16x16x32_bf16 v[220:223], v[160:163], v[92:95], v[220:223]\n" \
  "s_waitcnt lgkmcnt(0)\n" \
  "v_mfma_f32_16x16x32_bf16 v[208:211], v[164:167], v[96:99], v[208:211]\n" \
  "ds_read_b64_tr_b16 v[112:113], %7 offset:24576\n" \
  "ds_read_b64_tr_b16 v[114:115], %7 offset:25088\n" \
  "ds_read_b64_tr_b16 v[116:117], %8 offset:24576\n" \
  "v_mfma_f32_16x16x32_bf16 v[212:215], v[164:167], v[100:103], v[212:215]\n" \
  "ds_read_b64_tr_b16 v[118:119], %8 offset:25088\n" \
  "ds_read_b64_tr_b16 v[120:121], %9 offset:24576\n" \
  "ds_read_b64_tr_b16 v[122:123], %9 offset:25088\n" \
  "v_mfma_f32_16x16x32_bf16 v[216:219], v[164:167], v[104:107], v[216:219]\n" \
  "ds_read_b64_tr_b16 v[124:125], %10 offset:24576\n" \
  "ds_read_b64_tr_b16 v[126:127], %10 offset:25088\n" \
  "ds_read_b128 v[168:171], v250 offset:256\n" \
  "v_mfma_f32_16x16x32_bf16 v[220:223], v[164:167], v[108:111], v[220:223]\n" \
  "s_waitcnt lgkmcnt(0)\n" \
  "v_mfma_f32_16x16x32_bf16 v[208:211], v[168:171], v[112:115], v[208:211]\n" \
  "ds_read_b64_tr_b16 v[128:129], %7 offset:28672\n" \
  "ds_read_b64_tr_b16 v[130:131], %7 offset:29184\n" \
  "ds_read_b64_tr_b16 v[132:133], %8 offset:28672\n" \
  "v_mfma_f32_16x16x32_bf16 v[212:215], v[168:171], v[116:119], v[212:215]\n" \
  "ds_read_b64_tr_b16 v[134:135], %8 offset:29184\n" \
  "ds_read_b64_tr_b16 v[136:137], %9 offset:28672\n" \
  "ds_read_b64_tr_b16 v[138:139], %9 offset:29184\n" \
  "v_mfma_f32_16x16x32_bf16 v[216:219], v[168:171], v[120:123], v[216:219]\n" \
  "ds_read_b64_tr_b16 v[140:141], %10 offset:28672\n" \
  "ds_read_b64_tr_b16 v[142:143], %10 offset:29184\n" \
  "ds_read_b128 v[172:175], v251 offset:256\n" \
  "v_mfma_f32_16x16x32_bf16 v[220:223], v[168:171], v[124:127], v[220:223]\n" \
  "s_waitcnt lgkmcnt(0)\n" \
  "v_mfma_f32_16x16x32_bf16 v[208:211], v[172:175], v[128:131], v[208:211]\n" \
  "s_waitcnt vmcnt(16)\n" \
  "ds_read_b128 v[176:179], v248 offset:8192\n" \
  "ds_read_b128 v[180:183], v249 offset:8192\n" \
  "v_mfma_f32_16x16x32_bf16 v[212:215], v[172:175], v[132:135], v[212:215]\n" \
  "ds_read_b128 v[184:187], v250 offset:8192\n" \
  "ds_read_b128 v[188:191], v251 offset:8192\n" \
  "ds_read_b128 v[192:195], v248 offset:8448\n" \
  "v_mfma_f32_16x16x32_bf16 v[216:219], v[172:175], v[136:139], v[216:219]\n" \
  "ds_read_b128 v[196:199], v249 offset:8448\n" \
  "ds_read_b128 v[200:203], v250 offset:8448\n" \
  "ds_read_b128 v[204:207], v251 offset:8448\n" \
  "v_mfma_f32_16x16x32_bf16 v[220:223], v[172:175], v[140:143], v[220:223]\n" \
  "s_waitcnt lgkmcnt(7)\n" \
  "v_mfma_f32_16x16x32_bf16 v[224:227], v[176:179], v[16:19], 0\n" \
  "s_barrier\n" \
  "s_lshl_b32 s43, s51, 17\n" \
  "s_add_i32 s43, s43, %3\n" \
  "v_mfma_f32_16x16x32_bf16 v[228:231], v[176:179], v[20:23], 0\n" \
  "s_add_i32 m0, %4, 0\n" \
  "v_xor_b32 v252, 0, %2\n" \
  "s_add_i32 s40, s43, 0\n" \
  "buffer_load_dwordx4 v252, %1, s40 offen lds\n" \
  "s_add_i32 m0, %4, 1024\n" \
  "v_xor_b32 v252, 32, %2\n" \
  "s_add_i32 s40, s43, 16384\n" \
  "buffer_load_dwordx4 v252, %1, s40 offen lds\n" \
  "v_mfma_f32_16x16x32_bf16 v[232:235], v[176:179], v[24:27], 0\n" \
  "s_add_i32 m0, %4, 2048\n" \
  "v_xor_b32 v252, 64, %2\n" \
  "s_add_i32 s40, s43, 32768\n" \
  "buffer_load_dwordx4 v252, %1, s40 offen lds\n" \
  "s_add_i32 m0, %4, 3072\n" \
  "v_xor_b32 v252, 96, %2\n" \
  "s_add_i32 s40, s43, 49152\n" \
  "buffer_load_dwordx4 v252, %1, s40 offen lds\n" \
  "v_mfma_f32_16x16x32_bf16 v[236:239], v[176:179], v[28:31], 0\n" \
  "s_add_i32 m0, %4, 4096\n" \
  "v_xor_b32 v252, 128, %2\n" \
  "s_add_i32 s40, s43, 65536\n" \
  "buffer_load_dwordx4 v252, %1, s40 offen lds\n" \
  "s_add_i32 m0, %4, 5120\n" \
  "v_xor_b32 v252, 160, %2\n" \
  "s_add_i32 s40, s43, 81920\n" \
  "buffer_load_dwordx4 v252, %1, s40 offen lds\n" \
  "s_waitcnt lgkmcnt(6)\n" \
  "v_mfma_f32_16x16x32_bf16 v[224:227], v[180:183], v[32:35], v[224:227]\n" \
  "s_add_i32 m0, %4, 6144\n" \
  "v_xor_b32 v252, 192, %2\n" \
  "s_add_i32 s40, s43, 98304\n" \
  "buffer_load_dwordx4 v252, %1, s40 offen lds\n" \
  "s_add_i32 m0, %4, 7168\n" \
  "v_xor_b32 v252, 224, %2\n" \
  "s_add_i32 s40, s43, 114688\n" \
  "buffer_load_dwordx4 v252, %1, s40 offen lds\n" \
  "v_mfma_f32_16x16x32_bf16 v[228:231], v[180:183], v[36:39], v[228:231]\n" \
  "s_lshl_b32 s43, s50, 17\n" \
  "s_add_i32 s43, s43, %3\n" \
  "s_add_i32 m0, %14, 0\n" \
  "v_xor_b32 v252, 0, %2\n" \
  "s_add_i32 s40, s43, 0\n" \
  "buffer_load_dwordx4 v252, %1, s40 offen lds\n" \
  "v_mfma_f32_16x16x32_bf16 v[232:235], v[180:183], v[40:43], v[232:235]\n" \
  "s_add_i32 m0, %14, 1024\n" \
  "v_xor_b32 v252, 32, %2\n" \
  "s_add_i32 s40, s43, 16384\n" \
  "buffer_load_dwordx4 v252, %1, s40 offen lds\n" \
  "s_add_i32 m0, %14, 2048\n" \
  "v_xor_b32 v252, 64, %2\n" \
  "s_add_i32 s40, s43, 32768\n" \
  "buffer_load_dwordx4 v252, %1, s40 offen lds\n" \
  "v_mfma_f32_16x16x32_bf16 v[236:239], v[180:183], v[44:47], v[236:239]\n" \
  "s_add_i32 m0, %14, 3072\n" \
  "v_xor_b32 v252, 96, %2\n" \
  "s_add_i32 s40, s43, 49152\n" \
  "buffer_load_dwordx4 v252, %1, s40 offen lds\n" \
  "s_add_i32 m0, %14, 4096\n" \
  "v_xor_b32 v252, 128, %2\n" \
  "s_add_i32 s40, s43, 65536\n" \
  "buffer_load_dwordx4 v252, %1, s40 offen lds\n" \
  "s_waitcnt lgkmcnt(5)\n" \
  "v_mfma_f32_16x16x32_bf16 v[224:227], v[184:187], v[48:51], v[224:227]\n" \
  "s_add_i32 m0, %14, 5120\n" \
  "v_xor_b32 v252, 160, %2\n" \
  "s_add_i32 s40, s43, 81920\n" \
  "buffer_load_dwordx4 v252, %1, s40 offen lds\n" \
  "s_add_i32 m0, %14, 6144\n" \
  "v_xor_b32 v252, 192, %2\n" \
  "s_add_i32 s40, s43, 98304\n" \
  "buffer_load_dwordx4 v252, %1, s40 offen lds\n" \
  "v_mfma_f32_16x16x32_bf16 v[228:231], v[184:187], v[52:55], v[228:231]\n" \
  "s_add_i32 m0, %14, 7168\n" \
  "v_xor_b32 v252, 224, %2\n" \
  "s_add_i32 s40, s43, 114688\n" \
  "buffer_load_dwordx4 v252, %1, s40 offen lds\n" \
  "s_waitcnt lgkmcnt(0)\n" \
  "v_mfma_f32_16x16x32_bf16 v[232:235], v[184:187], v[56:59], v[232:235]\n" \
  "s_lshl_b32 s43, s52, 17\n" \
  "s_add_i32 s43, s43, %3\n" \
  "s_add_i32 m0, %4, 8192\n" \
  "v_xor_b32 v252, 0, %2\n" \
  "s_add_i32 s40, s43, 0\n" \
  "buffer_load_dwordx4 v252, %1, s40 offen lds\n" \
  "v_mfma_f32_16x16x32_bf16 v[236:239], v[184:187], v[60:63], v[236:239]\n" \
  "s_add_i32 m0, %4, 9216\n" \
  "v_xor_b32 v252, 32, %2\n" \
  "s_add_i32 s40, s43, 16384\n" \
  "buffer_load_dwordx4 v252, %1, s40 offen lds\n" \
  "s_add_i32 m0, %4, 10240\n" \
  "v_xor_b32 v252, 64, %2\n" \
  "s_add_i32 s40, s43, 32768\n" \
  "buffer_load_dwordx4 v252, %1, s40 offen lds\n" \
  "s_waitcnt lgkmcnt(4)\n" \
  "v_mfma_f32_16x16x32_bf16 v[224:227], v[188:191], v[64:67], v[224:227]\n" \
  "s_add_i32 m0, %4, 11264\n" \
  "v_xor_b32 v252, 96, %2\n" \
  "s_add_i32 s40, s43, 49152\n" \
  "buffer_load_dwordx4 v252, %1, s40 offen lds\n" \
  "s_add_i32 m0, %4, 12288\n" \
  "v_xor_b32 v252, 128, %2\n" \
  "s_add_i32 s40, s43, 65536\n" \
  "buffer_load_dwordx4 v252, %1, s40 offen lds\n" \
  "v_mfma_f32_16x16x32_bf16 v[228:231], v[188:191], v[68:71], v[228:231]\n" \
  "s_add_i32 m0, %4, 13312\n" \
  "v_xor_b32 v252, 160, %2\n" \
  "s_add_i32 s40, s43, 81920\n" \
  "buffer_load_dwordx4 v252, %1, s40 offen lds\n" \
  "s_add_i32 m0, %4, 14336\n" \
  "v_xor_b32 v252, 192, %2\n" \
  "s_add_i32 s40, s43, 98304\n" \
  "buffer_load_dwordx4 v252, %1, s40 offen lds\n" \
  "v_mfma_f32_16x16x32_bf16 v[232:235], v[188:191], v[72:75], v[232:235]\n" \
  "s_add_i32 m0, %4, 15360\n" \
  "v_xor_b32 v252, 224, %2\n" \
  "s_add_i32 s40, s43, 114688\n" \
  "buffer_load_dwordx4 v252, %1, s40 offen lds\n" \
  "v_mfma_f32_16x16x32_bf16 v[236:239], v[188:191], v[76:79], v[236:239]\n" \
  "s_waitcnt lgkmcnt(3)\n" \
  "v_mfma_f32_16x16x32_bf16 v[224:227], v[192:195], v[80:83], v[224:227]\n" \
  "s_add_i32 s41, %13, s46\n" \
  "s_lshr_b32 s42, s41, 1\n" \
  "s_and_b32 s41, s41, 1\n" \
  "s_lshl_b32 s42, s42, 10\n" \
  "s_lshl_b32 s41, s41, 9\n" \
  "s_add_i32 s41, s41, s42\n" \
  "s_add_i32 s41, s41, %12\n" \
  "s_add_i32 s42, s41, 458752\n" \
  "v_mfma_f32_16x16x32_bf16 v[228:231], v[192:195], v[84:87], v[228:231]\n" \
  "v_cvt_pk_bf16_f32 v240, v208, v209\n" \
  "v_cvt_pk_bf16_f32 v242, v212, v213\n" \
  "v_mfma_f32_16x16x32_bf16 v[232:235], v[192:195], v[88:91], v[232:235]\n" \
  "v_cvt_pk_bf16_f32 v241, v210, v211\n" \
  "v_cvt_pk_bf16_f32 v243, v214, v215\n" \
  "v_mfma_f32_16x16x32_bf16 v[236:239], v[192:195], v[92:95], v[236:239]\n" \
  "s_nop 1\n" \
  "v_permlane16_swap_b32 v240, v242\n" \
  "s_waitcnt lgkmcnt(2)\n" \
  "v_mfma_f32_16x16x32_bf16 v[224:227], v[196:199], v[96:99], v[224:227]\n" \
  "v_permlane16_swap_b32 v241, v243\n" \
  "buffer_store_dwordx4 v[240:243], %11, %0, s41 offen sc1\n" \
  "s_waitcnt vmcnt(33)\n" \
  "v_mfma_f32_16x16x32_bf16 v[228:231], v[196:199], v[100:103], v[228:231]\n" \
  "v_cvt_pk_bf16_f32 v244, v216, v217\n" \
  "v_cvt_pk_bf16_f32 v246, v220, v221\n" \
  "ds_read_b128 v[144:147], v248 offset:16384\n" \
  "v_mfma_f32_16x16x32_bf16 v[232:235], v[196:199], v[104:107], v[232:235]\n" \
  "v_cvt_pk_bf16_f32 v245, v218, v219\n" \
  "v_cvt_pk_bf16_f32 v247, v222, v223\n" \
  "ds_read_b128 v[148:151], v249 offset:16384\n" \
  "v_mfma_f32_16x16x32_bf16 v[236:239], v[196:199], v[108:111], v[236:239]\n" \
  "s_nop 1\n" \
  "v_permlane16_swap_b32 v244, v246\n" \
  "ds_read_b128 v[152:155], v250 offset:16384\n" \
  "s_waitcnt lgkmcnt(4)\n" \
  "v_mfma_f32_16x16x32_bf16 v[224:227], v[200:203], v[112:115], v[224:227]\n" \
  "v_permlane16_swap_b32 v245, v247\n" \
  "buffer_store_dwordx4 v[244:247], %11, %0, s42 offen sc1\n" \
  "ds_read_b128 v[156:159], v251 offset:16384\n" \
  "v_mfma_f32_16x16x32_bf16 v[228:231], v[200:203], v[116:119], v[228:231]\n" \
  "ds_read_b128 v[160:163], v248 offset:16640\n" \
  "v_mfma_f32_16x16x32_bf16 v[232:235], v[200:203], v[120:123], v[232:235]\n" \
  "ds_read_b128 v[164:167], v249 offset:16640\n" \
  "v_mfma_f32_16x16x32_bf16 v[236:239], v[200:203], v[124:127], v[236:239]\n" \
  "ds_read_b128 v[168:171], v250 offset:16640\n" \
  "s_waitcnt lgkmcnt(7)\n" \
  "v_mfma_f32_16x16x32_bf16 v[224:227], v[204:207], v[128:131], v[224:227]\n" \
  "ds_read_b128 v[172:175], v251 offset:16640\n" \
  "v_mfma_f32_16x16x32_bf16 v[228:231], v[204:207], v[132:135], v[228:231]\n" \
  "v_mfma_f32_16x16x32_bf16 v[232:235], v[204:207], v[136:139], v[232:235]\n" \
  "v_mfma_f32_16x16x32_bf16 v[236:239], v[204:207], v[140:143], v[236:239]\n" \
  "s_waitcnt lgkmcnt(7)\n" \
  "v_mfma_f32_16x16x32_bf16 v[208:211], v[144:147], v[16:19], 0\n" \
  "v_mfma_f32_16x16x32_bf16 v[212:215], v[144:147], v[20:23], 0\n" \
  "v_mfma_f32_16x16x32_bf16 v[216:219], v[144:147], v[24:27], 0\n" \
  "v_mfma_f32_16x16x32_bf16 v[220:223], v[144:147], v[28:31], 0\n" \
  "s_add_i32 s41, %13, s47\n" \
  "s_lshr_b32 s42, s41, 1\n" \
  "s_and_b32 s41, s41, 1\n" \
  "s_lshl_b32 s42, s42, 10\n" \
  "s_waitcnt lgkmcnt(6)\n" \
  "v_mfma_f32_16x16x32_bf16 v[208:211], v[148:151], v[32:35], v[208:211]\n" \
  "s_lshl_b32 s41, s41, 9\n" \
  "s_add_i32 s41, s41, s42\n" \
  "s_add_i32 s41, s41, %12\n" \
  "s_add_i32 s42, s41, 458752\n" \
  "v_mfma_f32_16x16x32_bf16 v[212:215], v[148:151], v[36:39], v[212:215]\n" \
  "v_cvt_pk_bf16_f32 v240, v224, v225\n" \
  "v_mfma_f32_16x16x32_bf16 v[216:219], v[148:151], v[40:43], v[216:219]\n" \
  "v_cvt_pk_bf16_f32 v242, v228, v229\n" \
  "v_mfma_f32_16x16x32_bf16 v[220:223], v[148:151], v[44:47], v[220:223]\n" \
  "v_cvt_pk_bf16_f32 v241, v226, v227\n" \
  "s_waitcnt lgkmcnt(5)\n" \
  "v_mfma_f32_16x16x32_bf16 v[208:211], v[152:155], v[48:51], v[208:211]\n" \
  "v_cvt_pk_bf16_f32 v243, v230, v231\n" \
  "v_mfma_f32_16x16x32_bf16 v[212:215], v[152:155], v[52:55], v[212:215]\n" \
  "s_nop 1\n" \
  "v_mfma_f32_16x16x32_bf16 v[216:219], v[152:155], v[56:59], v[216:219]\n" \
  "v_permlane16_swap_b32 v240, v242\n" \
  "v_mfma_f32_16x16x32_bf16 v[220:223], v[152:155], v[60:63], v[220:223]\n" \
  "v_permlane16_swap_b32 v241, v243\n" \
  "s_waitcnt lgkmcnt(4)\n" \
  "v_mfma_f32_16x16x32_bf16 v[208:211], v[156:159], v[64:67], v[208:211]\n" \
  "buffer_store_dwordx4 v[240:243], %11, %0, s41 offen sc1\n" \
  "v_mfma_f32_16x16x32_bf16 v[212:215], v[156:159], v[68:71], v[212:215]\n" \
  "v_cvt_pk_bf16_f32 v244, v232, v233\n" \
  "v_mfma_f32_16x16x32_bf16 v[216:219], v[156:159], v[72:75], v[216:219]\n" \
  "v_cvt_pk_bf16_f32 v246, v236, v237\n" \
  "v_mfma_f32_16x16x32_bf16 v[220:223], v[156:159], v[76:79], v[220:223]\n" \
  "v_cvt_pk_bf16_f32 v245, v234, v235\n" \
  "s_waitcnt lgkmcnt(3)\n" \
  "v_mfma_f32_16x16x32_bf16 v[208:211], v[160:163], v[80:83], v[208:211]\n" \
  "v_cvt_pk_bf16_f32 v247, v238, v239\n" \
  "v_mfma_f32_16x16x32_bf16 v[212:215], v[160:163], v[84:87], v[212:215]\n" \
  "s_nop 1\n" \
  "v_mfma_f32_16x16x32_bf16 v[216:219], v[160:163], v[88:91], v[216:219]\n" \
  "v_permlane16_swap_b32 v244, v246\n" \
  "v_mfma_f32_16x16x32_bf16 v[220:223], v[160:163], v[92:95], v[220:223]\n" \
  "v_permlane16_swap_b32 v245, v247\n" \
  "s_waitcnt lgkmcnt(2)\n" \
  "v_mfma_f32_16x16x32_bf16 v[208:211], v[164:167], v[96:99], v[208:211]\n" \
  "buffer_store_dwordx4 v[244:247], %11, %0, s42 offen sc1\n" \
  "s_waitcnt vmcnt(28)\n" \
  "v_mfma_f32_16x16x32_bf16 v[212:215], v[164:167], v[100:103], v[212:215]\n" \
  "ds_read_b128 v[176:179], v248 offset:24576\n" \
  "v_mfma_f32_16x16x32_bf16 v[216:219], v[164:167], v[104:107], v[216:219]\n" \
  "ds_read_b128 v[180:183], v249 offset:24576\n" \
  "v_mfma_f32_16x16x32_bf16 v[220:223], v[164:167], v[108:111], v[220:223]\n" \
  "ds_read_b128 v[184:187], v250 offset:24576\n" \
  "s_waitcnt lgkmcnt(4)\n" \
  "v_mfma_f32_16x16x32_bf16 v[208:211], v[168:171], v[112:115], v[208:211]\n" \
  "ds_read_b128 v[188:191], v251 offset:24576\n" \
  "v_mfma_f32_16x16x32_bf16 v[212:215], v[168:171], v[116:119], v[212:215]\n" \
  "ds_read_b128 v[192:195], v248 offset:24832\n" \
  "v_mfma_f32_16x16x32_bf16 v[216:219], v[168:171], v[120:123], v[216:219]\n" \
  "ds_read_b128 v[196:199], v249 offset:24832\n" \
  "v_mfma_f32_16x16x32_bf16 v[220:223], v[168:171], v[124:127], v[220:223]\n" \
  "ds_read_b128 v[200:203], v250 offset:24832\n" \
  "s_waitcnt lgkmcnt(7)\n" \
  "v_mfma_f32_16x16x32_bf16 v[208:211], v[172:175], v[128:131], v[208:211]\n" \
  "ds_read_b128 v[204:207], v251 offset:24832\n" \
  "v_mfma_f32_16x16x32_bf16 v[212:215], v[172:175], v[132:135], v[212:215]\n" \
  "v_mfma_f32_16x16x32_bf16 v[216:219], v[172:175], v[136:139], v[216:219]\n" \
  "v_mfma_f32_16x16x32_bf16 v[220:223], v[172:175], v[140:143], v[220:223]\n" \
  "s_waitcnt lgkmcnt(7)\n" \
  "v_mfma_f32_16x16x32_bf16 v[224:227], v[176:179], v[16:19], 0\n" \
  "v_mfma_f32_16x16x32_bf16 v[228:231], v[176:179], v[20:23], 0\n" \
  "v_mfma_f32_16x16x32_bf16 v[232:235], v[176:179], v[24:27], 0\n" \
  "v_mfma_f32_16x16x32_bf16 v[236:239], v[176:179], v[28:31], 0\n" \
  "s_add_i32 s41, %13, s48\n" \
  "s_lshr_b32 s42, s41, 1\n" \
  "s_and_b32 s41, s41, 1\n" \
  "s_lshl_b32 s42, s42, 10\n" \
  "s_waitcnt lgkmcnt(6)\n" \
  "v_mfma_f32_16x16x32_bf16 v[224:227], v[180:183], v[32:35], v[224:227]\n" \
  "s_lshl_b32 s41, s41, 9\n" \
  "s_add_i32 s41, s41, s42\n" \
  "s_add_i32 s41, s41, %12\n" \
  "s_add_i32 s42, s41, 458752\n" \
  "v_mfma_f32_16x16x32_bf16 v[228:231], v[180:183], v[36:39], v[228:231]\n" \
  "v_cvt_pk_bf16_f32 v240, v208, v209\n" \
  "v_mfma_f32_16x16x32_bf16 v[232:235], v[180:183], v[40:43], v[232:235]\n" \
  "v_cvt_pk_bf16_f32 v242, v212, v213\n" \
  "v_mfma_f32_16x16x32_bf16 v[236:239], v[180:183], v[44:47], v[236:239]\n" \
  "v_cvt_pk_bf16_f32 v241, v210, v211\n" \
  "s_waitcnt lgkmcnt(5)\n" \
  "v_mfma_f32_16x16x32_bf16 v[224:227], v[184:187], v[48:51], v[224:227]\n" \
  "v_cvt_pk_bf16_f32 v243, v214, v215\n" \
  "v_mfma_f32_16x16x32_bf16 v[228:231], v[184:187], v[52:55], v[228:231]\n" \
  "s_nop 1\n" \
  "v_mfma_f32_16x16x32_bf16 v[232:235], v[184:187], v[56:59], v[232:235]\n" \
  "v_permlane16_swap_b32 v240, v242\n" \
  "v_mfma_f32_16x16x32_bf16 v[236:239], v[184:187], v[60:63], v[236:239]\n" \
  "v_permlane16_swap_b32 v241, v243\n" \
  "s_waitcnt lgkmcnt(4)\n" \
  "v_mfma_f32_16x16x32_bf16 v[224:227], v[188:191], v[64:67], v[224:227]\n" \
  "buffer_store_dwordx4 v[240:243], %11, %0, s41 offen sc1\n" \
  "v_mfma_f32_16x16x32_bf16 v[228:231], v[188:191], v[68:71], v[228:231]\n" \
  "v_cvt_pk_bf16_f32 v244, v216, v217\n" \
  "v_mfma_f32_16x16x32_bf16 v[232:235], v[188:191], v[72:75], v[232:235]\n" \
  "v_cvt_pk_bf16_f32 v246, v220, v221\n" \
  "v_mfma_f32_16x16x32_bf16 v[236:239], v[188:191], v[76:79], v[236:239]\n" \
  "v_cvt_pk_bf16_f32 v245, v218, v219\n" \
  "s_waitcnt lgkmcnt(3)\n" \
  "v_mfma_f32_16x16x32_bf16 v[224:227], v[192:195], v[80:83], v[224:227]\n" \
  "v_cvt_pk_bf16_f32 v247, v222, v223\n" \
  "v_mfma_f32_16x16x32_bf16 v[228:231], v[192:195], v[84:87], v[228:231]\n" \
  "s_nop 1\n" \
  "v_mfma_f32_16x16x32_bf16 v[232:235], v[192:195], v[88:91], v[232:235]\n" \
  "v_permlane16_swap_b32 v244, v246\n" \
  "v_mfma_f32_16x16x32_bf16 v[236:239], v[192:195], v[92:95], v[236:239]\n" \
  "v_permlane16_swap_b32 v245, v247\n" \
  "s_waitcnt lgkmcnt(2)\n" \
  "v_mfma_f32_16x16x32_bf16 v[224:227], v[196:199], v[96:99], v[224:227]\n" \
  "buffer_store_dwordx4 v[244:247], %11, %0, s42 offen sc1\n" \
  "s_waitcnt vmcnt(14)\n" \
  "v_mfma_f32_16x16x32_bf16 v[228:231], v[196:199], v[100:103], v[228:231]\n" \
  "ds_read_b128 v[144:147], v12 offset:0\n" \
  "v_mfma_f32_16x16x32_bf16 v[232:235], v[196:199], v[104:107], v[232:235]\n" \
  "ds_read_b128 v[148:151], v13 offset:0\n" \
  "v_mfma_f32_16x16x32_bf16 v[236:239], v[196:199], v[108:111], v[236:239]\n" \
  "ds_read_b128 v[152:155], v14 offset:0\n" \
  "s_waitcnt lgkmcnt(4)\n" \
  "v_mfma_f32_16x16x32_bf16 v[224:227], v[200:203], v[112:115], v[224:227]\n" \
  "ds_read_b128 v[156:159], v15 offset:0\n" \
  "v_mfma_f32_16x16x32_bf16 v[228:231], v[200:203], v[116:119], v[228:231]\n" \
  "ds_read_b128 v[160:163], v12 offset:256\n" \
  "v_mfma_f32_16x16x32_bf16 v[232:235], v[200:203], v[120:123], v[232:235]\n" \
  "ds_read_b128 v[164:167], v13 offset:256\n" \
  "v_mfma_f32_16x16x32_bf16 v[236:239], v[200:203], v[124:127], v[236:239]\n" \
  "ds_read_b128 v[168:171], v14 offset:256\n" \
  "s_waitcnt lgkmcnt(7)\n" \
  "v_mfma_f32_16x16x32_bf16 v[224:227], v[204:207], v[128:131], v[224:227]\n" \
  "ds_read_b128 v[172:175], v15 offset:256\n" \
  "v_mfma_f32_16x16x32_bf16 v[228:231], v[204:207], v[132:135], v[228:231]\n" \
  "v_mfma_f32_16x16x32_bf16 v[232:235], v[204:207], v[136:139], v[232:235]\n" \
  "v_mfma_f32_16x16x32_bf16 v[236:239], v[204:207], v[140:143], v[236:239]\n" \
  "s_waitcnt lgkmcnt(7)\n" \
  "v_mfma_f32_16x16x32_bf16 v[208:211], v[144:147], v[16:19], 0\n" \
  "v_mfma_f32_16x16x32_bf16 v[212:215], v[144:147], v[20:23], 0\n" \
  "v_mfma_f32_16x16x32_bf16 v[216:219], v[144:147], v[24:27], 0\n" \
  "v_mfma_f32_16x16x32_bf16 v[220:223], v[144:147], v[28:31], 0\n" \
  "s_add_i32 s41, %13, s49\n" \
  "s_lshr_b32 s42, s41, 1\n" \
  "s_and_b32 s41, s41, 1\n" \
  "s_lshl_b32 s42, s42, 10\n" \
  "s_waitcnt lgkmcnt(6)\n" \
  "v_mfma_f32_16x16x32_bf16 v[208:211], v[148:151], v[32:35], v[208:211]\n" \
  "s_lshl_b32 s41, s41, 9\n" \
  "s_add_i32 s41, s41, s42\n" \
  "s_add_i32 s41, s41, %12\n" \
  "s_add_i32 s42, s41, 458752\n" \
  "v_mfma_f32_16x16x32_bf16 v[212:215], v[148:151], v[36:39], v[212:215]\n" \
  "v_cvt_pk_bf16_f32 v240, v224, v225\n" \
  "v_mfma_f32_16x16x32_bf16 v[216:219], v[148:151], v[40:43], v[216:219]\n" \
  "v_cvt_pk_bf16_f32 v242, v228, v229\n" \
  "v_mfma_f32_16x16x32_bf16 v[220:223], v[148:151], v[44:47], v[220:223]\n" \
  "v_cvt_pk_bf16_f32 v241, v226, v227\n" \
  "s_waitcnt lgkmcnt(5)\n" \
  "v_mfma_f32_16x16x32_bf16 v[208:211], v[152:155], v[48:51], v[208:211]\n" \
  "v_cvt_pk_bf16_f32 v243, v230, v231\n" \
  "v_mfma_f32_16x16x32_bf16 v[212:215], v[152:155], v[52:55], v[212:215]\n" \
  "s_nop 1\n" \
  "v_mfma_f32_16x16x32_bf16 v[216:219], v[152:155], v[56:59], v[216:219]\n" \
  "v_permlane16_swap_b32 v240, v242\n" \
  "v_mfma_f32_16x16x32_bf16 v[220:223], v[152:155], v[60:63], v[220:223]\n" \
  "v_permlane16_swap_b32 v241, v243\n" \
  "s_waitcnt lgkmcnt(4)\n" \
  "v_mfma_f32_16x16x32_bf16 v[208:211], v[156:159], v[64:67], v[208:211]\n" \
  "buffer_store_dwordx4 v[240:243], %11, %0, s41 offen sc1\n" \
  "v_mfma_f32_16x16x32_bf16 v[212:215], v[156:159], v[68:71], v[212:215]\n" \
  "v_cvt_pk_bf16_f32 v244, v232, v233\n" \
  "v_mfma_f32_16x16x32_bf16 v[216:219], v[156:159], v[72:75], v[216:219]\n" \
  "v_cvt_pk_bf16_f32 v246, v236, v237\n" \
  "v_mfma_f32_16x16x32_bf16 v[220:223], v[156:159], v[76:79], v[220:223]\n" \
  "v_cvt_pk_bf16_f32 v245, v234, v235\n" \
  "s_waitcnt lgkmcnt(3)\n" \
  "v_mfma_f32_16x16x32_bf16 v[208:211], v[160:163], v[80:83], v[208:211]\n" \
  "v_cvt_pk_bf16_f32 v247, v238, v239\n" \
  "v_mfma_f32_16x16x32_bf16 v[212:215], v[160:163], v[84:87], v[212:215]\n" \
  "s_nop 1\n" \
  "v_mfma_f32_16x16x32_bf16 v[216:219], v[160:163], v[88:91], v[216:219]\n" \
  "v_permlane16_swap_b32 v244, v246\n" \
  "v_mfma_f32_16x16x32_bf16 v[220:223], v[160:163], v[92:95], v[220:223]\n" \
  "v_permlane16_swap_b32 v245, v247\n" \
  "s_waitcnt lgkmcnt(2)\n" \
  "v_mfma_f32_16x16x32_bf16 v[208:211], v[164:167], v[96:99], v[208:211]\n" \
  "buffer_store_dwordx4 v[244:247], %11, %0, s42 offen sc1\n" \
  "s_waitcnt vmcnt(24)\n" \
  "v_mfma_f32_16x16x32_bf16 v[212:215], v[164:167], v[100:103], v[212:215]\n" \
  "ds_read_b128 v[176:179], v248 offset:0\n" \
  "v_mfma_f32_16x16x32_bf16 v[216:219], v[164:167], v[104:107], v[216:219]\n" \
  "ds_read_b128 v[180:183], v249 offset:0\n" \
  "v_mfma_f32_16x16x32_bf16 v[220:223], v[164:167], v[108:111], v[220:223]\n" \
  "ds_read_b128 v[184:187], v250 offset:0\n" \
  "s_waitcnt lgkmcnt(4)\n" \
  "v_mfma_f32_16x16x32_bf16 v[208:211], v[168:171], v[112:115], v[208:211]\n" \
  "ds_read_b128 v[188:191], v251 offset:0\n" \
  "v_mfma_f32_16x16x32_bf16 v[212:215], v[168:171], v[116:119], v[212:215]\n" \
  "ds_read_b128 v[192:195], v248 offset:256\n" \
  "v_mfma_f32_16x16x32_bf16 v[216:219], v[168:171], v[120:123], v[216:219]\n" \
  "ds_read_b128 v[196:199], v249 offset:256\n" \
  "v_mfma_f32_16x16x32_bf16 v[220:223], v[168:171], v[124:127], v[220:223]\n" \
  "ds_read_b128 v[200:203], v250 offset:256\n" \
  "s_waitcnt lgkmcnt(7)\n" \
  "v_mfma_f32_16x16x32_bf16 v[208:211], v[172:175], v[128:131], v[208:211]\n" \
  "ds_read_b128 v[204:207], v251 offset:256\n" \
  "v_mfma_f32_16x16x32_bf16 v[212:215], v[172:175], v[132:135], v[212:215]\n" \
  "v_mfma_f32_16x16x32_bf16 v[216:219], v[172:175], v[136:139], v[216:219]\n" \
  "v_mfma_f32_16x16x32_bf16 v[220:223], v[172:175], v[140:143], v[220:223]\n" \
  "s_waitcnt lgkmcnt(7)\n" \
  "v_mfma_f32_16x16x32_bf16 v[224:227], v[176:179], v[16:19], 0\n" \
  "v_mfma_f32_16x16x32_bf16 v[228:231], v[176:179], v[20:23], 0\n" \
  "v_mfma_f32_16x16x32_bf16 v[232:235], v[176:179], v[24:27], 0\n" \
  "v_mfma_f32_16x16x32_bf16 v[236:239], v[176:179], v[28:31], 0\n" \
  "s_add_i32 s41, %13, s50\n" \
  "s_lshr_b32 s42, s41, 1\n" \
  "s_and_b32 s41, s41, 1\n" \
  "s_lshl_b32 s42, s42, 10\n" \
  "s_waitcnt lgkmcnt(6)\n" \
  "v_mfma_f32_16x16x32_bf16 v[224:227], v[180:183], v[32:35], v[224:227]\n" \
  "s_lshl_b32 s41, s41, 9\n" \
  "s_add_i32 s41, s41, s42\n" \
  "s_add_i32 s41, s41, %12\n" \
  "s_add_i32 s42, s41, 458752\n" \
  "v_mfma_f32_16x16x32_bf16 v[228:231], v[180:183], v[36:39], v[228:231]\n" \
  "v_cvt_pk_bf16_f32 v240, v208, v209\n" \
  "v_mfma_f32_16x16x32_bf16 v[232:235], v[180:183], v[40:43], v[232:235]\n" \
  "v_cvt_pk_bf16_f32 v242, v212, v213\n" \
  "v_mfma_f32_16x16x32_bf16 v[236:239], v[180:183], v[44:47], v[236:239]\n" \
  "v_cvt_pk_bf16_f32 v241, v210, v211\n" \
  "s_waitcnt lgkmcnt(5)\n" \
  "v_mfma_f32_16x16x32_bf16 v[224:227], v[184:187], v[48:51], v[224:227]\n" \
  "v_cvt_pk_bf16_f32 v243, v214, v215\n" \
  "v_mfma_f32_16x16x32_bf16 v[228:231], v[184:187], v[52:55], v[228:231]\n" \
  "s_nop 1\n" \
  "v_mfma_f32_16x16x32_bf16 v[232:235], v[184:187], v[56:59], v[232:235]\n" \
  "v_permlane16_swap_b32 v240, v242\n" \
  "v_mfma_f32_16x16x32_bf16 v[236:239], v[184:187], v[60:63], v[236:239]\n" \
  "v_permlane16_swap_b32 v241, v243\n" \
  "s_waitcnt lgkmcnt(4)\n" \
  "v_mfma_f32_16x16x32_bf16 v[224:227], v[188:191], v[64:67], v[224:227]\n" \
  "buffer_store_dwordx4 v[240:243], %11, %0, s41 offen sc1\n" \
  "v_mfma_f32_16x16x32_bf16 v[228:231], v[188:191], v[68:71], v[228:231]\n" \
  "v_cvt_pk_bf16_f32 v244, v216, v217\n" \
  "v_mfma_f32_16x16x32_bf16 v[232:235], v[188:191], v[72:75], v[232:235]\n" \
  "v_cvt_pk_bf16_f32 v246, v220, v221\n" \
  "v_mfma_f32_16x16x32_bf16 v[236:239], v[188:191], v[76:79], v[236:239]\n" \
  "v_cvt_pk_bf16_f32 v245, v218, v219\n" \
  "s_waitcnt lgkmcnt(3)\n" \
  "v_mfma_f32_16x16x32_bf16 v[224:227], v[192:195], v[80:83], v[224:227]\n" \
  "v_cvt_pk_bf16_f32 v247, v222, v223\n" \
  "v_mfma_f32_16x16x32_bf16 v[228:231], v[192:195], v[84:87], v[228:231]\n" \
  "s_nop 1\n" \
  "v_mfma_f32_16x16x32_bf16 v[232:235], v[192:195], v[88:91], v[232:235]\n" \
  "v_permlane16_swap_b32 v244, v246\n" \
  "v_mfma_f32_16x16x32_bf16 v[236:239], v[192:195], v[92:95], v[236:239]\n" \
  "v_permlane16_swap_b32 v245, v247\n" \
  "s_waitcnt lgkmcnt(2)\n" \
  "v_mfma_f32_16x16x32_bf16 v[224:227], v[196:199], v[96:99], v[224:227]\n" \
  "buffer_store_dwordx4 v[244:247], %11, %0, s42 offen sc1\n" \
  "s_waitcnt vmcnt(10)\n" \
  "v_mfma_f32_16x16x32_bf16 v[228:231], v[196:199], v[100:103], v[228:231]\n" \
  "ds_read_b128 v[144:147], v248 offset:8192\n" \
  "v_mfma_f32_16x16x32_bf16 v[232:235], v[196:199], v[104:107], v[232:235]\n" \
  "ds_read_b128 v[148:151], v249 offset:8192\n" \
  "v_mfma_f32_16x16x32_bf16 v[236:239], v[196:199], v[108:111], v[236:239]\n" \
  "ds_read_b128 v[152:155], v250 offset:8192\n" \
  "s_waitcnt lgkmcnt(4)\n" \
  "v_mfma_f32_16x16x32_bf16 v[224:227], v[200:203], v[112:115], v[224:227]\n" \
  "ds_read_b128 v[156:159], v251 offset:8192\n" \
  "v_mfma_f32_16x16x32_bf16 v[228:231], v[200:203], v[116:119], v[228:231]\n" \
  "ds_read_b128 v[160:163], v248 offset:8448\n" \
  "v_mfma_f32_16x16x32_bf16 v[232:235], v[200:203], v[120:123], v[232:235]\n" \
  "ds_read_b128 v[164:167], v249 offset:8448\n" \
  "v_mfma_f32_16x16x32_bf16 v[236:239], v[200:203], v[124:127], v[236:239]\n" \
  "ds_read_b128 v[168:171], v250 offset:8448\n" \
  "s_waitcnt lgkmcnt(7)\n" \
  "v_mfma_f32_16x16x32_bf16 v[224:227], v[204:207], v[128:131], v[224:227]\n" \
  "ds_read_b128 v[172:175], v251 offset:8448\n" \
  "v_mfma_f32_16x16x32_bf16 v[228:231], v[204:207], v[132:135], v[228:231]\n" \
  "v_mfma_f32_16x16x32_bf16 v[232:235], v[204:207], v[136:139], v[232:235]\n" \
  "v_mfma_f32_16x16x32_bf16 v[236:239], v[204:207], v[140:143], v[236:239]\n" \
  "s_waitcnt lgkmcnt(7)\n" \
  "v_mfma_f32_16x16x32_bf16 v[208:211], v[144:147], v[16:19], 0\n" \
  "v_mfma_f32_16x16x32_bf16 v[212:215], v[144:147], v[20:23], 0\n" \
  "v_mfma_f32_16x16x32_bf16 v[216:219], v[144:147], v[24:27], 0\n" \
  "v_mfma_f32_16x16x32_bf16 v[220:223], v[144:147], v[28:31], 0\n" \
  "s_add_i32 s41, %13, s51\n" \
  "s_lshr_b32 s42, s41, 1\n" \
  "s_and_b32 s41, s41, 1\n" \
  "s_lshl_b32 s42, s42, 10\n" \
  "s_waitcnt lgkmcnt(6)\n" \
  "v_mfma_f32_16x16x32_bf16 v[208:211], v[148:151], v[32:35], v[208:211]\n" \
  "s_lshl_b32 s41, s41, 9\n" \
  "s_add_i32 s41, s41, s42\n" \
  "s_add_i32 s41, s41, %12\n" \
  "s_add_i32 s42, s41, 458752\n" \
  "v_mfma_f32_16x16x32_bf16 v[212:215], v[148:151], v[36:39], v[212:215]\n" \
  "v_cvt_pk_bf16_f32 v240, v224, v225\n" \
  "v_mfma_f32_16x16x32_bf16 v[216:219], v[148:151], v[40:43], v[216:219]\n" \
  "v_cvt_pk_bf16_f32 v242, v228, v229\n" \
  "v_mfma_f32_16x16x32_bf16 v[220:223], v[148:151], v[44:47], v[220:223]\n" \
  "v_cvt_pk_bf16_f32 v241, v226, v227\n" \
  "s_waitcnt lgkmcnt(5)\n" \
  "v_mfma_f32_16x16x32_bf16 v[208:211], v[152:155], v[48:51], v[208:211]\n" \
  "v_cvt_pk_bf16_f32 v243, v230, v231\n" \
  "v_mfma_f32_16x16x32_bf16 v[212:215], v[152:155], v[52:55], v[212:215]\n" \
  "s_nop 1\n" \
  "v_mfma_f32_16x16x32_bf16 v[216:219], v[152:155], v[56:59], v[216:219]\n" \
  "v_permlane16_swap_b32 v240, v242\n" \
  "v_mfma_f32_16x16x32_bf16 v[220:223], v[152:155], v[60:63], v[220:223]\n" \
  "v_permlane16_swap_b32 v241, v243\n" \
  "s_waitcnt lgkmcnt(4)\n" \
  "v_mfma_f32_16x16x32_bf16 v[208:211], v[156:159], v[64:67], v[208:211]\n" \
  "buffer_store_dwordx4 v[240:243], %11, %0, s41 offen sc1\n" \
  "v_mfma_f32_16x16x32_bf16 v[212:215], v[156:159], v[68:71], v[212:215]\n" \
  "v_cvt_pk_bf16_f32 v244, v232, v233\n" \
  "v_mfma_f32_16x16x32_bf16 v[216:219], v[156:159], v[72:75], v[216:219]\n" \
  "v_cvt_pk_bf16_f32 v246, v236, v237\n" \
  "v_mfma_f32_16x16x32_bf16 v[220:223], v[156:159], v[76:79], v[220:223]\n" \
  "v_cvt_pk_bf16_f32 v245, v234, v235\n" \
  "s_waitcnt lgkmcnt(3)\n" \
  "v_mfma_f32_16x16x32_bf16 v[208:211], v[160:163], v[80:83], v[208:211]\n" \
  "v_cvt_pk_bf16_f32 v247, v238, v239\n" \
  "v_mfma_f32_16x16x32_bf16 v[212:215], v[160:163], v[84:87], v[212:215]\n" \
  "s_nop 1\n" \
  "v_mfma_f32_16x16x32_bf16 v[216:219], v[160:163], v[88:91], v[216:219]\n" \
  "v_permlane16_swap_b32 v244, v246\n" \
  "v_mfma_f32_16x16x32_bf16 v[220:223], v[160:163], v[92:95], v[220:223]\n" \
  "v_permlane16_swap_b32 v245, v247\n" \
  "s_waitcnt lgkmcnt(2)\n" \
  "v_mfma_f32_16x16x32_bf16 v[208:211], v[164:167], v[96:99], v[208:211]\n" \
  "buffer_store_dwordx4 v[244:247], %11, %0, s42 offen sc1\n" \
  "v_mfma_f32_16x16x32_bf16 v[212:215], v[164:167], v[100:103], v[212:215]\n" \
  "v_mfma_f32_16x16x32_bf16 v[216:219], v[164:167], v[104:107], v[216:219]\n" \
  "v_mfma_f32_16x16x32_bf16 v[220:223], v[164:167], v[108:111], v[220:223]\n" \
  "s_waitcnt lgkmcnt(1)\n" \
  "v_mfma_f32_16x16x32_bf16 v[208:211], v[168:171], v[112:115], v[208:211]\n" \
  "v_mfma_f32_16x16x32_bf16 v[212:215], v[168:171], v[116:119], v[212:215]\n" \
  "v_mfma_f32_16x16x32_bf16 v[216:219], v[168:171], v[120:123], v[216:219]\n" \
  "v_mfma_f32_16x16x32_bf16 v[220:223], v[168:171], v[124:127], v[220:223]\n" \
  "s_waitcnt lgkmcnt(0)\n" \
  "v_mfma_f32_16x16x32_bf16 v[208:211], v[172:175], v[128:131], v[208:211]\n" \
  "v_mfma_f32_16x16x32_bf16 v[212:215], v[172:175], v[132:135], v[212:215]\n" \
  "v_mfma_f32_16x16x32_bf16 v[216:219], v[172:175], v[136:139], v[216:219]\n" \
  "v_mfma_f32_16x16x32_bf16 v[220:223], v[172:175], v[140:143], v[220:223]\n" \
  "s_nop 7\n" \
  "s_add_i32 s41, %13, s52\n" \
  "s_lshr_b32 s42, s41, 1\n" \
  "s_and_b32 s41, s41, 1\n" \
  "s_lshl_b32 s42, s42, 10\n" \
  "s_lshl_b32 s41, s41, 9\n" \
  "s_add_i32 s41, s41, s42\n" \
  "s_add_i32 s41, s41, %12\n" \
  "s_add_i32 s42, s41, 458752\n" \
  "v_cvt_pk_bf16_f32 v240, v208, v209\n" \
  "v_cvt_pk_bf16_f32 v242, v212, v213\n" \
  "v_cvt_pk_bf16_f32 v241, v210, v211\n" \
  "v_cvt_pk_bf16_f32 v243, v214, v215\n" \
  "s_nop 1\n" \
  "v_permlane16_swap_b32 v240, v242\n" \
  "v_permlane16_swap_b32 v241, v243\n" \
  "buffer_store_dwordx4 v[240:243], %11, %0, s41 offen sc1\n" \
  "v_cvt_pk_bf16_f32 v244, v216, v217\n" \
  "v_cvt_pk_bf16_f32 v246, v220, v221\n" \
  "v_cvt_pk_bf16_f32 v245, v218, v219\n" \
  "v_cvt_pk_bf16_f32 v247, v222, v223\n" \
  "s_nop 1\n" \
  "v_permlane16_swap_b32 v244, v246\n" \
  "v_permlane16_swap_b32 v245, v247\n" \
  "buffer_store_dwordx4 v[244:247], %11, %0, s42 offen sc1\n"

#define ASM_SCA \
  "v_mbcnt_lo_u32_b32 v7, -1, 0\n" \
  "v_mbcnt_hi_u32_b32 v7, -1, v7\n" \
  "v_lshlrev_b32 v0, 4, v7\n" \
  "v_and_b32 v6, 8, v7\n" \
  "v_mov_b32 v1, 0x4444\n" \
  "v_mov_b32 v2, 0xeeee\n" \
  "v_cmp_ne_u32 vcc, 0, v6\n" \
  "s_nop 1\n" \
  "v_cndmask_b32 v1, v1, v2, vcc\n" \
  "s_mov_b32 s40, 0\n" \
  "s_mov_b32 s41, 262144\n" \
  "s_mov_b32 s42, %7\n" \
  "s_mov_b32 s47, 104857600\n" \
  "s_mov_b32 s46, 0\n" \
  "s_mul_i32 s68, %1, 401408\n" \
  "buffer_load_dwordx4 a[0:3], v0, %0, s68 offen offset:0 sc1\n" \
  "buffer_load_dwordx4 a[4:7], v0, %0, s68 offen offset:1024 sc1\n" \
  "buffer_load_dwordx4 a[8:11], v0, %0, s68 offen offset:2048 sc1\n" \
  "buffer_load_dwordx4 a[12:15], v0, %0, s68 offen offset:3072 sc1\n" \
  "s_add_i32 s68, s68, 4096\n" \
  "buffer_load_dwordx4 a[16:19], v0, %0, s68 offen offset:0 sc1\n" \
  "buffer_load_dwordx4 a[20:23], v0, %0, s68 offen offset:1024 sc1\n" \
  "buffer_load_dwordx4 a[24:27], v0, %0, s68 offen offset:2048 sc1\n" \
  "buffer_load_dwordx4 a[28:31], v0, %0, s68 offen offset:3072 sc1\n" \
  "s_add_i32 s68, s68, 4096\n" \
  "buffer_load_dwordx4 a[32:35], v0, %0, s68 offen offset:0 sc1\n" \
  "buffer_load_dwordx4 a[36:39], v0, %0, s68 offen offset:1024 sc1\n" \
  "buffer_load_dwordx4 a[40:43], v0, %0, s68 offen offset:2048 sc1\n" \
  "buffer_load_dwordx4 a[44:47], v0, %0, s68 offen offset:3072 sc1\n" \
  "s_add_i32 s68, s68, 4096\n" \
  "buffer_load_dwordx4 a[48:51], v0, %0, s68 offen offset:0 sc1\n" \
  "buffer_load_dwordx4 a[52:55], v0, %0, s68 offen offset:1024 sc1\n" \
  "buffer_load_dwordx4 a[56:59], v0, %0, s68 offen offset:2048 sc1\n" \
  "buffer_load_dwordx4 a[60:63], v0, %0, s68 offen offset:3072 sc1\n" \
  "s_add_i32 s68, s68, 4096\n" \
  "buffer_load_dwordx4 a[64:67], v0, %0, s68 offen offset:0 sc1\n" \
  "buffer_load_dwordx4 a[68:71], v0, %0, s68 offen offset:1024 sc1\n" \
  "buffer_load_dwordx4 a[72:75], v0, %0, s68 offen offset:2048 sc1\n" \
  "buffer_load_dwordx4 a[76:79], v0, %0, s68 offen offset:3072 sc1\n" \
  "s_add_i32 s68, s68, 4096\n" \
  "buffer_load_dwordx4 a[80:83], v0, %0, s68 offen offset:0 sc1\n" \
  "buffer_load_dwordx4 a[84:87], v0, %0, s68 offen offset:1024 sc1\n" \
  "buffer_load_dwordx4 a[88:91], v0, %0, s68 offen offset:2048 sc1\n" \
  "buffer_load_dwordx4 a[92:95], v0, %0, s68 offen offset:3072 sc1\n" \
  "s_add_i32 s68, s68, 4096\n" \
  "buffer_load_dwordx4 a[96:99], v0, %0, s68 offen offset:0 sc1\n" \
  "buffer_load_dwordx4 a[100:103], v0, %0, s68 offen offset:1024 sc1\n" \
  "buffer_load_dwordx4 a[104:107], v0, %0, s68 offen offset:2048 sc1\n" \
  "buffer_load_dwordx4 a[108:111], v0, %0, s68 offen offset:3072 sc1\n" \
  "s_add_i32 s68, s68, 4096\n" \
  "buffer_load_dwordx4 a[112:115], v0, %0, s68 offen offset:0 sc1\n" \
  "buffer_load_dwordx4 a[116:119], v0, %0, s68 offen offset:1024 sc1\n" \
  "buffer_load_dwordx4 a[120:123], v0, %0, s68 offen offset:2048 sc1\n" \
  "buffer_load_dwordx4 a[124:127], v0, %0, s68 offen offset:3072 sc1\n" \
  "s_add_i32 s68, s68, 4096\n" \
  "buffer_load_dwordx4 a[128:131], v0, %0, s68 offen offset:0 sc1\n" \
  "buffer_load_dwordx4 a[132:135], v0, %0, s68 offen offset:1024 sc1\n" \
  "buffer_load_dwordx4 a[136:139], v0, %0, s68 offen offset:2048 sc1\n" \
  "buffer_load_dwordx4 a[140:143], v0, %0, s68 offen offset:3072 sc1\n" \
  "s_add_i32 s68, s68, 4096\n" \
  "buffer_load_dwordx4 a[144:147], v0, %0, s68 offen offset:0 sc1\n" \
  "buffer_load_dwordx4 a[148:151], v0, %0, s68 offen offset:1024 sc1\n" \
  "buffer_load_dwordx4 a[152:155], v0, %0, s68 offen offset:2048 sc1\n" \
  "buffer_load_dwordx4 a[156:159], v0, %0, s68 offen offset:3072 sc1\n" \
  "s_add_i32 s68, s68, 4096\n" \
  "buffer_load_dwordx4 a[160:163], v0, %0, s68 offen offset:0 sc1\n" \
  "buffer_load_dwordx4 a[164:167], v0, %0, s68 offen offset:1024 sc1\n" \
  "buffer_load_dwordx4 a[168:171], v0, %0, s68 offen offset:2048 sc1\n" \
  "buffer_load_dwordx4 a[172:175], v0, %0, s68 offen offset:3072 sc1\n" \
  "s_add_i32 s68, s68, 4096\n" \
  "buffer_load_dwordx4 a[176:179], v0, %0, s68 offen offset:0 sc1\n" \
  "buffer_load_dwordx4 a[180:183], v0, %0, s68 offen offset:1024 sc1\n" \
  "buffer_load_dwordx4 a[184:187], v0, %0, s68 offen offset:2048 sc1\n" \
  "buffer_load_dwordx4 a[188:191], v0, %0, s68 offen offset:3072 sc1\n" \
  "s_add_i32 s68, s68, 4096\n" \
  "buffer_load_dwordx4 a[192:195], v0, %0, s68 offen offset:0 sc1\n" \
  "buffer_load_dwordx4 a[196:199], v0, %0, s68 offen offset:1024 sc1\n" \
  "buffer_load_dwordx4 a[200:203], v0, %0, s68 offen offset:2048 sc1\n" \
  "buffer_load_dwordx4 a[204:207], v0, %0, s68 offen offset:3072 sc1\n" \
  "s_add_i32 s68, s68, 4096\n" \
  "buffer_load_dwordx4 a[208:211], v0, %0, s68 offen offset:0 sc1\n" \
  "buffer_load_dwordx4 a[212:215], v0, %0, s68 offen offset:1024 sc1\n" \
  "buffer_load_dwordx4 a[216:219], v0, %0, s68 offen offset:2048 sc1\n" \
  "buffer_load_dwordx4 a[220:223], v0, %0, s68 offen offset:3072 sc1\n" \
  "s_add_i32 s68, s68, 4096\n" \
  "s_lshl_b32 s43, 0, 9\n" \
  "s_add_i32 s43, s43, %6\n" \
  "s_add_i32 s43, s43, 43008\n" \
  "v_lshlrev_b32 v3, 3, v7\n" \
  "v_xor_b32 v118, 32, v3\n" \
  "v_add_u32 v3, s43, v3\n" \
  "v_add_u32 v118, s43, v118\n" \
  "v_bfe_u32 v6, v7, 2, 2\n" \
  "v_lshlrev_b32 v5, 3, v6\n" \
  "v_bfe_u32 v6, v7, 4, 1\n" \
  "v_lshl_add_u32 v5, v6, 6, v5\n" \
  "v_lshrrev_b32 v6, 5, v7\n" \
  "v_lshl_add_u32 v5, v6, 8, v5\n" \
  "v_bfe_u32 v6, v7, 1, 1\n" \
  "v_lshlrev_b32 v130, 5, v6\n" \
  "v_xor_b32 v5, v5, v130\n" \
  "v_lshl_add_u32 v5, v6, 9, v5\n" \
  "v_and_b32 v6, 1, v7\n" \
  "v_lshl_add_u32 v5, v6, 7, v5\n" \
  "v_add_u32 v5, %6, v5\n" \
  "v_add_u32 v5, 43008, v5\n" \
  "v_lshrrev_b32 v6, 4, v7\n" \
  "v_lshlrev_b32 v119, 7, v6\n" \
  "v_and_b32 v6, 7, v7\n" \
  "v_lshl_add_u32 v119, v6, 4, v119\n" \
  "v_bfe_u32 v6, v7, 3, 1\n" \
  "v_lshlrev_b32 v120, 9, v6\n" \
  "v_xor_b32 v78, 32, v5\n" \
  "v_and_b32 v6, 15, v7\n" \
  "v_lshlrev_b32 v76, 5, v6\n" \
  "v_lshrrev_b32 v6, 5, v7\n" \
  "v_lshl_add_u32 v76, v6, 9, v76\n" \
  "v_bfe_u32 v6, v7, 4, 1\n" \
  "v_lshl_add_u32 v76, v6, 4, v76\n" \
  "v_and_b32 v6, 7, v7\n" \
  "v_lshlrev_b32 v77, 2, v6\n" \
  "v_bfe_u32 v6, v7, 3, 1\n" \
  "v_lshl_add_u32 v77, v6, 9, v77\n" \
  "v_lshrrev_b32 v6, 4, v7\n" \
  "v_lshl_add_u32 v77, v6, 7, v77\n" \
  "s_add_i32 s43, %6, 74752\n" \
  "v_add_u32 v76, s43, v76\n" \
  "v_add_u32 v77, s43, v77\n" \
  "v_and_b32 v6, 7, v7\n" \
  "v_lshlrev_b32 v116, 4, v6\n" \
  "v_mov_b32 v117, v116\n" \
  "v_bfe_u32 v6, v7, 3, 1\n" \
  "v_lshl_add_u32 v116, v6, 8, v116\n" \
  "v_lshrrev_b32 v6, 5, v7\n" \
  "v_lshl_add_u32 v116, v6, 7, v116\n" \
  "v_lshl_add_u32 v117, v6, 2, v117\n" \
  "v_bfe_u32 v6, v7, 4, 1\n" \
  "v_lshl_add_u32 v116, v6, 3, v116\n" \
  "v_lshl_add_u32 v117, v6, 3, v117\n" \
  "s_lshl_b32 s43, %1, 9\n" \
  "v_add_u32 v116, s43, v116\n" \
  "s_lshl_b32 s43, %1, 7\n" \
  "v_add_u32 v117, s43, v117\n" \
  "s_mov_b32 s52, 0x00ff00ff\n" \
  "s_mov_b32 s53, 0x00ff00ff\n" \
  "s_mov_b32 s54, 0x00ff00ff\n" \
  "s_mov_b32 s55, 0xff00ff00\n" \
  "v_min_u32 v79, 6, v7\n" \
  "s_mul_i32 s43, %1, 7\n" \
  "v_add_u32 v79, s43, v79\n" \
  "v_lshlrev_b32 v79, 7, v79\n" \
  "v_add_u32 v79, 12288, v79\n" \
  "fg0_%=:\n" \
  "buffer_load_dword v6, v79, %0, s47 offen sc1\n" \
  "s_waitcnt vmcnt(0)\n" \
  "v_cmp_ge_u32 vcc, 0, v6\n" \
  "s_cbranch_vccz fg0ok_%=\n" \
  "s_sleep 3\n" \
  "s_sub_u32 s41, s41, 1\n" \
  "s_cbranch_scc1 tmo_%=\n" \
  "s_branch fg0_%=\n" \
  "fg0ok_%=:\n" \
  "s_mul_i32 s68, %1, 28672\n" \
  "s_add_i32 s68, s68, 17825792\n" \
  "s_add_i32 s61, s68, 0\n" \
  "s_add_i32 s62, s68, 4096\n" \
  "s_add_i32 s63, s68, 8192\n" \
  "s_add_i32 s64, s68, 12288\n" \
  "s_add_i32 s65, s68, 16384\n" \
  "s_add_i32 s66, s68, 20480\n" \
  "s_add_i32 s67, s68, 24576\n" \
  "s_add_i32 s68, s68, 458752\n" \
  "buffer_load_dwordx4 v[144:147], v0, %0, s61 offen offset:0 sc1\n" \
  "buffer_load_dwordx4 v[148:151], v0, %0, s61 offen offset:1024 sc1\n" \
  "buffer_load_dwordx4 v[152:155], v0, %0, s61 offen offset:2048 sc1\n" \
  "buffer_load_dwordx4 v[156:159], v0, %0, s61 offen offset:3072 sc1\n" \
  "buffer_load_dwordx4 v[160:163], v0, %0, s62 offen offset:0 sc1\n" \
  "buffer_load_dwordx4 v[164:167], v0, %0, s62 offen offset:1024 sc1\n" \
  "buffer_load_dwordx4 v[168:171], v0, %0, s62 offen offset:2048 sc1\n" \
  "buffer_load_dwordx4 v[172:175], v0, %0, s62 offen offset:3072 sc1\n" \
  "buffer_load_dwordx4 v[176:179], v0, %0, s63 offen offset:0 sc1\n" \
  "buffer_load_dwordx4 v[180:183], v0, %0, s63 offen offset:1024 sc1\n" \
  "buffer_load_dwordx4 v[184:187], v0, %0, s63 offen offset:2048 sc1\n" \
  "buffer_load_dwordx4 v[188:191], v0, %0, s63 offen offset:3072 sc1\n" \
  "buffer_load_dwordx4 v[192:195], v0, %0, s64 offen offset:0 sc1\n" \
  "buffer_load_dwordx4 v[196:199], v0, %0, s64 offen offset:1024 sc1\n" \
  "buffer_load_dwordx4 v[200:203], v0, %0, s64 offen offset:2048 sc1\n" \
  "buffer_load_dwordx4 v[204:207], v0, %0, s64 offen offset:3072 sc1\n" \
  "buffer_load_dwordx4 v[208:211], v0, %0, s65 offen offset:0 sc1\n" \
  "buffer_load_dwordx4 v[212:215], v0, %0, s65 offen offset:1024 sc1\n" \
  "buffer_load_dwordx4 v[216:219], v0, %0, s65 offen offset:2048 sc1\n" \
  "buffer_load_dwordx4 v[220:223], v0, %0, s65 offen offset:3072 sc1\n" \
  "buffer_load_dwordx4 v[224:227], v0, %0, s66 offen offset:0 sc1\n" \
  "buffer_load_dwordx4 v[228:231], v0, %0, s66 offen offset:1024 sc1\n" \
  "buffer_load_dwordx4 v[232:235], v0, %0, s66 offen offset:2048 sc1\n" \
  "buffer_load_dwordx4 v[236:239], v0, %0, s66 offen offset:3072 sc1\n" \
  "buffer_load_dwordx4 v[240:243], v0, %0, s67 offen offset:0 sc1\n" \
  "buffer_load_dwordx4 v[244:247], v0, %0, s67 offen offset:1024 sc1\n" \
  "buffer_load_dwordx4 v[248:251], v0, %0, s67 offen offset:2048 sc1\n" \
  "buffer_load_dwordx4 v[252:255], v0, %0, s67 offen offset:3072 sc1\n" \
  "s_add_i32 s43, %6, 50176\n" \
  "v_add_u32 v2, s43, v119\n" \
  "v_add_u32 v123, v2, v120\n" \
  "s_waitcnt vmcnt(0)\n" \
  "v_mul_u32_u24 v4, 114688, v7\n" \
  "v_add_u32 v4, 0, v4\n" \
  "v_add_u32 v6, %4, v4\n" \
  "v_lshrrev_b32 v6, 21, v6\n" \
  "v_lshrrev_b32 v6, v6, %5\n" \
  "v_and_b32 v6, 1, v6\n" \
  "v_lshl_add_u32 v4, v6, 13, v4\n" \
  "v_add_u32 v4, %3, v4\n" \
  "v_add_u32 v4, 0, v4\n" \
  "v_mul_u32_u24 v120, 114688, v7\n" \
  "v_add_u32 v120, 32768, v120\n" \
  "v_add_u32 v6, %4, v120\n" \
  "v_lshrrev_b32 v6, 21, v6\n" \
  "v_lshrrev_b32 v6, v6, %5\n" \
  "v_and_b32 v6, 1, v6\n" \
  "v_lshl_add_u32 v120, v6, 13, v120\n" \
  "v_add_u32 v120, %3, v120\n" \
  "v_add_u32 v120, 0, v120\n" \
  "v_mul_u32_u24 v131, 114688, v7\n" \
  "v_add_u32 v131, 98304, v131\n" \
  "v_add_u32 v6, %4, v131\n" \
  "v_lshrrev_b32 v6, 21, v6\n" \
  "v_lshrrev_b32 v6, v6, %5\n" \
  "v_and_b32 v6, 1, v6\n" \
  "v_lshl_add_u32 v131, v6, 13, v131\n" \
  "v_add_u32 v131, %3, v131\n" \
  "v_add_u32 v131, 0, v131\n" \
  "v_mul_u32_u24 v132, 114688, v7\n" \
  "v_add_u32 v132, 16384, v132\n" \
  "v_add_u32 v6, %4, v132\n" \
  "v_lshrrev_b32 v6, 21, v6\n" \
  "v_lshrrev_b32 v6, v6, %5\n" \
  "v_and_b32 v6, 1, v6\n" \
  "v_lshl_add_u32 v132, v6, 13, v132\n" \
  "v_add_u32 v132, %3, v132\n" \
  "v_add_u32 v132, 0, v132\n" \
  "v_mov_b32 v7, v132\n" \
  "loop_%=:\n" \
  "s_sub_i32 s44, s40, 1\n" \
  "s_cmp_lt_u32 s44, 62\n" \
  "s_cselect_b32 s43, 0, 458752\n" \
  "s_sub_i32 s68, s68, s43\n" \
  "s_add_i32 s61, s68, 0\n" \
  "s_add_i32 s62, s68, 4096\n" \
  "s_add_i32 s63, s68, 8192\n" \
  "s_add_i32 s64, s68, 12288\n" \
  "s_add_i32 s65, s68, 16384\n" \
  "s_add_i32 s66, s68, 20480\n" \
  "s_add_i32 s67, s68, 24576\n" \
  "s_add_i32 s68, s68, 458752\n" \
  "v_readlane_b32 s56, v4, s40\n" \
  "v_readlane_b32 s57, v7, s40\n" \
  "v_readlane_b32 s58, v120, s40\n" \
  "v_readlane_b32 s59, v131, s40\n" \
  "v_mov_b32 v64, 0\n" \
  "v_mov_b32 v65, 0\n" \
  "v_mov_b32 v66, 0\n" \
  "v_mov_b32 v67, 0\n" \
  "v_mov_b32 v68, 0\n" \
  "v_mov_b32 v69, 0\n" \
  "v_mov_b32 v70, 0\n" \
  "v_mov_b32 v71, 0\n" \
  "v_mov_b32 v72, 0\n" \
  "v_mov_b32 v73, 0\n" \
  "v_mov_b32 v74, 0\n" \
  "v_mov_b32 v75, 0\n" \
  "s_waitcnt vmcnt(6)\n" \
  "s_barrier\n" \
  "ds_read_b128 v[8:11], v2 offset:0\n" \
  "ds_read_b128 v[12:15], v2 offset:512\n" \
  "ds_read_b128 v[16:19], v2 offset:1024\n" \
  "ds_read_b128 v[20:23], v2 offset:1536\n" \
  "ds_read_b128 v[24:27], v2 offset:2048\n" \
  "ds_read_b128 v[28:31], v2 offset:2560\n" \
  "s_waitcnt lgkmcnt(5)\n" \
  "v_smfmac_f32_16x16x64_bf16 v[72:75], v[8:11], v[144:151], v1\n" \
  "ds_read_b128 v[32:35], v2 offset:3072\n" \
  "s_waitcnt lgkmcnt(5)\n" \
  "v_smfmac_f32_16x16x64_bf16 v[72:75], v[12:15], v[152:159], v1\n" \
  "ds_read_b128 v[36:39], v2 offset:3584\n" \
  "buffer_load_dwordx4 v[144:147], v0, %0, s61 offen offset:0 sc1\n" \
  "s_waitcnt lgkmcnt(5)\n" \
  "v_smfmac_f32_16x16x64_bf16 v[72:75], v[16:19], v[160:167], v1\n" \
  "ds_read_b128 v[40:43], v2 offset:4096\n" \
  "buffer_load_dwordx4 v[148:151], v0, %0, s61 offen offset:1024 sc1\n" \
  "s_waitcnt lgkmcnt(5)\n" \
  "v_smfmac_f32_16x16x64_bf16 v[72:75], v[20:23], v[168:175], v1\n" \
  "ds_read_b128 v[44:47], v2 offset:4608\n" \
  "buffer_load_dwordx4 v[152:155], v0, %0, s61 offen offset:2048 sc1\n" \
  "s_waitcnt lgkmcnt(5)\n" \
  "v_smfmac_f32_16x16x64_bf16 v[72:75], v[24:27], v[176:183], v1\n" \
  "ds_read_b128 v[48:51], v2 offset:5120\n" \
  "buffer_load_dwordx4 v[156:159], v0, %0, s61 offen offset:3072 sc1\n" \
  "s_waitcnt lgkmcnt(5)\n" \
  "v_smfmac_f32_16x16x64_bf16 v[72:75], v[28:31], v[184:191], v1\n" \
  "ds_read_b128 v[52:55], v2 offset:5632\n" \
  "buffer_load_dwordx4 v[160:163], v0, %0, s62 offen offset:0 sc1\n" \
  "s_waitcnt lgkmcnt(5)\n" \
  "v_smfmac_f32_16x16x64_bf16 v[72:75], v[32:35], v[192:199], v1\n" \
  "ds_read_b128 v[56:59], v2 offset:6144\n" \
  "buffer_load_dwordx4 v[164:167], v0, %0, s62 offen offset:1024 sc1\n" \
  "s_waitcnt lgkmcnt(5)\n" \
  "v_smfmac_f32_16x16x64_bf16 v[72:75], v[36:39], v[200:207], v1\n" \
  "ds_read_b128 v[60:63], v2 offset:6656\n" \
  "buffer_load_dwordx4 v[168:171], v0, %0, s62 offen offset:2048 sc1\n" \
  "s_waitcnt lgkmcnt(5)\n" \
  "v_smfmac_f32_16x16x64_bf16 v[72:75], v[40:43], v[208:215], v1\n" \
  "buffer_load_dwordx4 v[172:175], v0, %0, s62 offen offset:3072 sc1\n" \
  "s_waitcnt lgkmcnt(4)\n" \
  "v_smfmac_f32_16x16x64_bf16 v[72:75], v[44:47], v[216:223], v1\n" \
  "buffer_load_dwordx4 v[176:179], v0, %0, s63 offen offset:0 sc1\n" \
  "s_waitcnt lgkmcnt(3)\n" \
  "v_smfmac_f32_16x16x64_bf16 v[72:75], v[48:51], v[224:231], v1\n" \
  "buffer_load_dwordx4 v[180:183], v0, %0, s63 offen offset:1024 sc1\n" \
  "s_waitcnt lgkmcnt(2)\n" \
  "v_smfmac_f32_16x16x64_bf16 v[72:75], v[52:55], v[232:239], v1\n" \
  "buffer_load_dwordx4 v[184:187], v0, %0, s63 offen offset:2048 sc1\n" \
  "s_waitcnt lgkmcnt(1)\n" \
  "v_smfmac_f32_16x16x64_bf16 v[72:75], v[56:59], v[240:247], v1\n" \
  "buffer_load_dwordx4 v[188:191], v0, %0, s63 offen offset:3072 sc1\n" \
  "s_waitcnt lgkmcnt(0)\n" \
  "v_smfmac_f32_16x16x64_bf16 v[72:75], v[60:63], v[248:255], v1\n" \
  "buffer_load_dwordx4 v[192:195], v0, %0, s64 offen offset:0 sc1\n" \
  "v_smfmac_f32_16x16x64_bf16 v[64:67], v[8:11], a[0:7], v1\n" \
  "buffer_load_dwordx4 v[196:199], v0, %0, s64 offen offset:1024 sc1\n" \
  "v_smfmac_f32_16x16x64_bf16 v[64:67], v[12:15], a[8:15], v1\n" \
  "buffer_load_dwordx4 v[200:203], v0, %0, s64 offen offset:2048 sc1\n" \
  "v_smfmac_f32_16x16x64_bf16 v[64:67], v[16:19], a[16:23], v1\n" \
  "buffer_load_dwordx4 v[204:207], v0, %0, s64 offen offset:3072 sc1\n" \
  "v_smfmac_f32_16x16x64_bf16 v[64:67], v[20:23], a[24:31], v1\n" \
  "buffer_load_dwordx4 v[208:211], v0, %0, s65 offen offset:0 sc1\n" \
  "ds_write_b128 v76, v[72:75]\n" \
  "v_smfmac_f32_16x16x64_bf16 v[64:67], v[24:27], a[32:39], v1\n" \
  "buffer_load_dwordx4 v[212:215], v0, %0, s65 offen offset:1024 sc1\n" \
  "ds_read_b32 v112, v77 offset:0\n" \
  "v_smfmac_f32_16x16x64_bf16 v[68:71], v[8:11], a[112:119], v1\n" \
  "buffer_load_dwordx4 v[216:219], v0, %0, s65 offen offset:2048 sc1\n" \
  "ds_read_b32 v113, v77 offset:32\n" \
  "v_smfmac_f32_16x16x64_bf16 v[64:67], v[28:31], a[40:47], v1\n" \
  "buffer_load_dwordx4 v[220:223], v0, %0, s65 offen offset:3072 sc1\n" \
  "ds_read_b32 v114, v77 offset:64\n" \
  "v_smfmac_f32_16x16x64_bf16 v[68:71], v[12:15], a[120:127], v1\n" \
  "buffer_load_dwordx4 v[224:227], v0, %0, s66 offen offset:0 sc1\n" \
  "ds_read_b32 v115, v77 offset:96\n" \
  "v_smfmac_f32_16x16x64_bf16 v[64:67], v[32:35], a[48:55], v1\n" \
  "buffer_load_dwordx4 v[228:231], v0, %0, s66 offen offset:1024 sc1\n" \
  "ds_read_b128 v[84:87], v123 offset:0\n" \
  "v_smfmac_f32_16x16x64_bf16 v[68:71], v[16:19], a[128:135], v1\n" \
  "buffer_load_dwordx4 v[232:235], v0, %0, s66 offen offset:2048 sc1\n" \
  "ds_read_b128 v[88:91], v123 offset:1024\n" \
  "v_smfmac_f32_16x16x64_bf16 v[64:67], v[36:39], a[56:63], v1\n" \
  "buffer_load_dwordx4 v[236:239], v0, %0, s66 offen offset:3072 sc1\n" \
  "ds_read_b128 v[92:95], v123 offset:2048\n" \
  "v_smfmac_f32_16x16x64_bf16 v[68:71], v[20:23], a[136:143], v1\n" \
  "buffer_load_dwordx4 v[240:243], v0, %0, s67 offen offset:0 sc1\n" \
  "ds_read_b128 v[96:99], v123 offset:3072\n" \
  "v_smfmac_f32_16x16x64_bf16 v[64:67], v[40:43], a[64:71], v1\n" \
  "buffer_load_dwordx4 v[244:247], v0, %0, s67 offen offset:1024 sc1\n" \
  "ds_read_b128 v[100:103], v123 offset:4096\n" \
  "s_waitcnt lgkmcnt(5)\n" \
  "v_max3_f32 v121, v112, v113, v114\n" \
  "v_smfmac_f32_16x16x64_bf16 v[68:71], v[24:27], a[144:151], v1\n" \
  "buffer_load_dwordx4 v[248:251], v0, %0, s67 offen offset:2048 sc1\n" \
  "ds_read_b128 v[104:107], v123 offset:5120\n" \
  "v_max_f32 v121, v121, v115\n" \
  "v_smfmac_f32_16x16x64_bf16 v[64:67], v[44:47], a[72:79], v1\n" \
  "buffer_load_dwordx4 v[252:255], v0, %0, s67 offen offset:3072 sc1\n" \
  "ds_read_b128 v[108:111], v123 offset:6144\n" \
  "s_nop 0\n" \
  "v_smfmac_f32_16x16x64_bf16 v[68:71], v[28:31], a[152:159], v1\n" \
  "v_max_f32_dpp v121, v121, v121 row_ror:8 row_mask:0xf bank_mask:0xf\n" \
  "v_smfmac_f32_16x16x64_bf16 v[64:67], v[48:51], a[80:87], v1\n" \
  "v_mov_b32 v124, v121\n" \
  "v_smfmac_f32_16x16x64_bf16 v[68:71], v[32:35], a[160:167], v1\n" \
  "s_nop 0\n" \
  "s_barrier\n" \
  "v_smfmac_f32_16x16x64_bf16 v[64:67], v[52:55], a[88:95], v1\n" \
  "v_permlane16_swap_b32 v121, v124\n" \
  "v_smfmac_f32_16x16x64_bf16 v[68:71], v[36:39], a[168:175], v1\n" \
  "v_max_f32 v121, v121, v124\n" \
  "v_smfmac_f32_16x16x64_bf16 v[64:67], v[56:59], a[96:103], v1\n" \
  "v_mov_b32 v124, v121\n" \
  "v_smfmac_f32_16x16x64_bf16 v[68:71], v[40:43], a[176:183], v1\n" \
  "s_nop 0\n" \
  "v_smfmac_f32_16x16x64_bf16 v[64:67], v[60:63], a[104:111], v1\n" \
  "v_permlane32_swap_b32 v121, v124\n" \
  "v_smfmac_f32_16x16x64_bf16 v[68:71], v[44:47], a[184:191], v1\n" \
  "v_max_f32 v121, v121, v124\n" \
  "v_smfmac_f32_16x16x64_bf16 v[68:71], v[48:51], a[192:199], v1\n" \
  "v_smfmac_f32_16x16x64_bf16 v[68:71], v[52:55], a[200:207], v1\n" \
  "v_cvt_pk_bf16_f32 v64, v64, v65\n" \
  "v_smfmac_f32_16x16x64_bf16 v[68:71], v[56:59], a[208:215], v1\n" \
  "v_cvt_pk_bf16_f32 v65, v66, v67\n" \
  "v_smfmac_f32_16x16x64_bf16 v[68:71], v[60:63], a[216:223], v1\n" \
  "ds_write_b64 v3, v[64:65] offset:0\n" \
  "ds_read_b64_tr_b16 v[132:133], v5 offset:1024\n" \
  "ds_read_b64_tr_b16 v[134:135], v78 offset:1024\n" \
  "ds_read_b64_tr_b16 v[136:137], v5 offset:3072\n" \
  "ds_read_b64_tr_b16 v[138:139], v78 offset:3072\n" \
  "ds_read_b64_tr_b16 v[140:141], v5 offset:5120\n" \
  "ds_read_b64_tr_b16 v[142:143], v78 offset:5120\n" \
  "v_mul_f32 v80, -0.5, v121\n" \
  "v_mul_f32 v81, -0.5, v121\n" \
  "v_mul_f32 v82, -0.5, v121\n" \
  "v_mul_f32 v83, -0.5, v121\n" \
  "v_cvt_pk_bf16_f32 v68, v68, v69\n" \
  "v_cvt_pk_bf16_f32 v69, v70, v71\n" \
  "ds_write_b64 v118, v[68:69] offset:512\n" \
  "ds_read_b64_tr_b16 v[8:9], v5 offset:0\n" \
  "ds_read_b64_tr_b16 v[10:11], v78 offset:0\n" \
  "s_waitcnt lgkmcnt(7)\n" \
  "v_mfma_f32_16x16x32_bf16 v[80:83], v[132:135], v[88:91], v[80:83]\n" \
  "v_sub_f32 v112, v112, v121\n" \
  "v_sub_f32 v113, v113, v121\n" \
  "s_waitcnt lgkmcnt(5)\n" \
  "v_mfma_f32_16x16x32_bf16 v[80:83], v[136:139], v[96:99], v[80:83]\n" \
  "v_sub_f32 v114, v114, v121\n" \
  "v_sub_f32 v115, v115, v121\n" \
  "s_waitcnt lgkmcnt(3)\n" \
  "v_mfma_f32_16x16x32_bf16 v[80:83], v[140:143], v[104:107], v[80:83]\n" \
  "s_waitcnt lgkmcnt(0)\n" \
  "v_mfma_f32_16x16x32_bf16 v[80:83], v[8:11], v[84:87], v[80:83]\n" \
  "s_mov_b64 vcc, s[54:55]\n" \
  "s_cmp_eq_u32 s40, 63\n" \
  "s_barrier\n" \
  "s_cbranch_scc1 end_%=\n" \
  "ds_read_b64_tr_b16 v[12:13], v5 offset:2048\n" \
  "ds_read_b64_tr_b16 v[14:15], v78 offset:2048\n" \
  "ds_read_b64_tr_b16 v[16:17], v5 offset:4096\n" \
  "ds_read_b64_tr_b16 v[18:19], v78 offset:4096\n" \
  "ds_read_b64_tr_b16 v[20:21], v5 offset:6144\n" \
  "ds_read_b64_tr_b16 v[22:23], v78 offset:6144\n" \
  "v_exp_f32 v112, v112\n" \
  "v_exp_f32 v113, v113\n" \
  "v_exp_f32 v114, v114\n" \
  "v_exp_f32 v115, v115\n" \
  "s_waitcnt lgkmcnt(4)\n" \
  "v_mfma_f32_16x16x32_bf16 v[80:83], v[12:15], v[92:95], v[80:83]\n" \
  "v_add_f32 v124, v112, v113\n" \
  "v_add_f32 v122, v114, v115\n" \
  "s_waitcnt lgkmcnt(2)\n" \
  "v_mfma_f32_16x16x32_bf16 v[80:83], v[16:19], v[100:103], v[80:83]\n" \
  "v_add_f32 v122, v122, v124\n" \
  "s_waitcnt lgkmcnt(0)\n" \
  "v_mfma_f32_16x16x32_bf16 v[80:83], v[20:23], v[108:111], v[80:83]\n" \
  "v_add_f32_dpp v122, v122, v122 row_ror:8 row_mask:0xf bank_mask:0xf\n" \
  "s_nop 6\n" \
  "v_cndmask_b32_dpp v124, v80, v80, vcc row_ror:8 row_mask:0xf bank_mask:0xf\n" \
  "v_cndmask_b32_dpp v125, v81, v81, vcc row_ror:8 row_mask:0xf bank_mask:0xf\n" \
  "v_cndmask_b32_dpp v126, v82, v82, vcc row_ror:8 row_mask:0xf bank_mask:0xf\n" \
  "v_cndmask_b32_dpp v127, v83, v83, vcc row_ror:8 row_mask:0xf bank_mask:0xf\n" \
  "v_permlane32_swap_b32 v124, v125\n" \
  "v_add_f32 v124, v124, v125\n" \
  "v_permlane32_swap_b32 v126, v127\n" \
  "v_exp_f32 v128, v124\n" \
  "v_add_f32 v126, v126, v127\n" \
  "v_exp_f32 v129, v126\n" \
  "v_max_f32 v130, v124, v126\n" \
  "fin_%=:\n" \
  "v_add_f32 v125, v122, v128\n" \
  "v_add_f32 v125, v125, v129\n" \
  "v_mov_b32 v127, v125\n" \
  "v_cmp_lt_f32 vcc, 0x42200000, v130\n" \
  "s_cbranch_vccnz slow_%=\n" \
  "v_permlane16_swap_b32 v125, v127\n" \
  "v_add_f32 v125, v125, v127\n" \
  "v_mov_b32 v127, v125\n" \
  "s_nop 1\n" \
  "v_permlane32_swap_b32 v125, v127\n" \
  "v_add_f32 v122, v125, v127\n" \
  "v_rcp_f32 v122, v122\n" \
  "s_nop 0\n" \
  "v_mul_f32 v112, v112, v122\n" \
  "v_mul_f32 v113, v113, v122\n" \
  "v_mul_f32 v114, v114, v122\n" \
  "v_mul_f32 v115, v115, v122\n" \
  "v_cvt_pk_bf16_f32 v124, v112, v113\n" \
  "v_cvt_pk_bf16_f32 v125, v114, v115\n" \
  "v_mul_f32 v128, v128, v122\n" \
  "buffer_store_dwordx2 v[124:125], v116, %0, s56 offen sc1\n" \
  "v_mul_f32 v129, v129, v122\n" \
  "v_cvt_pk_bf16_f32 v126, v128, v129\n" \
  "s_mov_b64 exec, s[52:53]\n" \
  "buffer_store_dword v126, v117, %0, s59 offen sc1\n" \
  "buffer_store_dword v126, v117, %0, s59 offen offset:2048 sc1\n" \
  "s_mov_b64 exec, -1\n" \
  "buffer_store_dwordx2 v[124:125], v116, %0, s57 offen sc1\n" \
  "s_add_i32 s59, s59, 4096\n" \
  "buffer_store_dwordx2 v[124:125], v116, %0, s58 offen sc1\n" \
  "s_mov_b64 exec, s[52:53]\n" \
  "buffer_store_dword v126, v117, %0, s59 offen sc1\n" \
  "s_mov_b64 exec, -1\n" \
  "s_add_i32 s40, s40, 1\n" \
  "s_cmp_eq_u32 s40, 1\n" \
  "s_cbranch_scc0 loop_%=\n" \
  "fg1_%=:\n" \
  "buffer_load_dword v6, v79, %0, s47 offen sc1\n" \
  "s_waitcnt vmcnt(0)\n" \
  "v_cmp_ge_u32 vcc, 1, v6\n" \
  "s_cbranch_vccz fg1ok_%=\n" \
  "s_sleep 3\n" \
  "s_sub_u32 s41, s41, 1\n" \
  "s_cbranch_scc1 tmo_%=\n" \
  "s_branch fg1_%=\n" \
  "fg1ok_%=:\n" \
  "s_add_i32 s61, s68, 0\n" \
  "s_add_i32 s62, s68, 4096\n" \
  "s_add_i32 s63, s68, 8192\n" \
  "s_add_i32 s64, s68, 12288\n" \
  "s_add_i32 s65, s68, 16384\n" \
  "s_add_i32 s66, s68, 20480\n" \
  "s_add_i32 s67, s68, 24576\n" \
  "s_add_i32 s68, s68, 458752\n" \
  "buffer_load_dwordx4 v[144:147], v0, %0, s61 offen offset:0 sc1\n" \
  "buffer_load_dwordx4 v[148:151], v0, %0, s61 offen offset:1024 sc1\n" \
  "buffer_load_dwordx4 v[152:155], v0, %0, s61 offen offset:2048 sc1\n" \
  "buffer_load_dwordx4 v[156:159], v0, %0, s61 offen offset:3072 sc1\n" \
  "buffer_load_dwordx4 v[160:163], v0, %0, s62 offen offset:0 sc1\n" \
  "buffer_load_dwordx4 v[164:167], v0, %0, s62 offen offset:1024 sc1\n" \
  "buffer_load_dwordx4 v[168:171], v0, %0, s62 offen offset:2048 sc1\n" \
  "buffer_load_dwordx4 v[172:175], v0, %0, s62 offen offset:3072 sc1\n" \
  "buffer_load_dwordx4 v[176:179], v0, %0, s63 offen offset:0 sc1\n" \
  "buffer_load_dwordx4 v[180:183], v0, %0, s63 offen offset:1024 sc1\n" \
  "buffer_load_dwordx4 v[184:187], v0, %0, s63 offen offset:2048 sc1\n" \
  "buffer_load_dwordx4 v[188:191], v0, %0, s63 offen offset:3072 sc1\n" \
  "buffer_load_dwordx4 v[192:195], v0, %0, s64 offen offset:0 sc1\n" \
  "buffer_load_dwordx4 v[196:199], v0, %0, s64 offen offset:1024 sc1\n" \
  "buffer_load_dwordx4 v[200:203], v0, %0, s64 offen offset:2048 sc1\n" \
  "buffer_load_dwordx4 v[204:207], v0, %0, s64 offen offset:3072 sc1\n" \
  "buffer_load_dwordx4 v[208:211], v0, %0, s65 offen offset:0 sc1\n" \
  "buffer_load_dwordx4 v[212:215], v0, %0, s65 offen offset:1024 sc1\n" \
  "buffer_load_dwordx4 v[216:219], v0, %0, s65 offen offset:2048 sc1\n" \
  "buffer_load_dwordx4 v[220:223], v0, %0, s65 offen offset:3072 sc1\n" \
  "buffer_load_dwordx4 v[224:227], v0, %0, s66 offen offset:0 sc1\n" \
  "buffer_load_dwordx4 v[228:231], v0, %0, s66 offen offset:1024 sc1\n" \
  "buffer_load_dwordx4 v[232:235], v0, %0, s66 offen offset:2048 sc1\n" \
  "buffer_load_dwordx4 v[236:239], v0, %0, s66 offen offset:3072 sc1\n" \
  "buffer_load_dwordx4 v[240:243], v0, %0, s67 offen offset:0 sc1\n" \
  "buffer_load_dwordx4 v[244:247], v0, %0, s67 offen offset:1024 sc1\n" \
  "buffer_load_dwordx4 v[248:251], v0, %0, s67 offen offset:2048 sc1\n" \
  "buffer_load_dwordx4 v[252:255], v0, %0, s67 offen offset:3072 sc1\n" \
  "s_waitcnt vmcnt(0)\n" \
  "s_branch loop_%=\n" \
  "slow_%=:\n" \
  "v_mov_b32 v127, v130\n" \
  "s_nop 1\n" \
  "v_permlane16_swap_b32 v130, v127\n" \
  "v_max_f32 v130, v130, v127\n" \
  "v_mov_b32 v127, v130\n" \
  "s_nop 1\n" \
  "v_permlane32_swap_b32 v130, v127\n" \
  "v_max3_f32 v130, v130, v127, 0\n" \
  "v_sub_f32 v124, v124, v130\n" \
  "v_sub_f32 v126, v126, v130\n" \
  "v_exp_f32 v127, -v130\n" \
  "v_exp_f32 v128, v124\n" \
  "v_exp_f32 v129, v126\n" \
  "v_mov_b32 v130, 0\n" \
  "v_mul_f32 v112, v112, v127\n" \
  "v_mul_f32 v113, v113, v127\n" \
  "v_mul_f32 v114, v114, v127\n" \
  "v_mul_f32 v115, v115, v127\n" \
  "v_mul_f32 v122, v122, v127\n" \
  "s_branch fin_%=\n" \
  "s_branch end_%=\n" \
  "tmo_%=:\n" \
  "s_mov_b64 exec, 1\n" \
  "v_mov_b32 v7, 1\n" \
  "buffer_store_dword v7, off, %0, s47 sc1\n" \
  "s_mov_b64 exec, -1\n" \
  "end_%=:\n" \
  "s_waitcnt vmcnt(0) lgkmcnt(0)\n"

#define ASM_SCB \
  "v_mbcnt_lo_u32_b32 v7, -1, 0\n" \
  "v_mbcnt_hi_u32_b32 v7, -1, v7\n" \
  "v_lshlrev_b32 v0, 4, v7\n" \
  "v_and_b32 v6, 8, v7\n" \
  "v_mov_b32 v1, 0x4444\n" \
  "v_mov_b32 v2, 0xeeee\n" \
  "v_cmp_ne_u32 vcc, 0, v6\n" \
  "s_nop 1\n" \
  "v_cndmask_b32 v1, v1, v2, vcc\n" \
  "s_mov_b32 s40, 0\n" \
  "s_mov_b32 s41, 262144\n" \
  "s_mov_b32 s42, %7\n" \
  "s_mov_b32 s47, 104857600\n" \
  "s_mov_b32 s46, 0\n" \
  "s_sub_i32 s45, %2, 1\n" \
  "s_lshl_b32 s66, s45, 2\n" \
  "s_add_i32 s66, s66, 2\n" \
  "s_mul_i32 s67, s45, 14336\n" \
  "s_add_i32 s67, s67, %6\n" \
  "v_add_u32 v4, s67, v0\n" \
  "s_mul_i32 s68, %1, 14\n" \
  "s_add_i32 s68, s68, s66\n" \
  "s_mul_i32 s68, s68, 28672\n" \
  "s_add_i32 s69, s68, 100352\n" \
  "s_add_i32 m0, s67, 0\n" \
  "s_add_i32 s44, s69, 0\n" \
  "buffer_load_dwordx4 v0, %0, s44 offen offset:0 sc1 lds\n" \
  "buffer_load_dwordx4 v0, %0, s44 offen offset:1024 sc1 lds\n" \
  "buffer_load_dwordx4 v0, %0, s44 offen offset:2048 sc1 lds\n" \
  "buffer_load_dwordx4 v0, %0, s44 offen offset:3072 sc1 lds\n" \
  "s_add_i32 m0, s67, 4096\n" \
  "s_add_i32 s44, s69, 4096\n" \
  "buffer_load_dwordx4 v0, %0, s44 offen offset:0 sc1 lds\n" \
  "buffer_load_dwordx4 v0, %0, s44 offen offset:1024 sc1 lds\n" \
  "buffer_load_dwordx4 v0, %0, s44 offen offset:2048 sc1 lds\n" \
  "buffer_load_dwordx4 v0, %0, s44 offen offset:3072 sc1 lds\n" \
  "s_add_i32 m0, s67, 8192\n" \
  "s_add_i32 s44, s69, 8192\n" \
  "buffer_load_dwordx4 v0, %0, s44 offen offset:0 sc1 lds\n" \
  "buffer_load_dwordx4 v0, %0, s44 offen offset:1024 sc1 lds\n" \
  "buffer_load_dwordx4 v0, %0, s44 offen offset:2048 sc1 lds\n" \
  "buffer_load_dwordx4 v0, %0, s44 offen offset:3072 sc1 lds\n" \
  "s_add_i32 m0, s67, 12288\n" \
  "s_add_i32 s44, s69, 12288\n" \
  "buffer_load_dwordx4 v0, %0, s44 offen offset:0 sc1 lds\n" \
  "buffer_load_dwordx4 v0, %0, s44 offen offset:1024 sc1 lds\n" \
  "buffer_load_dwordx4 a[0:3], v0, %0, s68 offen offset:0 sc1\n" \
  "buffer_load_dwordx4 a[4:7], v0, %0, s68 offen offset:1024 sc1\n" \
  "buffer_load_dwordx4 a[8:11], v0, %0, s68 offen offset:2048 sc1\n" \
  "buffer_load_dwordx4 a[12:15], v0, %0, s68 offen offset:3072 sc1\n" \
  "s_add_i32 s68, s68, 4096\n" \
  "buffer_load_dwordx4 a[16:19], v0, %0, s68 offen offset:0 sc1\n" \
  "buffer_load_dwordx4 a[20:23], v0, %0, s68 offen offset:1024 sc1\n" \
  "buffer_load_dwordx4 a[24:27], v0, %0, s68 offen offset:2048 sc1\n" \
  "buffer_load_dwordx4 a[28:31], v0, %0, s68 offen offset:3072 sc1\n" \
  "s_add_i32 s68, s68, 4096\n" \
  "buffer_load_dwordx4 a[32:35], v0, %0, s68 offen offset:0 sc1\n" \
  "buffer_load_dwordx4 a[36:39], v0, %0, s68 offen offset:1024 sc1\n" \
  "buffer_load_dwordx4 a[40:43], v0, %0, s68 offen offset:2048 sc1\n" \
  "buffer_load_dwordx4 a[44:47], v0, %0, s68 offen offset:3072 sc1\n" \
  "s_add_i32 s68, s68, 4096\n" \
  "buffer_load_dwordx4 a[48:51], v0, %0, s68 offen offset:0 sc1\n" \
  "buffer_load_dwordx4 a[52:55], v0, %0, s68 offen offset:1024 sc1\n" \
  "buffer_load_dwordx4 a[56:59], v0, %0, s68 offen offset:2048 sc1\n" \
  "buffer_load_dwordx4 a[60:63], v0, %0, s68 offen offset:3072 sc1\n" \
  "s_add_i32 s68, s68, 4096\n" \
  "buffer_load_dwordx4 a[64:67], v0, %0, s68 offen offset:0 sc1\n" \
  "buffer_load_dwordx4 a[68:71], v0, %0, s68 offen offset:1024 sc1\n" \
  "buffer_load_dwordx4 a[72:75], v0, %0, s68 offen offset:2048 sc1\n" \
  "buffer_load_dwordx4 a[76:79], v0, %0, s68 offen offset:3072 sc1\n" \
  "s_add_i32 s68, s68, 4096\n" \
  "buffer_load_dwordx4 a[80:83], v0, %0, s68 offen offset:0 sc1\n" \
  "buffer_load_dwordx4 a[84:87], v0, %0, s68 offen offset:1024 sc1\n" \
  "buffer_load_dwordx4 a[88:91], v0, %0, s68 offen offset:2048 sc1\n" \
  "buffer_load_dwordx4 a[92:95], v0, %0, s68 offen offset:3072 sc1\n" \
  "s_add_i32 s68, s68, 4096\n" \
  "buffer_load_dwordx4 a[96:99], v0, %0, s68 offen offset:0 sc1\n" \
  "buffer_load_dwordx4 a[100:103], v0, %0, s68 offen offset:1024 sc1\n" \
  "buffer_load_dwordx4 a[104:107], v0, %0, s68 offen offset:2048 sc1\n" \
  "buffer_load_dwordx4 a[108:111], v0, %0, s68 offen offset:3072 sc1\n" \
  "s_add_i32 s68, s68, 4096\n" \
  "buffer_load_dwordx4 a[112:115], v0, %0, s68 offen offset:0 sc1\n" \
  "buffer_load_dwordx4 a[116:119], v0, %0, s68 offen offset:1024 sc1\n" \
  "buffer_load_dwordx4 a[120:123], v0, %0, s68 offen offset:2048 sc1\n" \
  "buffer_load_dwordx4 a[124:127], v0, %0, s68 offen offset:3072 sc1\n" \
  "s_add_i32 s68, s68, 4096\n" \
  "buffer_load_dwordx4 a[128:131], v0, %0, s68 offen offset:0 sc1\n" \
  "buffer_load_dwordx4 a[132:135], v0, %0, s68 offen offset:1024 sc1\n" \
  "buffer_load_dwordx4 a[136:139], v0, %0, s68 offen offset:2048 sc1\n" \
  "buffer_load_dwordx4 a[140:143], v0, %0, s68 offen offset:3072 sc1\n" \
  "s_add_i32 s68, s68, 4096\n" \
  "buffer_load_dwordx4 a[144:147], v0, %0, s68 offen offset:0 sc1\n" \
  "buffer_load_dwordx4 a[148:151], v0, %0, s68 offen offset:1024 sc1\n" \
  "buffer_load_dwordx4 a[152:155], v0, %0, s68 offen offset:2048 sc1\n" \
  "buffer_load_dwordx4 a[156:159], v0, %0, s68 offen offset:3072 sc1\n" \
  "s_add_i32 s68, s68, 4096\n" \
  "buffer_load_dwordx4 a[160:163], v0, %0, s68 offen offset:0 sc1\n" \
  "buffer_load_dwordx4 a[164:167], v0, %0, s68 offen offset:1024 sc1\n" \
  "buffer_load_dwordx4 a[168:171], v0, %0, s68 offen offset:2048 sc1\n" \
  "buffer_load_dwordx4 a[172:175], v0, %0, s68 offen offset:3072 sc1\n" \
  "s_add_i32 s68, s68, 4096\n" \
  "buffer_load_dwordx4 a[176:179], v0, %0, s68 offen offset:0 sc1\n" \
  "buffer_load_dwordx4 a[180:183], v0, %0, s68 offen offset:1024 sc1\n" \
  "buffer_load_dwordx4 a[184:187], v0, %0, s68 offen offset:2048 sc1\n" \
  "buffer_load_dwordx4 a[188:191], v0, %0, s68 offen offset:3072 sc1\n" \
  "s_add_i32 s68, s68, 4096\n" \
  "buffer_load_dwordx4 a[192:195], v0, %0, s68 offen offset:0 sc1\n" \
  "buffer_load_dwordx4 a[196:199], v0, %0, s68 offen offset:1024 sc1\n" \
  "buffer_load_dwordx4 a[200:203], v0, %0, s68 offen offset:2048 sc1\n" \
  "buffer_load_dwordx4 a[204:207], v0, %0, s68 offen offset:3072 sc1\n" \
  "s_add_i32 s68, s68, 4096\n" \
  "buffer_load_dwordx4 a[208:211], v0, %0, s68 offen offset:0 sc1\n" \
  "buffer_load_dwordx4 a[212:215], v0, %0, s68 offen offset:1024 sc1\n" \
  "buffer_load_dwordx4 a[216:219], v0, %0, s68 offen offset:2048 sc1\n" \
  "buffer_load_dwordx4 a[220:223], v0, %0, s68 offen offset:3072 sc1\n" \
  "s_add_i32 s68, s68, 4096\n" \
  "buffer_load_dwordx4 a[224:227], v0, %0, s68 offen offset:0 sc1\n" \
  "buffer_load_dwordx4 a[228:231], v0, %0, s68 offen offset:1024 sc1\n" \
  "buffer_load_dwordx4 a[232:235], v0, %0, s68 offen offset:2048 sc1\n" \
  "buffer_load_dwordx4 a[236:239], v0, %0, s68 offen offset:3072 sc1\n" \
  "s_add_i32 s68, s68, 4096\n" \
  "buffer_load_dwordx4 a[240:243], v0, %0, s68 offen offset:0 sc1\n" \
  "buffer_load_dwordx4 a[244:247], v0, %0, s68 offen offset:1024 sc1\n" \
  "buffer_load_dwordx4 a[248:251], v0, %0, s68 offen offset:2048 sc1\n" \
  "buffer_load_dwordx4 a[252:255], v0, %0, s68 offen offset:3072 sc1\n" \
  "s_add_i32 s68, s68, 4096\n" \
  "buffer_load_dwordx4 v[120:123], v0, %0, s68 offen offset:0 sc1\n" \
  "buffer_load_dwordx4 v[124:127], v0, %0, s68 offen offset:1024 sc1\n" \
  "buffer_load_dwordx4 v[128:131], v0, %0, s68 offen offset:2048 sc1\n" \
  "buffer_load_dwordx4 v[132:135], v0, %0, s68 offen offset:3072 sc1\n" \
  "s_add_i32 s68, s68, 4096\n" \
  "buffer_load_dwordx4 v[136:139], v0, %0, s68 offen offset:0 sc1\n" \
  "buffer_load_dwordx4 v[140:143], v0, %0, s68 offen offset:1024 sc1\n" \
  "buffer_load_dwordx4 v[144:147], v0, %0, s68 offen offset:2048 sc1\n" \
  "buffer_load_dwordx4 v[148:151], v0, %0, s68 offen offset:3072 sc1\n" \
  "s_add_i32 s68, s68, 4096\n" \
  "buffer_load_dwordx4 v[152:155], v0, %0, s68 offen offset:0 sc1\n" \
  "buffer_load_dwordx4 v[156:159], v0, %0, s68 offen offset:1024 sc1\n" \
  "buffer_load_dwordx4 v[160:163], v0, %0, s68 offen offset:2048 sc1\n" \
  "buffer_load_dwordx4 v[164:167], v0, %0, s68 offen offset:3072 sc1\n" \
  "s_add_i32 s68, s68, 4096\n" \
  "buffer_load_dwordx4 v[168:171], v0, %0, s68 offen offset:0 sc1\n" \
  "buffer_load_dwordx4 v[172:175], v0, %0, s68 offen offset:1024 sc1\n" \
  "buffer_load_dwordx4 v[176:179], v0, %0, s68 offen offset:2048 sc1\n" \
  "buffer_load_dwordx4 v[180:183], v0, %0, s68 offen offset:3072 sc1\n" \
  "s_add_i32 s68, s68, 4096\n" \
  "buffer_load_dwordx4 v[184:187], v0, %0, s68 offen offset:0 sc1\n" \
  "buffer_load_dwordx4 v[188:191], v0, %0, s68 offen offset:1024 sc1\n" \
  "buffer_load_dwordx4 v[192:195], v0, %0, s68 offen offset:2048 sc1\n" \
  "buffer_load_dwordx4 v[196:199], v0, %0, s68 offen offset:3072 sc1\n" \
  "s_add_i32 s68, s68, 4096\n" \
  "buffer_load_dwordx4 v[200:203], v0, %0, s68 offen offset:0 sc1\n" \
  "buffer_load_dwordx4 v[204:207], v0, %0, s68 offen offset:1024 sc1\n" \
  "buffer_load_dwordx4 v[208:211], v0, %0, s68 offen offset:2048 sc1\n" \
  "buffer_load_dwordx4 v[212:215], v0, %0, s68 offen offset:3072 sc1\n" \
  "s_add_i32 s68, s68, 4096\n" \
  "buffer_load_dwordx4 v[216:219], v0, %0, s68 offen offset:0 sc1\n" \
  "buffer_load_dwordx4 v[220:223], v0, %0, s68 offen offset:1024 sc1\n" \
  "buffer_load_dwordx4 v[224:227], v0, %0, s68 offen offset:2048 sc1\n" \
  "buffer_load_dwordx4 v[228:231], v0, %0, s68 offen offset:3072 sc1\n" \
  "s_add_i32 s68, s68, 4096\n" \
  "buffer_load_dwordx4 v[232:235], v0, %0, s68 offen offset:0 sc1\n" \
  "buffer_load_dwordx4 v[236:239], v0, %0, s68 offen offset:1024 sc1\n" \
  "buffer_load_dwordx4 v[240:243], v0, %0, s68 offen offset:2048 sc1\n" \
  "buffer_load_dwordx4 v[244:247], v0, %0, s68 offen offset:3072 sc1\n" \
  "s_add_i32 s68, s68, 4096\n" \
  "buffer_load_dwordx4 v[248:251], v0, %0, s68 offen offset:0 sc1\n" \
  "buffer_load_dwordx4 v[252:255], v0, %0, s68 offen offset:1024 sc1\n" \
  "s_lshl_b32 s43, s66, 9\n" \
  "s_add_i32 s43, s43, %6\n" \
  "s_add_i32 s43, s43, 43008\n" \
  "v_lshlrev_b32 v3, 3, v7\n" \
  "v_xor_b32 v112, 32, v3\n" \
  "v_add_u32 v3, s43, v3\n" \
  "v_add_u32 v112, s43, v112\n" \
  "v_lshrrev_b32 v6, 2, v7\n" \
  "v_lshlrev_b32 v5, 3, v6\n" \
  "v_bfe_u32 v6, v7, 1, 1\n" \
  "v_lshlrev_b32 v115, 5, v6\n" \
  "v_xor_b32 v5, v5, v115\n" \
  "v_lshl_add_u32 v5, v6, 9, v5\n" \
  "v_and_b32 v6, 1, v7\n" \
  "v_lshl_add_u32 v5, v6, 7, v5\n" \
  "v_add_u32 v5, %6, v5\n" \
  "v_add_u32 v5, 43008, v5\n" \
  "v_lshrrev_b32 v6, 4, v7\n" \
  "v_lshlrev_b32 v113, 7, v6\n" \
  "v_and_b32 v6, 7, v7\n" \
  "v_lshl_add_u32 v113, v6, 4, v113\n" \
  "v_min_u32 v114, 6, v7\n" \
  "s_mul_i32 s43, %1, 7\n" \
  "v_add_u32 v114, s43, v114\n" \
  "v_lshlrev_b32 v114, 7, v114\n" \
  "v_add_u32 v114, 12288, v114\n" \
  "v_min_u32 v115, 55, v7\n" \
  "s_mul_i32 s43, 0, 0\n" \
  "v_add_u32 v115, s43, v115\n" \
  "v_lshlrev_b32 v115, 7, v115\n" \
  "v_add_u32 v115, 4096, v115\n" \
  "v_mov_b32 v117, 0\n" \
  "s_waitcnt vmcnt(0)\n" \
  "s_cmp_lt_u32 %2, 2\n" \
  "s_cbranch_scc1 fiok_%=\n" \
  "fi_%=:\n" \
  "buffer_load_dword v6, v115, %0, s47 offen sc1\n" \
  "s_waitcnt vmcnt(0)\n" \
  "v_cmp_ge_u32 vcc, 0, v6\n" \
  "s_cbranch_vccz fiok_%=\n" \
  "s_sleep 3\n" \
  "s_sub_u32 s41, s41, 1\n" \
  "s_cbranch_scc1 tmo_%=\n" \
  "s_branch fi_%=\n" \
  "fiok_%=:\n" \
  "s_add_i32 s43, %6, 50176\n" \
  "v_add_u32 v2, s43, v113\n" \
  "s_sub_i32 s65, %2, 2\n" \
  "s_lshl_b32 s64, s65, 12\n" \
  "s_add_i32 s43, s43, s64\n" \
  "v_add_u32 v116, s43, v0\n" \
  "s_lshl_b32 s65, s65, 10\n" \
  "s_add_i32 s63, s43, 8192\n" \
  "v_min_u32 v118, 31, v7\n" \
  "v_lshlrev_b32 v118, 6, v118\n" \
  "s_mul_i32 s43, s40, 114688\n" \
  "s_add_i32 s43, s43, 98304\n" \
  "s_add_i32 s44, s43, %4\n" \
  "s_lshr_b32 s44, s44, 21\n" \
  "s_lshr_b32 s44, %5, s44\n" \
  "s_and_b32 s44, s44, 1\n" \
  "s_lshl_b32 s44, s44, 13\n" \
  "s_add_i32 s98, s43, s44\n" \
  "s_add_i32 s98, s98, %3\n" \
  "s_add_i32 s98, s98, 6144\n" \
  "s_cmp_eq_u32 %2, 1\n" \
  "s_cbranch_scc1 notab_%=\n" \
  "v_mul_u32_u24 v5, 114688, v7\n" \
  "v_add_u32 v5, 49152, v5\n" \
  "v_add_u32 v6, %4, v5\n" \
  "v_lshrrev_b32 v6, 21, v6\n" \
  "v_lshrrev_b32 v6, v6, %5\n" \
  "v_and_b32 v6, 1, v6\n" \
  "v_lshl_add_u32 v5, v6, 13, v5\n" \
  "v_add_u32 v5, %3, v5\n" \
  "v_add_u32 v5, s64, v5\n" \
  "v_mul_u32_u24 v113, 114688, v7\n" \
  "v_add_u32 v113, 65536, v113\n" \
  "v_add_u32 v6, %4, v113\n" \
  "v_lshrrev_b32 v6, 21, v6\n" \
  "v_lshrrev_b32 v6, v6, %5\n" \
  "v_and_b32 v6, 1, v6\n" \
  "v_lshl_add_u32 v113, v6, 13, v113\n" \
  "v_add_u32 v113, %3, v113\n" \
  "v_add_u32 v113, s64, v113\n" \
  "v_mul_u32_u24 v115, 114688, v7\n" \
  "v_add_u32 v115, 81920, v115\n" \
  "v_add_u32 v6, %4, v115\n" \
  "v_lshrrev_b32 v6, 21, v6\n" \
  "v_lshrrev_b32 v6, v6, %5\n" \
  "v_and_b32 v6, 1, v6\n" \
  "v_lshl_add_u32 v115, v6, 13, v115\n" \
  "v_add_u32 v115, %3, v115\n" \
  "v_add_u32 v115, s64, v115\n" \
  "v_mul_u32_u24 v119, 114688, v7\n" \
  "v_add_u32 v119, 98304, v119\n" \
  "v_add_u32 v6, %4, v119\n" \
  "v_lshrrev_b32 v6, 21, v6\n" \
  "v_lshrrev_b32 v6, v6, %5\n" \
  "v_and_b32 v6, 1, v6\n" \
  "v_lshl_add_u32 v119, v6, 13, v119\n" \
  "v_add_u32 v119, %3, v119\n" \
  "v_add_u32 v119, 0x1800, v119\n" \
  "v_mov_b32 v7, v119\n" \
  "notab_%=:\n" \
  "s_cmp_eq_u32 %2, 1\n" \
  "s_cselect_b32 s68, 0, 3\n" \
  "s_lshl_b32 s43, %1, 1\n" \
  "s_add_i32 s43, s43, %2\n" \
  "s_sub_i32 s43, s43, 2\n" \
  "s_lshl_b32 s69, s43, 6\n" \
  "s_mov_b32 s70, 104858624\n" \
  "s_add_i32 s43, s98, s69\n" \
  "s_cmp_eq_u32 %2, 1\n" \
  "s_cselect_b32 s70, s70, s43\n" \
  "s_waitcnt lgkmcnt(0)\n" \
  "s_memtime s[50:51]\n" \
  "s_memrealtime s[48:49]\n" \
  "s_nop 1\n" \
  "s_memrealtime s[72:73]\n" \
  "s_nop 1\n" \
  "s_memrealtime s[74:75]\n" \
  "s_nop 1\n" \
  "s_memrealtime s[90:91]\n" \
  "s_nop 1\n" \
  "s_memrealtime s[92:93]\n" \
  "s_nop 1\n" \
  "s_memrealtime s[94:95]\n" \
  "s_nop 1\n" \
  "s_memrealtime s[100:101]\n" \
  "s_nop 1\n" \
  "s_memrealtime s[54:55]\n" \
  "s_nop 1\n" \
  "s_waitcnt lgkmcnt(0)\n" \
  "s_add_u32 s48, s48, s72\n" \
  "s_add_u32 s48, s48, s74\n" \
  "s_add_u32 s48, s48, s90\n" \
  "s_add_u32 s48, s48, s92\n" \
  "s_add_u32 s48, s48, s94\n" \
  "s_add_u32 s48, s48, s100\n" \
  "s_add_u32 s48, s48, s54\n" \
  "s_mul_i32 s48, s48, 3\n" \
  "s_sub_u32 s99, s48, s50\n" \
  "loop_%=:\n" \
  "ds_read_b128 v[80:83], v4 offset:0\n" \
  "ds_read_b128 v[84:87], v4 offset:1024\n" \
  "ds_read_b128 v[88:91], v4 offset:2048\n" \
  "ds_read_b128 v[92:95], v4 offset:3072\n" \
  "ds_read_b128 v[96:99], v4 offset:4096\n" \
  "ds_read_b128 v[100:103], v4 offset:5120\n" \
  "ds_read_b128 v[104:107], v4 offset:6144\n" \
  "ds_read_b128 v[108:111], v4 offset:7168\n" \
  "v_mov_b32 v64, 0\n" \
  "v_mov_b32 v65, 0\n" \
  "v_mov_b32 v66, 0\n" \
  "v_mov_b32 v67, 0\n" \
  "v_mov_b32 v68, 0\n" \
  "v_mov_b32 v69, 0\n" \
  "v_mov_b32 v70, 0\n" \
  "v_mov_b32 v71, 0\n" \
  "v_mov_b32 v72, 0\n" \
  "v_mov_b32 v73, 0\n" \
  "v_mov_b32 v74, 0\n" \
  "v_mov_b32 v75, 0\n" \
  "v_mov_b32 v76, 0\n" \
  "v_mov_b32 v77, 0\n" \
  "v_mov_b32 v78, 0\n" \
  "v_mov_b32 v79, 0\n" \
  "s_mov_b32 s46, 0\n" \
  "s_cmp_eq_u32 %2, 1\n" \
  "s_cbranch_scc1 b1_%=\n" \
  "v_readlane_b32 s56, v5, s40\n" \
  "s_sub_i32 s59, s56, s65\n" \
  "v_readlane_b32 s57, v113, s40\n" \
  "s_sub_i32 s60, s57, s65\n" \
  "v_readlane_b32 s58, v115, s40\n" \
  "s_sub_i32 s61, s58, s65\n" \
  "s_waitcnt lgkmcnt(0)\n" \
  "s_memtime s[50:51]\n" \
  "s_memrealtime s[48:49]\n" \
  "s_nop 1\n" \
  "s_memrealtime s[72:73]\n" \
  "s_nop 1\n" \
  "s_memrealtime s[74:75]\n" \
  "s_nop 1\n" \
  "s_memrealtime s[90:91]\n" \
  "s_nop 1\n" \
  "s_memrealtime s[92:93]\n" \
  "s_nop 1\n" \
  "s_memrealtime s[94:95]\n" \
  "s_nop 1\n" \
  "s_memrealtime s[100:101]\n" \
  "s_nop 1\n" \
  "s_memrealtime s[54:55]\n" \
  "s_nop 1\n" \
  "s_waitcnt lgkmcnt(0)\n" \
  "s_add_u32 s48, s48, s72\n" \
  "s_add_u32 s48, s48, s74\n" \
  "s_add_u32 s48, s48, s90\n" \
  "s_add_u32 s48, s48, s92\n" \
  "s_add_u32 s48, s48, s94\n" \
  "s_add_u32 s48, s48, s100\n" \
  "s_add_u32 s48, s48, s54\n" \
  "s_mul_i32 s48, s48, 3\n" \
  "s_sub_u32 s99, s48, s50\n" \
  "s_cmp_eq_u32 s40, 0\n" \
  "s_cbranch_scc1 go_%=\n" \
  "s_waitcnt vmcnt(1)\n" \
  "bell_%=:\n" \
  "v_cmp_eq_u32 vcc, -1, v119\n" \
  "s_cbranch_vccz bellok_%=\n" \
  "s_sleep 1\n" \
  "s_sub_u32 s41, s41, 1\n" \
  "s_cbranch_scc1 tmo_%=\n" \
  "buffer_load_dword v119, v118, %0, s98 offen sc1\n" \
  "s_waitcnt vmcnt(0)\n" \
  "s_branch bell_%=\n" \
  "bellok_%=:\n" \
  "s_add_u32 s43, s50, s99\n" \
  "v_subrev_u32 v119, s43, v119\n" \
  "s_nop 1\n" \
  "v_max_i32_dpp v119, v119, v119 row_ror:8 row_mask:0xf bank_mask:0xf\n" \
  "s_nop 1\n" \
  "v_max_i32_dpp v119, v119, v119 row_ror:4 row_mask:0xf bank_mask:0xf\n" \
  "s_nop 1\n" \
  "v_max_i32_dpp v119, v119, v119 row_ror:2 row_mask:0xf bank_mask:0xf\n" \
  "s_nop 1\n" \
  "v_max_i32_dpp v119, v119, v119 row_ror:1 row_mask:0xf bank_mask:0xf\n" \
  "s_nop 0\n" \
  "v_readlane_b32 s43, v119, 0\n" \
  "v_readlane_b32 s44, v119, 16\n" \
  "s_max_i32 s43, s43, s44\n" \
  "s_min_i32 s43, s43, 0x2000\n" \
  "s_add_i32 s43, s43, s42\n" \
  "s_add_u32 s48, s43, s50\n" \
  "v_readlane_b32 s98, v7, s40\n" \
  "s_min_u32 s45, s40, 62\n" \
  "s_add_i32 s45, s45, 1\n" \
  "s_add_i32 s70, s98, s69\n" \
  "s_waitcnt vmcnt(0)\n" \
  "v_cmp_ge_u32 vcc, s45, v117\n" \
  "s_cbranch_vccz fgok_%=\n" \
  "fg_%=:\n" \
  "buffer_load_dword v117, v114, %0, s47 offen sc1\n" \
  "s_waitcnt vmcnt(0)\n" \
  "v_cmp_ge_u32 vcc, s45, v117\n" \
  "s_cbranch_vccz fgok_%=\n" \
  "s_sleep 3\n" \
  "s_sub_u32 s41, s41, 1\n" \
  "s_cbranch_scc1 tmo_%=\n" \
  "s_branch fg_%=\n" \
  "fgok_%=:\n" \
  "bellsp_%=:\n" \
  "s_memtime s[50:51]\n" \
  "s_waitcnt lgkmcnt(0)\n" \
  "s_sub_i32 s43, s48, s50\n" \
  "s_cmp_gt_i32 s43, 0x70\n" \
  "s_cbranch_scc1 bellsp_%=\n" \
  "s_max_i32 s43, s43, 0\n" \
  "bellcd_%=:\n" \
  "s_sub_u32 s43, s43, 16\n" \
  "s_cbranch_scc0 bellcd_%=\n" \
  "go_%=:\n" \
  "buffer_load_dwordx4 v[8:11], v0, %0, s56 offen offset:0 sc1\n" \
  "buffer_load_dwordx4 v[12:15], v0, %0, s56 offen offset:1024 sc1\n" \
  "buffer_load_dwordx4 v[16:19], v0, %0, s56 offen offset:2048 sc1\n" \
  "buffer_load_dwordx4 v[20:23], v0, %0, s59 offen offset:3072 sc1\n" \
  "s_sleep 3\n" \
  "s_add_i32 m0, s63, 0\n" \
  "s_nop 0\n" \
  "buffer_load_dwordx4 v0, %0, s57 offen offset:0 sc1 lds\n" \
  "buffer_load_dwordx4 v0, %0, s57 offen offset:1024 sc1 lds\n" \
  "buffer_load_dwordx4 v0, %0, s57 offen offset:2048 sc1 lds\n" \
  "buffer_load_dwordx4 v0, %0, s60 offen offset:3072 sc1 lds\n" \
  "s_waitcnt vmcnt(7)\n" \
  "ds_write_b128 v116, v[8:11] offset:0\n" \
  "v_max3_u32 v6, v8, v9, 0\n" \
  "v_max3_u32 v6, v6, v10, v11\n" \
  "s_waitcnt vmcnt(6)\n" \
  "ds_write_b128 v116, v[12:15] offset:1024\n" \
  "v_max3_u32 v6, v12, v13, v6\n" \
  "v_max3_u32 v6, v6, v14, v15\n" \
  "s_waitcnt vmcnt(5)\n" \
  "ds_write_b128 v116, v[16:19] offset:2048\n" \
  "v_max3_u32 v6, v16, v17, v6\n" \
  "v_max3_u32 v6, v6, v18, v19\n" \
  "s_waitcnt vmcnt(4)\n" \
  "ds_write_b128 v116, v[20:23] offset:3072\n" \
  "s_memtime s[96:97]\n" \
  "v_max3_u32 v6, v20, v21, v6\n" \
  "v_max3_u32 v6, v6, v22, v23\n" \
  "v_cmp_eq_u32 vcc, -1, v6\n" \
  "s_cbranch_vccz b1_%=\n" \
  "s_add_i32 s46, s46, 1\n" \
  "s_add_i32 m0, s63, 8192\n" \
  "s_nop 0\n" \
  "buffer_load_dwordx4 v0, %0, s58 offen offset:0 sc1 lds\n" \
  "buffer_load_dwordx4 v0, %0, s58 offen offset:1024 sc1 lds\n" \
  "buffer_load_dwordx4 v0, %0, s58 offen offset:2048 sc1 lds\n" \
  "buffer_load_dwordx4 v0, %0, s61 offen offset:3072 sc1 lds\n" \
  "s_waitcnt vmcnt(4) lgkmcnt(0)\n" \
  "ds_read_b128 v[8:11], v116 offset:8192\n" \
  "ds_read_b128 v[12:15], v116 offset:9216\n" \
  "ds_read_b128 v[16:19], v116 offset:10240\n" \
  "ds_read_b128 v[20:23], v116 offset:11264\n" \
  "s_waitcnt lgkmcnt(3)\n" \
  "ds_write_b128 v116, v[8:11] offset:0\n" \
  "v_max3_u32 v6, v8, v9, 0\n" \
  "v_max3_u32 v6, v6, v10, v11\n" \
  "s_waitcnt lgkmcnt(3)\n" \
  "ds_write_b128 v116, v[12:15] offset:1024\n" \
  "v_max3_u32 v6, v12, v13, v6\n" \
  "v_max3_u32 v6, v6, v14, v15\n" \
  "s_waitcnt lgkmcnt(3)\n" \
  "ds_write_b128 v116, v[16:19] offset:2048\n" \
  "v_max3_u32 v6, v16, v17, v6\n" \
  "v_max3_u32 v6, v6, v18, v19\n" \
  "s_waitcnt lgkmcnt(3)\n" \
  "ds_write_b128 v116, v[20:23] offset:3072\n" \
  "s_memtime s[96:97]\n" \
  "v_max3_u32 v6, v20, v21, v6\n" \
  "v_max3_u32 v6, v6, v22, v23\n" \
  "v_cmp_eq_u32 vcc, -1, v6\n" \
  "s_cbranch_vccz b1_%=\n" \
  "s_add_i32 s46, s46, 1\n" \
  "s_waitcnt vmcnt(0) lgkmcnt(0)\n" \
  "ds_read_b128 v[8:11], v116 offset:16384\n" \
  "ds_read_b128 v[12:15], v116 offset:17408\n" \
  "ds_read_b128 v[16:19], v116 offset:18432\n" \
  "ds_read_b128 v[20:23], v116 offset:19456\n" \
  "s_waitcnt lgkmcnt(3)\n" \
  "ds_write_b128 v116, v[8:11] offset:0\n" \
  "v_max3_u32 v6, v8, v9, 0\n" \
  "v_max3_u32 v6, v6, v10, v11\n" \
  "s_waitcnt lgkmcnt(3)\n" \
  "ds_write_b128 v116, v[12:15] offset:1024\n" \
  "v_max3_u32 v6, v12, v13, v6\n" \
  "v_max3_u32 v6, v6, v14, v15\n" \
  "s_waitcnt lgkmcnt(3)\n" \
  "ds_write_b128 v116, v[16:19] offset:2048\n" \
  "v_max3_u32 v6, v16, v17, v6\n" \
  "v_max3_u32 v6, v6, v18, v19\n" \
  "s_waitcnt lgkmcnt(3)\n" \
  "ds_write_b128 v116, v[20:23] offset:3072\n" \
  "s_memtime s[96:97]\n" \
  "v_max3_u32 v6, v20, v21, v6\n" \
  "v_max3_u32 v6, v6, v22, v23\n" \
  "v_cmp_eq_u32 vcc, -1, v6\n" \
  "s_cbranch_vccz b1_%=\n" \
  "s_add_i32 s46, s46, 1\n" \
  "poll_%=:\n" \
  "s_sub_u32 s41, s41, 1\n" \
  "s_cbranch_scc1 tmo_%=\n" \
  "s_waitcnt lgkmcnt(0)\n" \
  "buffer_load_dwordx4 v[8:11], v0, %0, s58 offen offset:0 sc1\n" \
  "buffer_load_dwordx4 v[12:15], v0, %0, s58 offen offset:1024 sc1\n" \
  "buffer_load_dwordx4 v[16:19], v0, %0, s58 offen offset:2048 sc1\n" \
  "buffer_load_dwordx4 v[20:23], v0, %0, s61 offen offset:3072 sc1\n" \
  "s_waitcnt vmcnt(3)\n" \
  "ds_write_b128 v116, v[8:11] offset:0\n" \
  "v_max3_u32 v6, v8, v9, 0\n" \
  "v_max3_u32 v6, v6, v10, v11\n" \
  "s_waitcnt vmcnt(2)\n" \
  "ds_write_b128 v116, v[12:15] offset:1024\n" \
  "v_max3_u32 v6, v12, v13, v6\n" \
  "v_max3_u32 v6, v6, v14, v15\n" \
  "s_waitcnt vmcnt(1)\n" \
  "ds_write_b128 v116, v[16:19] offset:2048\n" \
  "v_max3_u32 v6, v16, v17, v6\n" \
  "v_max3_u32 v6, v6, v18, v19\n" \
  "s_waitcnt vmcnt(0)\n" \
  "ds_write_b128 v116, v[20:23] offset:3072\n" \
  "s_memtime s[96:97]\n" \
  "v_max3_u32 v6, v20, v21, v6\n" \
  "v_max3_u32 v6, v6, v22, v23\n" \
  "v_cmp_eq_u32 vcc, -1, v6\n" \
  "s_cbranch_vccz b1_%=\n" \
  "s_add_i32 s46, s46, 1\n" \
  "s_branch poll_%=\n" \
  "b1_%=:\n" \
  "s_waitcnt lgkmcnt(0)\n" \
  "s_barrier\n" \
  "ds_read_b128 v[8:11], v2 offset:0\n" \
  "ds_read_b128 v[12:15], v2 offset:512\n" \
  "ds_read_b128 v[16:19], v2 offset:1024\n" \
  "s_cmp_lt_u32 s40, s68\n" \
  "s_cbranch_scc0 stamped_%=\n" \
  "s_memtime s[96:97]\n" \
  "s_waitcnt lgkmcnt(0)\n" \
  "s_sub_i32 s96, s96, 60\n" \
  "stamped_%=:\n" \
  "s_add_i32 s96, s96, 1500\n" \
  "s_add_i32 s43, s96, s99\n" \
  "s_cmp_eq_u32 s43, -1\n" \
  "s_cselect_b32 s43, 0, s43\n" \
  "v_mov_b32 v6, s43\n" \
  "buffer_store_dword v6, off, %0, s70 sc1\n" \
  "s_waitcnt lgkmcnt(2)\n" \
  "v_smfmac_f32_16x16x64_bf16 v[64:67], v[8:11], a[0:7], v1\n" \
  "ds_read_b128 v[20:23], v2 offset:1536\n" \
  "v_smfmac_f32_16x16x64_bf16 v[68:71], v[8:11], a[112:119], v1\n" \
  "s_waitcnt lgkmcnt(2)\n" \
  "v_smfmac_f32_16x16x64_bf16 v[64:67], v[12:15], a[8:15], v1\n" \
  "ds_read_b128 v[24:27], v2 offset:2048\n" \
  "v_smfmac_f32_16x16x64_bf16 v[68:71], v[12:15], a[120:127], v1\n" \
  "s_waitcnt lgkmcnt(2)\n" \
  "v_smfmac_f32_16x16x64_bf16 v[64:67], v[16:19], a[16:23], v1\n" \
  "ds_read_b128 v[28:31], v2 offset:2560\n" \
  "v_smfmac_f32_16x16x64_bf16 v[68:71], v[16:19], a[128:135], v1\n" \
  "s_waitcnt lgkmcnt(2)\n" \
  "v_smfmac_f32_16x16x64_bf16 v[64:67], v[20:23], a[24:31], v1\n" \
  "ds_read_b128 v[32:35], v2 offset:3072\n" \
  "v_smfmac_f32_16x16x64_bf16 v[68:71], v[20:23], a[136:143], v1\n" \
  "s_waitcnt lgkmcnt(2)\n" \
  "v_smfmac_f32_16x16x64_bf16 v[64:67], v[24:27], a[32:39], v1\n" \
  "ds_read_b128 v[36:39], v2 offset:3584\n" \
  "v_smfmac_f32_16x16x64_bf16 v[68:71], v[24:27], a[144:151], v1\n" \
  "s_waitcnt lgkmcnt(2)\n" \
  "v_smfmac_f32_16x16x64_bf16 v[64:67], v[28:31], a[40:47], v1\n" \
  "ds_read_b128 v[40:43], v2 offset:4096\n" \
  "v_smfmac_f32_16x16x64_bf16 v[68:71], v[28:31], a[152:159], v1\n" \
  "s_waitcnt lgkmcnt(2)\n" \
  "v_smfmac_f32_16x16x64_bf16 v[64:67], v[32:35], a[48:55], v1\n" \
  "ds_read_b128 v[44:47], v2 offset:4608\n" \
  "v_smfmac_f32_16x16x64_bf16 v[68:71], v[32:35], a[160:167], v1\n" \
  "s_waitcnt lgkmcnt(2)\n" \
  "v_smfmac_f32_16x16x64_bf16 v[64:67], v[36:39], a[56:63], v1\n" \
  "ds_read_b128 v[48:51], v2 offset:5120\n" \
  "v_smfmac_f32_16x16x64_bf16 v[68:71], v[36:39], a[168:175], v1\n" \
  "s_waitcnt lgkmcnt(2)\n" \
  "v_smfmac_f32_16x16x64_bf16 v[64:67], v[40:43], a[64:71], v1\n" \
  "ds_read_b128 v[52:55], v2 offset:5632\n" \
  "v_smfmac_f32_16x16x64_bf16 v[68:71], v[40:43], a[176:183], v1\n" \
  "s_waitcnt lgkmcnt(2)\n" \
  "v_smfmac_f32_16x16x64_bf16 v[64:67], v[44:47], a[72:79], v1\n" \
  "ds_read_b128 v[56:59], v2 offset:6144\n" \
  "v_smfmac_f32_16x16x64_bf16 v[68:71], v[44:47], a[184:191], v1\n" \
  "s_waitcnt lgkmcnt(2)\n" \
  "v_smfmac_f32_16x16x64_bf16 v[64:67], v[48:51], a[80:87], v1\n" \
  "ds_read_b128 v[60:63], v2 offset:6656\n" \
  "v_smfmac_f32_16x16x64_bf16 v[68:71], v[48:51], a[192:199], v1\n" \
  "s_waitcnt lgkmcnt(2)\n" \
  "v_smfmac_f32_16x16x64_bf16 v[64:67], v[52:55], a[88:95], v1\n" \
  "v_smfmac_f32_16x16x64_bf16 v[68:71], v[52:55], a[200:207], v1\n" \
  "s_waitcnt lgkmcnt(1)\n" \
  "v_smfmac_f32_16x16x64_bf16 v[64:67], v[56:59], a[96:103], v1\n" \
  "v_smfmac_f32_16x16x64_bf16 v[68:71], v[56:59], a[208:215], v1\n" \
  "s_waitcnt lgkmcnt(0)\n" \
  "v_smfmac_f32_16x16x64_bf16 v[64:67], v[60:63], a[104:111], v1\n" \
  "v_smfmac_f32_16x16x64_bf16 v[68:71], v[60:63], a[216:223], v1\n" \
  "v_smfmac_f32_16x16x64_bf16 v[76:79], v[8:11], v[200:207], v1\n" \
  "v_smfmac_f32_16x16x64_bf16 v[76:79], v[12:15], v[208:215], v1\n" \
  "v_smfmac_f32_16x16x64_bf16 v[76:79], v[16:19], v[216:223], v1\n" \
  "v_cvt_pk_bf16_f32 v64, v64, v65\n" \
  "v_smfmac_f32_16x16x64_bf16 v[76:79], v[20:23], v[224:231], v1\n" \
  "v_cvt_pk_bf16_f32 v65, v66, v67\n" \
  "v_smfmac_f32_16x16x64_bf16 v[76:79], v[24:27], v[232:239], v1\n" \
  "ds_write_b64 v3, v[64:65] offset:0\n" \
  "v_cvt_pk_bf16_f32 v68, v68, v69\n" \
  "v_smfmac_f32_16x16x64_bf16 v[72:75], v[8:11], a[224:231], v1\n" \
  "v_cvt_pk_bf16_f32 v69, v70, v71\n" \
  "v_smfmac_f32_16x16x64_bf16 v[76:79], v[28:31], v[240:247], v1\n" \
  "ds_write_b64 v112, v[68:69] offset:512\n" \
  "v_smfmac_f32_16x16x64_bf16 v[72:75], v[12:15], a[232:239], v1\n" \
  "v_smfmac_f32_16x16x64_bf16 v[76:79], v[32:35], v[248:255], v1\n" \
  "v_smfmac_f32_16x16x64_bf16 v[72:75], v[16:19], a[240:247], v1\n" \
  "s_waitcnt lgkmcnt(0)\n" \
  "s_barrier\n" \
  "v_smfmac_f32_16x16x64_bf16 v[76:79], v[36:39], v[80:87], v1\n" \
  "ds_read_b128 v[80:83], v4 offset:8192\n" \
  "v_smfmac_f32_16x16x64_bf16 v[72:75], v[20:23], a[248:255], v1\n" \
  "ds_read_b128 v[84:87], v4 offset:9216\n" \
  "v_smfmac_f32_16x16x64_bf16 v[76:79], v[40:43], v[88:95], v1\n" \
  "ds_read_b128 v[88:91], v4 offset:10240\n" \
  "v_smfmac_f32_16x16x64_bf16 v[72:75], v[24:27], v[120:127], v1\n" \
  "ds_read_b128 v[92:95], v4 offset:11264\n" \
  "v_smfmac_f32_16x16x64_bf16 v[76:79], v[44:47], v[96:103], v1\n" \
  "ds_read_b128 v[96:99], v4 offset:12288\n" \
  "v_smfmac_f32_16x16x64_bf16 v[72:75], v[28:31], v[128:135], v1\n" \
  "ds_read_b128 v[100:103], v4 offset:13312\n" \
  "v_smfmac_f32_16x16x64_bf16 v[76:79], v[48:51], v[104:111], v1\n" \
  "v_smfmac_f32_16x16x64_bf16 v[72:75], v[32:35], v[136:143], v1\n" \
  "s_waitcnt lgkmcnt(4)\n" \
  "v_smfmac_f32_16x16x64_bf16 v[76:79], v[52:55], v[80:87], v1\n" \
  "v_smfmac_f32_16x16x64_bf16 v[72:75], v[36:39], v[144:151], v1\n" \
  "s_waitcnt lgkmcnt(2)\n" \
  "v_smfmac_f32_16x16x64_bf16 v[76:79], v[56:59], v[88:95], v1\n" \
  "v_smfmac_f32_16x16x64_bf16 v[72:75], v[40:43], v[152:159], v1\n" \
  "s_waitcnt lgkmcnt(0)\n" \
  "v_smfmac_f32_16x16x64_bf16 v[76:79], v[60:63], v[96:103], v1\n" \
  "v_smfmac_f32_16x16x64_bf16 v[72:75], v[44:47], v[160:167], v1\n" \
  "v_smfmac_f32_16x16x64_bf16 v[72:75], v[48:51], v[168:175], v1\n" \
  "v_smfmac_f32_16x16x64_bf16 v[72:75], v[52:55], v[176:183], v1\n" \
  "v_cvt_pk_bf16_f32 v76, v76, v77\n" \
  "v_smfmac_f32_16x16x64_bf16 v[72:75], v[56:59], v[184:191], v1\n" \
  "v_cvt_pk_bf16_f32 v77, v78, v79\n" \
  "v_smfmac_f32_16x16x64_bf16 v[72:75], v[60:63], v[192:199], v1\n" \
  "ds_write_b64 v112, v[76:77] offset:1536\n" \
  "s_nop 6\n" \
  "v_cvt_pk_bf16_f32 v72, v72, v73\n" \
  "v_cvt_pk_bf16_f32 v73, v74, v75\n" \
  "ds_write_b64 v3, v[72:73] offset:1024\n" \
  "s_waitcnt lgkmcnt(0)\n" \
  "s_barrier\n" \
  "s_cmp_eq_u32 %2, 1\n" \
  "s_cbranch_scc1 pub_%=\n" \
  "buffer_load_dword v119, v118, %0, s98 offen sc1\n" \
  "buffer_load_dword v117, v114, %0, s47 offen sc1\n" \
  "s_sleep 5\n" \
  "s_branch nopub_%=\n" \
  "pub_%=:\n" \
  "s_cmp_eq_u32 s40, 63\n" \
  "s_cbranch_scc1 pubgo_%=\n" \
  "s_sleep 8\n" \
  "pubgo_%=:\n" \
  "ds_read_b64_tr_b16 v[8:9], v5 offset:0\n" \
  "ds_read_b64_tr_b16 v[10:11], v5 offset:256\n" \
  "ds_read_b64_tr_b16 v[12:13], v5 offset:1024\n" \
  "ds_read_b64_tr_b16 v[14:15], v5 offset:1280\n" \
  "ds_read_b64_tr_b16 v[16:17], v5 offset:2048\n" \
  "ds_read_b64_tr_b16 v[18:19], v5 offset:2304\n" \
  "ds_read_b64_tr_b16 v[20:21], v5 offset:3072\n" \
  "ds_read_b64_tr_b16 v[22:23], v5 offset:3328\n" \
  "ds_read_b64_tr_b16 v[24:25], v5 offset:4096\n" \
  "ds_read_b64_tr_b16 v[26:27], v5 offset:4352\n" \
  "ds_read_b64_tr_b16 v[28:29], v5 offset:5120\n" \
  "ds_read_b64_tr_b16 v[30:31], v5 offset:5376\n" \
  "ds_read_b64_tr_b16 v[32:33], v5 offset:6144\n" \
  "ds_read_b64_tr_b16 v[34:35], v5 offset:6400\n" \
  "s_lshl_b32 s43, s40, 4\n" \
  "s_add_i32 s43, s43, %1\n" \
  "s_mul_i32 s44, s43, 7168\n" \
  "s_add_i32 s44, s44, 76546048\n" \
  "s_add_i32 s45, s44, 4096\n" \
  "s_lshl_b32 s43, %1, 7\n" \
  "s_add_i32 s43, s43, 104898560\n" \
  "s_waitcnt lgkmcnt(0)\n" \
  "buffer_store_dwordx4 v[8:11], v0, %0, s44 offen offset:0 sc1\n" \
  "buffer_store_dwordx4 v[12:15], v0, %0, s44 offen offset:1024 sc1\n" \
  "buffer_store_dwordx4 v[16:19], v0, %0, s44 offen offset:2048 sc1\n" \
  "buffer_store_dwordx4 v[20:23], v0, %0, s44 offen offset:3072 sc1\n" \
  "buffer_store_dwordx4 v[24:27], v0, %0, s45 offen offset:0 sc1\n" \
  "buffer_store_dwordx4 v[28:31], v0, %0, s45 offen offset:1024 sc1\n" \
  "buffer_store_dwordx4 v[32:35], v0, %0, s45 offen offset:2048 sc1\n" \
  "s_waitcnt vmcnt(0)\n" \
  "s_mov_b32 s70, s43\n" \
  "s_sub_i32 s96, s40, 1500\n" \
  "s_mov_b32 s99, 1\n" \
  "s_cmp_eq_u32 s40, 63\n" \
  "s_cbranch_scc0 nopub_%=\n" \
  "s_add_i32 s50, s40, 1\n" \
  "s_mov_b64 exec, 1\n" \
  "v_mov_b32 v6, s50\n" \
  "buffer_store_dword v6, off, %0, s43 sc1\n" \
  "s_mov_b64 exec, -1\n" \
  "nopub_%=:\n" \
  "s_cmp_eq_u32 s46, 0\n" \
  "s_cselect_b32 s43, -1, 14\n" \
  "s_cmp_eq_u32 s46, 2\n" \
  "s_cselect_b32 s43, 6, s43\n" \
  "s_cmp_gt_u32 s46, 2\n" \
  "s_cselect_b32 s43, 12, s43\n" \
  "s_add_i32 s42, s42, s43\n" \
  "s_add_i32 s40, s40, 1\n" \
  "s_cmp_lt_u32 s40, 64\n" \
  "s_cbranch_scc1 loop_%=\n" \
  "s_branch end_%=\n" \
  "tmo_%=:\n" \
  "s_mov_b64 exec, 1\n" \
  "v_mov_b32 v7, 1\n" \
  "buffer_store_dword v7, off, %0, s47 sc1\n" \
  "s_mov_b64 exec, -1\n" \
  "end_%=:\n" \
  "s_waitcnt vmcnt(0) lgkmcnt(0)\n"

#define ASM_VA \
  "v_mbcnt_lo_u32_b32 v7, -1, 0\n" \
  "v_mbcnt_hi_u32_b32 v7, -1, v7\n" \
  "v_lshlrev_b32 v0, 4, v7\n" \
  "v_and_b32 v6, 8, v7\n" \
  "v_mov_b32 v1, 0x4444\n" \
  "v_mov_b32 v2, 0xeeee\n" \
  "v_cmp_ne_u32 vcc, 0, v6\n" \
  "s_nop 1\n" \
  "v_cndmask_b32 v1, v1, v2, vcc\n" \
  "s_mov_b32 s40, 0\n" \
  "s_mov_b32 s41, 262144\n" \
  "s_mov_b32 s42, %7\n" \
  "s_mov_b32 s47, 104857600\n" \
  "s_mov_b32 s46, 0\n" \
  "s_lshl_b32 s69, %1, 2\n" \
  "s_add_i32 s69, s69, %2\n" \
  "s_lshl_b32 s43, s69, 2\n" \
  "s_add_i32 s43, s43, 448\n" \
  "s_mul_i32 s68, s43, 14336\n" \
  "s_add_i32 s44, s68, 0\n" \
  "s_add_i32 s45, s44, 14336\n" \
  "buffer_load_dwordx4 a[0:3], v0, %0, s44 offen offset:0 sc1\n" \
  "buffer_load_dwordx4 a[4:7], v0, %0, s45 offen offset:0 sc1\n" \
  "buffer_load_dwordx4 a[8:11], v0, %0, s44 offen offset:1024 sc1\n" \
  "buffer_load_dwordx4 a[12:15], v0, %0, s45 offen offset:1024 sc1\n" \
  "buffer_load_dwordx4 a[16:19], v0, %0, s44 offen offset:2048 sc1\n" \
  "buffer_load_dwordx4 a[20:23], v0, %0, s45 offen offset:2048 sc1\n" \
  "buffer_load_dwordx4 a[24:27], v0, %0, s44 offen offset:3072 sc1\n" \
  "buffer_load_dwordx4 a[28:31], v0, %0, s45 offen offset:3072 sc1\n" \
  "s_add_i32 s44, s68, 4096\n" \
  "s_add_i32 s45, s44, 14336\n" \
  "buffer_load_dwordx4 a[32:35], v0, %0, s44 offen offset:0 sc1\n" \
  "buffer_load_dwordx4 a[36:39], v0, %0, s45 offen offset:0 sc1\n" \
  "buffer_load_dwordx4 a[40:43], v0, %0, s44 offen offset:1024 sc1\n" \
  "buffer_load_dwordx4 a[44:47], v0, %0, s45 offen offset:1024 sc1\n" \
  "buffer_load_dwordx4 a[48:51], v0, %0, s44 offen offset:2048 sc1\n" \
  "buffer_load_dwordx4 a[52:55], v0, %0, s45 offen offset:2048 sc1\n" \
  "buffer_load_dwordx4 a[56:59], v0, %0, s44 offen offset:3072 sc1\n" \
  "buffer_load_dwordx4 a[60:63], v0, %0, s45 offen offset:3072 sc1\n" \
  "s_add_i32 s44, s68, 8192\n" \
  "s_add_i32 s45, s44, 14336\n" \
  "buffer_load_dwordx4 a[64:67], v0, %0, s44 offen offset:0 sc1\n" \
  "buffer_load_dwordx4 a[68:71], v0, %0, s45 offen offset:0 sc1\n" \
  "buffer_load_dwordx4 a[72:75], v0, %0, s44 offen offset:1024 sc1\n" \
  "buffer_load_dwordx4 a[76:79], v0, %0, s45 offen offset:1024 sc1\n" \
  "buffer_load_dwordx4 a[80:83], v0, %0, s44 offen offset:2048 sc1\n" \
  "buffer_load_dwordx4 a[84:87], v0, %0, s45 offen offset:2048 sc1\n" \
  "buffer_load_dwordx4 a[88:91], v0, %0, s44 offen offset:3072 sc1\n" \
  "buffer_load_dwordx4 a[92:95], v0, %0, s45 offen offset:3072 sc1\n" \
  "s_add_i32 s44, s68, 12288\n" \
  "s_add_i32 s45, s44, 14336\n" \
  "buffer_load_dwordx4 a[96:99], v0, %0, s44 offen offset:0 sc1\n" \
  "buffer_load_dwordx4 a[100:103], v0, %0, s45 offen offset:0 sc1\n" \
  "buffer_load_dwordx4 a[104:107], v0, %0, s44 offen offset:1024 sc1\n" \
  "buffer_load_dwordx4 a[108:111], v0, %0, s45 offen offset:1024 sc1\n" \
  "s_add_i32 s44, s68, 28672\n" \
  "s_add_i32 s45, s44, 14336\n" \
  "buffer_load_dwordx4 a[112:115], v0, %0, s44 offen offset:0 sc1\n" \
  "buffer_load_dwordx4 a[116:119], v0, %0, s45 offen offset:0 sc1\n" \
  "buffer_load_dwordx4 a[120:123], v0, %0, s44 offen offset:1024 sc1\n" \
  "buffer_load_dwordx4 a[124:127], v0, %0, s45 offen offset:1024 sc1\n" \
  "buffer_load_dwordx4 a[128:131], v0, %0, s44 offen offset:2048 sc1\n" \
  "buffer_load_dwordx4 a[132:135], v0, %0, s45 offen offset:2048 sc1\n" \
  "buffer_load_dwordx4 a[136:139], v0, %0, s44 offen offset:3072 sc1\n" \
  "buffer_load_dwordx4 a[140:143], v0, %0, s45 offen offset:3072 sc1\n" \
  "s_add_i32 s44, s68, 32768\n" \
  "s_add_i32 s45, s44, 14336\n" \
  "buffer_load_dwordx4 a[144:147], v0, %0, s44 offen offset:0 sc1\n" \
  "buffer_load_dwordx4 a[148:151], v0, %0, s45 offen offset:0 sc1\n" \
  "buffer_load_dwordx4 a[152:155], v0, %0, s44 offen offset:1024 sc1\n" \
  "buffer_load_dwordx4 a[156:159], v0, %0, s45 offen offset:1024 sc1\n" \
  "buffer_load_dwordx4 a[160:163], v0, %0, s44 offen offset:2048 sc1\n" \
  "buffer_load_dwordx4 a[164:167], v0, %0, s45 offen offset:2048 sc1\n" \
  "buffer_load_dwordx4 a[168:171], v0, %0, s44 offen offset:3072 sc1\n" \
  "buffer_load_dwordx4 a[172:175], v0, %0, s45 offen offset:3072 sc1\n" \
  "s_add_i32 s44, s68, 36864\n" \
  "s_add_i32 s45, s44, 14336\n" \
  "buffer_load_dwordx4 a[176:179], v0, %0, s44 offen offset:0 sc1\n" \
  "buffer_load_dwordx4 a[180:183], v0, %0, s45 offen offset:0 sc1\n" \
  "buffer_load_dwordx4 a[184:187], v0, %0, s44 offen offset:1024 sc1\n" \
  "buffer_load_dwordx4 a[188:191], v0, %0, s45 offen offset:1024 sc1\n" \
  "buffer_load_dwordx4 a[192:195], v0, %0, s44 offen offset:2048 sc1\n" \
  "buffer_load_dwordx4 a[196:199], v0, %0, s45 offen offset:2048 sc1\n" \
  "buffer_load_dwordx4 a[200:203], v0, %0, s44 offen offset:3072 sc1\n" \
  "buffer_load_dwordx4 a[204:207], v0, %0, s45 offen offset:3072 sc1\n" \
  "s_add_i32 s44, s68, 40960\n" \
  "s_add_i32 s45, s44, 14336\n" \
  "buffer_load_dwordx4 a[208:211], v0, %0, s44 offen offset:0 sc1\n" \
  "buffer_load_dwordx4 a[212:215], v0, %0, s45 offen offset:0 sc1\n" \
  "buffer_load_dwordx4 a[216:219], v0, %0, s44 offen offset:1024 sc1\n" \
  "buffer_load_dwordx4 a[220:223], v0, %0, s45 offen offset:1024 sc1\n" \
  "v_and_b32 v6, 7, v7\n" \
  "v_lshlrev_b32 v8, 4, v6\n" \
  "v_mov_b32 v9, v8\n" \
  "v_bfe_u32 v6, v7, 3, 1\n" \
  "v_lshl_add_u32 v8, v6, 9, v8\n" \
  "v_lshl_add_u32 v9, v6, 8, v9\n" \
  "v_lshrrev_b32 v6, 4, v7\n" \
  "v_lshl_add_u32 v8, v6, 7, v8\n" \
  "v_lshrrev_b32 v6, 5, v7\n" \
  "v_lshl_add_u32 v9, v6, 9, v9\n" \
  "v_bfe_u32 v6, v7, 4, 1\n" \
  "v_lshl_add_u32 v9, v6, 7, v9\n" \
  "v_lshlrev_b32 v10, 6, v6\n" \
  "v_lshrrev_b32 v6, 5, v7\n" \
  "v_lshl_add_u32 v10, v6, 4, v10\n" \
  "v_and_b32 v6, 1, v7\n" \
  "v_lshl_add_u32 v10, v6, 5, v10\n" \
  "v_bfe_u32 v6, v7, 2, 1\n" \
  "v_lshl_add_u32 v10, v6, 8, v10\n" \
  "v_bfe_u32 v6, v7, 3, 1\n" \
  "v_lshl_add_u32 v10, v6, 3, v10\n" \
  "v_bfe_u32 v6, v7, 1, 1\n" \
  "v_lshl_add_u32 v10, v6, 2, v10\n" \
  "s_lshr_b32 s43, %1, 1\n" \
  "s_lshl_b32 s43, s43, 9\n" \
  "s_and_b32 s44, %1, 1\n" \
  "s_lshl_b32 s44, s44, 7\n" \
  "s_add_i32 s43, s43, s44\n" \
  "v_add_u32 v10, s43, v10\n" \
  "v_min_u32 v11, 3, v7\n" \
  "s_mul_i32 s43, %1, 4\n" \
  "v_add_u32 v11, s43, v11\n" \
  "v_lshlrev_b32 v11, 7, v11\n" \
  "v_add_u32 v11, 26624, v11\n" \
  "v_add_u32 v13, %6, v0\n" \
  "s_lshl_b32 s43, %2, 10\n" \
  "v_add_u32 v12, s43, v13\n" \
  "v_lshrrev_b32 v6, 4, v7\n" \
  "v_lshlrev_b32 v14, 7, v6\n" \
  "v_and_b32 v6, 7, v7\n" \
  "v_lshl_add_u32 v14, v6, 4, v14\n" \
  "s_add_i32 s43, %6, 4096\n" \
  "v_add_u32 v14, s43, v14\n" \
  "s_lshl_b32 s43, %2, 12\n" \
  "v_add_u32 v15, s43, v13\n" \
  "s_mov_b32 s52, 0x55555555\n" \
  "s_mov_b32 s53, 0x55555555\n" \
  "v_min_u32 v4, 31, v7\n" \
  "v_lshlrev_b32 v4, 6, v4\n" \
  "v_mul_u32_u24 v240, 114688, v7\n" \
  "v_add_u32 v240, 0, v240\n" \
  "v_add_u32 v6, %4, v240\n" \
  "v_lshrrev_b32 v6, 21, v6\n" \
  "v_lshrrev_b32 v6, v6, %5\n" \
  "v_and_b32 v6, 1, v6\n" \
  "v_lshl_add_u32 v240, v6, 13, v240\n" \
  "v_add_u32 v240, %3, v240\n" \
  "v_add_u32 v240, 0, v240\n" \
  "v_mul_u32_u24 v244, 114688, v7\n" \
  "v_add_u32 v244, 163840, v244\n" \
  "v_add_u32 v6, %4, v244\n" \
  "v_lshrrev_b32 v6, 21, v6\n" \
  "v_lshrrev_b32 v6, v6, %5\n" \
  "v_and_b32 v6, 1, v6\n" \
  "v_lshl_add_u32 v244, v6, 13, v244\n" \
  "v_add_u32 v244, %3, v244\n" \
  "v_add_u32 v244, 0, v244\n" \
  "v_mul_u32_u24 v241, 114688, v7\n" \
  "v_add_u32 v241, 16384, v241\n" \
  "v_add_u32 v6, %4, v241\n" \
  "v_lshrrev_b32 v6, 21, v6\n" \
  "v_lshrrev_b32 v6, v6, %5\n" \
  "v_and_b32 v6, 1, v6\n" \
  "v_lshl_add_u32 v241, v6, 13, v241\n" \
  "v_add_u32 v241, %3, v241\n" \
  "v_add_u32 v241, 0, v241\n" \
  "v_mul_u32_u24 v245, 114688, v7\n" \
  "v_add_u32 v245, 180224, v245\n" \
  "v_add_u32 v6, %4, v245\n" \
  "v_lshrrev_b32 v6, 21, v6\n" \
  "v_lshrrev_b32 v6, v6, %5\n" \
  "v_and_b32 v6, 1, v6\n" \
  "v_lshl_add_u32 v245, v6, 13, v245\n" \
  "v_add_u32 v245, %3, v245\n" \
  "v_add_u32 v245, 0, v245\n" \
  "v_mul_u32_u24 v242, 114688, v7\n" \
  "v_add_u32 v242, 32768, v242\n" \
  "v_add_u32 v6, %4, v242\n" \
  "v_lshrrev_b32 v6, 21, v6\n" \
  "v_lshrrev_b32 v6, v6, %5\n" \
  "v_and_b32 v6, 1, v6\n" \
  "v_lshl_add_u32 v242, v6, 13, v242\n" \
  "v_add_u32 v242, %3, v242\n" \
  "v_add_u32 v242, 0, v242\n" \
  "v_mul_u32_u24 v246, 114688, v7\n" \
  "v_add_u32 v246, 196608, v246\n" \
  "v_add_u32 v6, %4, v246\n" \
  "v_lshrrev_b32 v6, 21, v6\n" \
  "v_lshrrev_b32 v6, v6, %5\n" \
  "v_and_b32 v6, 1, v6\n" \
  "v_lshl_add_u32 v246, v6, 13, v246\n" \
  "v_add_u32 v246, %3, v246\n" \
  "v_add_u32 v246, 0, v246\n" \
  "v_mul_u32_u24 v243, 114688, v7\n" \
  "v_add_u32 v243, 98304, v243\n" \
  "v_add_u32 v6, %4, v243\n" \
  "v_lshrrev_b32 v6, 21, v6\n" \
  "v_lshrrev_b32 v6, v6, %5\n" \
  "v_and_b32 v6, 1, v6\n" \
  "v_lshl_add_u32 v243, v6, 13, v243\n" \
  "v_add_u32 v243, %3, v243\n" \
  "v_add_u32 v243, 0, v243\n" \
  "v_mul_u32_u24 v247, 114688, v7\n" \
  "v_add_u32 v247, 81920, v247\n" \
  "v_add_u32 v6, %4, v247\n" \
  "v_lshrrev_b32 v6, 21, v6\n" \
  "v_lshrrev_b32 v6, v6, %5\n" \
  "v_and_b32 v6, 1, v6\n" \
  "v_lshl_add_u32 v247, v6, 13, v247\n" \
  "v_add_u32 v247, %3, v247\n" \
  "v_add_u32 v247, 0, v247\n" \
  "s_waitcnt vmcnt(54)\n" \
  "v_accvgpr_read_b32 v6, a1\n" \
  "v_accvgpr_read_b32 v5, a3\n" \
  "v_accvgpr_mov_b32 a1, a4\n" \
  "v_accvgpr_mov_b32 a3, a5\n" \
  "v_accvgpr_mov_b32 a4, a2\n" \
  "v_accvgpr_mov_b32 a5, a6\n" \
  "v_accvgpr_write_b32 a2, v6\n" \
  "v_accvgpr_write_b32 a6, v5\n" \
  "s_waitcnt vmcnt(52)\n" \
  "v_accvgpr_read_b32 v6, a9\n" \
  "v_accvgpr_read_b32 v5, a11\n" \
  "v_accvgpr_mov_b32 a9, a12\n" \
  "v_accvgpr_mov_b32 a11, a13\n" \
  "v_accvgpr_mov_b32 a12, a10\n" \
  "v_accvgpr_mov_b32 a13, a14\n" \
  "v_accvgpr_write_b32 a10, v6\n" \
  "v_accvgpr_write_b32 a14, v5\n" \
  "s_waitcnt vmcnt(50)\n" \
  "v_accvgpr_read_b32 v6, a17\n" \
  "v_accvgpr_read_b32 v5, a19\n" \
  "v_accvgpr_mov_b32 a17, a20\n" \
  "v_accvgpr_mov_b32 a19, a21\n" \
  "v_accvgpr_mov_b32 a20, a18\n" \
  "v_accvgpr_mov_b32 a21, a22\n" \
  "v_accvgpr_write_b32 a18, v6\n" \
  "v_accvgpr_write_b32 a22, v5\n" \
  "s_waitcnt vmcnt(48)\n" \
  "v_accvgpr_read_b32 v6, a25\n" \
  "v_accvgpr_read_b32 v5, a27\n" \
  "v_accvgpr_mov_b32 a25, a28\n" \
  "v_accvgpr_mov_b32 a27, a29\n" \
  "v_accvgpr_mov_b32 a28, a26\n" \
  "v_accvgpr_mov_b32 a29, a30\n" \
  "v_accvgpr_write_b32 a26, v6\n" \
  "v_accvgpr_write_b32 a30, v5\n" \
  "s_waitcnt vmcnt(46)\n" \
  "v_accvgpr_read_b32 v6, a33\n" \
  "v_accvgpr_read_b32 v5, a35\n" \
  "v_accvgpr_mov_b32 a33, a36\n" \
  "v_accvgpr_mov_b32 a35, a37\n" \
  "v_accvgpr_mov_b32 a36, a34\n" \
  "v_accvgpr_mov_b32 a37, a38\n" \
  "v_accvgpr_write_b32 a34, v6\n" \
  "v_accvgpr_write_b32 a38, v5\n" \
  "s_waitcnt vmcnt(44)\n" \
  "v_accvgpr_read_b32 v6, a41\n" \
  "v_accvgpr_read_b32 v5, a43\n" \
  "v_accvgpr_mov_b32 a41, a44\n" \
  "v_accvgpr_mov_b32 a43, a45\n" \
  "v_accvgpr_mov_b32 a44, a42\n" \
  "v_accvgpr_mov_b32 a45, a46\n" \
  "v_accvgpr_write_b32 a42, v6\n" \
  "v_accvgpr_write_b32 a46, v5\n" \
  "s_waitcnt vmcnt(42)\n" \
  "v_accvgpr_read_b32 v6, a49\n" \
  "v_accvgpr_read_b32 v5, a51\n" \
  "v_accvgpr_mov_b32 a49, a52\n" \
  "v_accvgpr_mov_b32 a51, a53\n" \
  "v_accvgpr_mov_b32 a52, a50\n" \
  "v_accvgpr_mov_b32 a53, a54\n" \
  "v_accvgpr_write_b32 a50, v6\n" \
  "v_accvgpr_write_b32 a54, v5\n" \
  "s_waitcnt vmcnt(40)\n" \
  "v_accvgpr_read_b32 v6, a57\n" \
  "v_accvgpr_read_b32 v5, a59\n" \
  "v_accvgpr_mov_b32 a57, a60\n" \
  "v_accvgpr_mov_b32 a59, a61\n" \
  "v_accvgpr_mov_b32 a60, a58\n" \
  "v_accvgpr_mov_b32 a61, a62\n" \
  "v_accvgpr_write_b32 a58, v6\n" \
  "v_accvgpr_write_b32 a62, v5\n" \
  "s_waitcnt vmcnt(38)\n" \
  "v_accvgpr_read_b32 v6, a65\n" \
  "v_accvgpr_read_b32 v5, a67\n" \
  "v_accvgpr_mov_b32 a65, a68\n" \
  "v_accvgpr_mov_b32 a67, a69\n" \
  "v_accvgpr_mov_b32 a68, a66\n" \
  "v_accvgpr_mov_b32 a69, a70\n" \
  "v_accvgpr_write_b32 a66, v6\n" \
  "v_accvgpr_write_b32 a70, v5\n" \
  "s_waitcnt vmcnt(36)\n" \
  "v_accvgpr_read_b32 v6, a73\n" \
  "v_accvgpr_read_b32 v5, a75\n" \
  "v_accvgpr_mov_b32 a73, a76\n" \
  "v_accvgpr_mov_b32 a75, a77\n" \
  "v_accvgpr_mov_b32 a76, a74\n" \
  "v_accvgpr_mov_b32 a77, a78\n" \
  "v_accvgpr_write_b32 a74, v6\n" \
  "v_accvgpr_write_b32 a78, v5\n" \
  "s_waitcnt vmcnt(34)\n" \
  "v_accvgpr_read_b32 v6, a81\n" \
  "v_accvgpr_read_b32 v5, a83\n" \
  "v_accvgpr_mov_b32 a81, a84\n" \
  "v_accvgpr_mov_b32 a83, a85\n" \
  "v_accvgpr_mov_b32 a84, a82\n" \
  "v_accvgpr_mov_b32 a85, a86\n" \
  "v_accvgpr_write_b32 a82, v6\n" \
  "v_accvgpr_write_b32 a86, v5\n" \
  "s_waitcnt vmcnt(32)\n" \
  "v_accvgpr_read_b32 v6, a89\n" \
  "v_accvgpr_read_b32 v5, a91\n" \
  "v_accvgpr_mov_b32 a89, a92\n" \
  "v_accvgpr_mov_b32 a91, a93\n" \
  "v_accvgpr_mov_b32 a92, a90\n" \
  "v_accvgpr_mov_b32 a93, a94\n" \
  "v_accvgpr_write_b32 a90, v6\n" \
  "v_accvgpr_write_b32 a94, v5\n" \
  "s_waitcnt vmcnt(30)\n" \
  "v_accvgpr_read_b32 v6, a97\n" \
  "v_accvgpr_read_b32 v5, a99\n" \
  "v_accvgpr_mov_b32 a97, a100\n" \
  "v_accvgpr_mov_b32 a99, a101\n" \
  "v_accvgpr_mov_b32 a100, a98\n" \
  "v_accvgpr_mov_b32 a101, a102\n" \
  "v_accvgpr_write_b32 a98, v6\n" \
  "v_accvgpr_write_b32 a102, v5\n" \
  "s_waitcnt vmcnt(28)\n" \
  "v_accvgpr_read_b32 v6, a105\n" \
  "v_accvgpr_read_b32 v5, a107\n" \
  "v_accvgpr_mov_b32 a105, a108\n" \
  "v_accvgpr_mov_b32 a107, a109\n" \
  "v_accvgpr_mov_b32 a108, a106\n" \
  "v_accvgpr_mov_b32 a109, a110\n" \
  "v_accvgpr_write_b32 a106, v6\n" \
  "v_accvgpr_write_b32 a110, v5\n" \
  "s_waitcnt vmcnt(26)\n" \
  "v_accvgpr_read_b32 v6, a113\n" \
  "v_accvgpr_read_b32 v5, a115\n" \
  "v_accvgpr_mov_b32 a113, a116\n" \
  "v_accvgpr_mov_b32 a115, a117\n" \
  "v_accvgpr_mov_b32 a116, a114\n" \
  "v_accvgpr_mov_b32 a117, a118\n" \
  "v_accvgpr_write_b32 a114, v6\n" \
  "v_accvgpr_write_b32 a118, v5\n" \
  "s_waitcnt vmcnt(24)\n" \
  "v_accvgpr_read_b32 v6, a121\n" \
  "v_accvgpr_read_b32 v5, a123\n" \
  "v_accvgpr_mov_b32 a121, a124\n" \
  "v_accvgpr_mov_b32 a123, a125\n" \
  "v_accvgpr_mov_b32 a124, a122\n" \
  "v_accvgpr_mov_b32 a125, a126\n" \
  "v_accvgpr_write_b32 a122, v6\n" \
  "v_accvgpr_write_b32 a126, v5\n" \
  "s_waitcnt vmcnt(22)\n" \
  "v_accvgpr_read_b32 v6, a129\n" \
  "v_accvgpr_read_b32 v5, a131\n" \
  "v_accvgpr_mov_b32 a129, a132\n" \
  "v_accvgpr_mov_b32 a131, a133\n" \
  "v_accvgpr_mov_b32 a132, a130\n" \
  "v_accvgpr_mov_b32 a133, a134\n" \
  "v_accvgpr_write_b32 a130, v6\n" \
  "v_accvgpr_write_b32 a134, v5\n" \
  "s_waitcnt vmcnt(20)\n" \
  "v_accvgpr_read_b32 v6, a137\n" \
  "v_accvgpr_read_b32 v5, a139\n" \
  "v_accvgpr_mov_b32 a137, a140\n" \
  "v_accvgpr_mov_b32 a139, a141\n" \
  "v_accvgpr_mov_b32 a140, a138\n" \
  "v_accvgpr_mov_b32 a141, a142\n" \
  "v_accvgpr_write_b32 a138, v6\n" \
  "v_accvgpr_write_b32 a142, v5\n" \
  "s_waitcnt vmcnt(18)\n" \
  "v_accvgpr_read_b32 v6, a145\n" \
  "v_accvgpr_read_b32 v5, a147\n" \
  "v_accvgpr_mov_b32 a145, a148\n" \
  "v_accvgpr_mov_b32 a147, a149\n" \
  "v_accvgpr_mov_b32 a148, a146\n" \
  "v_accvgpr_mov_b32 a149, a150\n" \
  "v_accvgpr_write_b32 a146, v6\n" \
  "v_accvgpr_write_b32 a150, v5\n" \
  "s_waitcnt vmcnt(16)\n" \
  "v_accvgpr_read_b32 v6, a153\n" \
  "v_accvgpr_read_b32 v5, a155\n" \
  "v_accvgpr_mov_b32 a153, a156\n" \
  "v_accvgpr_mov_b32 a155, a157\n" \
  "v_accvgpr_mov_b32 a156, a154\n" \
  "v_accvgpr_mov_b32 a157, a158\n" \
  "v_accvgpr_write_b32 a154, v6\n" \
  "v_accvgpr_write_b32 a158, v5\n" \
  "s_waitcnt vmcnt(14)\n" \
  "v_accvgpr_read_b32 v6, a161\n" \
  "v_accvgpr_read_b32 v5, a163\n" \
  "v_accvgpr_mov_b32 a161, a164\n" \
  "v_accvgpr_mov_b32 a163, a165\n" \
  "v_accvgpr_mov_b32 a164, a162\n" \
  "v_accvgpr_mov_b32 a165, a166\n" \
  "v_accvgpr_write_b32 a162, v6\n" \
  "v_accvgpr_write_b32 a166, v5\n" \
  "s_waitcnt vmcnt(12)\n" \
  "v_accvgpr_read_b32 v6, a169\n" \
  "v_accvgpr_read_b32 v5, a171\n" \
  "v_accvgpr_mov_b32 a169, a172\n" \
  "v_accvgpr_mov_b32 a171, a173\n" \
  "v_accvgpr_mov_b32 a172, a170\n" \
  "v_accvgpr_mov_b32 a173, a174\n" \
  "v_accvgpr_write_b32 a170, v6\n" \
  "v_accvgpr_write_b32 a174, v5\n" \
  "s_waitcnt vmcnt(10)\n" \
  "v_accvgpr_read_b32 v6, a177\n" \
  "v_accvgpr_read_b32 v5, a179\n" \
  "v_accvgpr_mov_b32 a177, a180\n" \
  "v_accvgpr_mov_b32 a179, a181\n" \
  "v_accvgpr_mov_b32 a180, a178\n" \
  "v_accvgpr_mov_b32 a181, a182\n" \
  "v_accvgpr_write_b32 a178, v6\n" \
  "v_accvgpr_write_b32 a182, v5\n" \
  "s_waitcnt vmcnt(8)\n" \
  "v_accvgpr_read_b32 v6, a185\n" \
  "v_accvgpr_read_b32 v5, a187\n" \
  "v_accvgpr_mov_b32 a185, a188\n" \
  "v_accvgpr_mov_b32 a187, a189\n" \
  "v_accvgpr_mov_b32 a188, a186\n" \
  "v_accvgpr_mov_b32 a189, a190\n" \
  "v_accvgpr_write_b32 a186, v6\n" \
  "v_accvgpr_write_b32 a190, v5\n" \
  "s_waitcnt vmcnt(6)\n" \
  "v_accvgpr_read_b32 v6, a193\n" \
  "v_accvgpr_read_b32 v5, a195\n" \
  "v_accvgpr_mov_b32 a193, a196\n" \
  "v_accvgpr_mov_b32 a195, a197\n" \
  "v_accvgpr_mov_b32 a196, a194\n" \
  "v_accvgpr_mov_b32 a197, a198\n" \
  "v_accvgpr_write_b32 a194, v6\n" \
  "v_accvgpr_write_b32 a198, v5\n" \
  "s_waitcnt vmcnt(4)\n" \
  "v_accvgpr_read_b32 v6, a201\n" \
  "v_accvgpr_read_b32 v5, a203\n" \
  "v_accvgpr_mov_b32 a201, a204\n" \
  "v_accvgpr_mov_b32 a203, a205\n" \
  "v_accvgpr_mov_b32 a204, a202\n" \
  "v_accvgpr_mov_b32 a205, a206\n" \
  "v_accvgpr_write_b32 a202, v6\n" \
  "v_accvgpr_write_b32 a206, v5\n" \
  "s_waitcnt vmcnt(2)\n" \
  "v_accvgpr_read_b32 v6, a209\n" \
  "v_accvgpr_read_b32 v5, a211\n" \
  "v_accvgpr_mov_b32 a209, a212\n" \
  "v_accvgpr_mov_b32 a211, a213\n" \
  "v_accvgpr_mov_b32 a212, a210\n" \
  "v_accvgpr_mov_b32 a213, a214\n" \
  "v_accvgpr_write_b32 a210, v6\n" \
  "v_accvgpr_write_b32 a214, v5\n" \
  "s_waitcnt vmcnt(0)\n" \
  "v_accvgpr_read_b32 v6, a217\n" \
  "v_accvgpr_read_b32 v5, a219\n" \
  "v_accvgpr_mov_b32 a217, a220\n" \
  "v_accvgpr_mov_b32 a219, a221\n" \
  "v_accvgpr_mov_b32 a220, a218\n" \
  "v_accvgpr_mov_b32 a221, a222\n" \
  "v_accvgpr_write_b32 a218, v6\n" \
  "v_accvgpr_write_b32 a222, v5\n" \
  "v_min_u32 v16, 55, v7\n" \
  "s_mul_i32 s43, 0, 0\n" \
  "v_add_u32 v16, s43, v16\n" \
  "v_lshlrev_b32 v16, 7, v16\n" \
  "v_add_u32 v16, 4096, v16\n" \
  "v_mov_b32 v17, 0\n" \
  "v_mov_b32 v18, 0\n" \
  "s_add_i32 s43, %6, 45056\n" \
  "v_mov_b32 v5, s43\n" \
  "ds_write_b32 v5, v18\n" \
  "s_cmp_eq_u32 %2, 0\n" \
  "s_cbranch_scc1 fiok_%=\n" \
  "s_cmp_eq_u32 %2, 3\n" \
  "s_cbranch_scc1 fiok_%=\n" \
  "fi_%=:\n" \
  "buffer_load_dword v6, v16, %0, s47 offen sc1\n" \
  "s_waitcnt vmcnt(0)\n" \
  "v_cmp_ge_u32 vcc, 0, v6\n" \
  "s_cbranch_vccz fiok_%=\n" \
  "s_sleep 3\n" \
  "s_sub_u32 s41, s41, 1\n" \
  "s_cbranch_scc1 tmo_%=\n" \
  "s_branch fi_%=\n" \
  "fiok_%=:\n" \
  "s_lshl_b32 s43, s69, 7\n" \
  "s_add_i32 s43, s43, 43008\n" \
  "s_sub_i32 s44, s43, 128\n" \
  "s_cmp_eq_u32 %2, 1\n" \
  "s_cselect_b32 s44, s44, s43\n" \
  "v_mov_b32 v16, s43\n" \
  "v_mov_b32 v6, s44\n" \
  "v_cmp_lt_u32 vcc, 31, v7\n" \
  "s_nop 1\n" \
  "v_cndmask_b32 v16, v16, v6, vcc\n" \
  "s_cmp_lg_u32 %2, 3\n" \
  "s_cbranch_scc1 loop_%=\n" \
  "s_add_i32 s70, s40, 0\n" \
  "v_cmp_ge_u32 vcc, s70, v17\n" \
  "s_cbranch_vccz fu0ok_%=\n" \
  "fu0_%=:\n" \
  "buffer_load_dword v17, v11, %0, s47 offen sc1\n" \
  "s_waitcnt vmcnt(0)\n" \
  "v_cmp_ge_u32 vcc, s70, v17\n" \
  "s_cbranch_vccz fu0ok_%=\n" \
  "s_sleep 3\n" \
  "s_sub_u32 s41, s41, 1\n" \
  "s_cbranch_scc1 tmo_%=\n" \
  "s_branch fu0_%=\n" \
  "fu0ok_%=:\n" \
  "s_mul_i32 s59, s70, 28\n" \
  "s_add_i32 s59, s59, %1\n" \
  "s_lshl_b32 s59, s59, 14\n" \
  "s_add_i32 s59, s59, 47185920\n" \
  "s_and_b32 s60, s70, 1\n" \
  "s_lshl_b32 s60, s60, 14\n" \
  "s_add_i32 s60, s60, %6\n" \
  "s_add_i32 s60, s60, 12288\n" \
  "s_add_i32 m0, s60, 0\n" \
  "s_add_i32 s43, s59, 0\n" \
  "buffer_load_dwordx4 v0, %0, s43 offen offset:0 sc1 lds\n" \
  "buffer_load_dwordx4 v0, %0, s43 offen offset:1024 sc1 lds\n" \
  "buffer_load_dwordx4 v0, %0, s43 offen offset:2048 sc1 lds\n" \
  "buffer_load_dwordx4 v0, %0, s43 offen offset:3072 sc1 lds\n" \
  "s_add_i32 m0, s60, 4096\n" \
  "s_add_i32 s43, s59, 4096\n" \
  "buffer_load_dwordx4 v0, %0, s43 offen offset:0 sc1 lds\n" \
  "buffer_load_dwordx4 v0, %0, s43 offen offset:1024 sc1 lds\n" \
  "buffer_load_dwordx4 v0, %0, s43 offen offset:2048 sc1 lds\n" \
  "buffer_load_dwordx4 v0, %0, s43 offen offset:3072 sc1 lds\n" \
  "s_add_i32 m0, s60, 8192\n" \
  "s_add_i32 s43, s59, 8192\n" \
  "buffer_load_dwordx4 v0, %0, s43 offen offset:0 sc1 lds\n" \
  "buffer_load_dwordx4 v0, %0, s43 offen offset:1024 sc1 lds\n" \
  "buffer_load_dwordx4 v0, %0, s43 offen offset:2048 sc1 lds\n" \
  "buffer_load_dwordx4 v0, %0, s43 offen offset:3072 sc1 lds\n" \
  "s_add_i32 m0, s60, 12288\n" \
  "s_add_i32 s43, s59, 12288\n" \
  "buffer_load_dwordx4 v0, %0, s43 offen offset:0 sc1 lds\n" \
  "buffer_load_dwordx4 v0, %0, s43 offen offset:1024 sc1 lds\n" \
  "buffer_load_dwordx4 v0, %0, s43 offen offset:2048 sc1 lds\n" \
  "buffer_load_dwordx4 v0, %0, s43 offen offset:3072 sc1 lds\n" \
  "loop_%=:\n" \
  "s_waitcnt vmcnt(3)\n" \
  "v_mov_b32 v152, 0\n" \
  "v_mov_b32 v153, 0\n" \
  "v_mov_b32 v154, 0\n" \
  "v_mov_b32 v155, 0\n" \
  "v_mov_b32 v156, 0\n" \
  "v_mov_b32 v157, 0\n" \
  "v_mov_b32 v158, 0\n" \
  "v_mov_b32 v159, 0\n" \
  "s_cmp_eq_u32 %2, 0\n" \
  "s_cbranch_scc1 w0pre_%=\n" \
  "s_cmp_eq_u32 %2, 3\n" \
  "s_cbranch_scc1 w3_%=\n" \
  "v_readlane_b32 s56, v247, s40\n" \
  "s_sub_i32 s43, %2, 1\n" \
  "s_lshl_b32 s43, s43, 12\n" \
  "s_add_i32 s56, s56, s43\n" \
  "s_lshl_b32 s43, %2, 10\n" \
  "s_sub_i32 s57, s56, s43\n" \
  "s_add_i32 s57, s57, 1024\n" \
  "s_cmp_eq_u32 s40, 0\n" \
  "s_cbranch_scc1 mem_%=\n" \
  "due_%=:\n" \
  "s_memtime s[50:51]\n" \
  "s_waitcnt lgkmcnt(0)\n" \
  "s_sub_i32 s43, s48, s50\n" \
  "s_cmp_gt_i32 s43, 0\n" \
  "s_cbranch_scc1 due_%=\n" \
  "mem_%=:\n" \
  "buffer_load_dwordx4 v[96:99], v0, %0, s56 offen offset:0 sc1\n" \
  "buffer_load_dwordx4 v[100:103], v0, %0, s56 offen offset:1024 sc1\n" \
  "buffer_load_dwordx4 v[104:107], v0, %0, s56 offen offset:2048 sc1\n" \
  "buffer_load_dwordx4 v[108:111], v0, %0, s57 offen offset:3072 sc1\n" \
  "s_waitcnt vmcnt(3)\n" \
  "v_max3_u32 v6, v96, v97, 0\n" \
  "v_max3_u32 v6, v6, v98, v99\n" \
  "s_waitcnt vmcnt(2)\n" \
  "v_max3_u32 v6, v100, v101, v6\n" \
  "v_max3_u32 v6, v6, v102, v103\n" \
  "s_waitcnt vmcnt(1)\n" \
  "v_max3_u32 v6, v104, v105, v6\n" \
  "v_max3_u32 v6, v6, v106, v107\n" \
  "s_waitcnt vmcnt(0)\n" \
  "v_max3_u32 v6, v108, v109, v6\n" \
  "v_max3_u32 v6, v6, v110, v111\n" \
  "v_cmp_eq_u32 vcc, -1, v6\n" \
  "s_cbranch_vccz memok_%=\n" \
  "s_sleep 2\n" \
  "s_sub_u32 s41, s41, 1\n" \
  "s_cbranch_scc1 tmo_%=\n" \
  "s_branch mem_%=\n" \
  "memok_%=:\n" \
  "ds_write_b128 v15, v[96:99] offset:0\n" \
  "ds_write_b128 v15, v[100:103] offset:1024\n" \
  "ds_write_b128 v15, v[104:107] offset:2048\n" \
  "ds_write_b128 v15, v[108:111] offset:3072\n" \
  "s_branch staged_%=\n" \
  "w0pre_%=:\n" \
  "v_readlane_b32 s56, v240, s40\n" \
  "v_readlane_b32 s65, v244, s40\n" \
  "s_add_i32 s59, s56, 4096\n" \
  "v_readlane_b32 s57, v241, s40\n" \
  "v_readlane_b32 s66, v245, s40\n" \
  "s_add_i32 s60, s57, 4096\n" \
  "v_readlane_b32 s58, v242, s40\n" \
  "v_readlane_b32 s67, v246, s40\n" \
  "s_add_i32 s61, s58, 4096\n" \
  "v_readlane_b32 s62, v243, s40\n" \
  "s_add_i32 s63, s62, 2048\n" \
  "s_add_i32 s64, s62, 4096\n" \
  "s_add_i32 s98, s62, 6144\n" \
  "v_mov_b32 v216, 0\n" \
  "v_mov_b32 v217, 0\n" \
  "v_mov_b32 v218, 0\n" \
  "v_mov_b32 v219, 0\n" \
  "s_and_b32 s43, s40, 1\n" \
  "s_lshl_b32 s43, s43, 14\n" \
  "s_add_i32 s43, s43, 12288\n" \
  "v_add_u32 v2, s43, v13\n" \
  "s_mov_b32 s46, 0\n" \
  "s_waitcnt lgkmcnt(0)\n" \
  "s_memtime s[50:51]\n" \
  "s_memrealtime s[48:49]\n" \
  "s_nop 1\n" \
  "s_memrealtime s[72:73]\n" \
  "s_nop 1\n" \
  "s_memrealtime s[74:75]\n" \
  "s_nop 1\n" \
  "s_memrealtime s[90:91]\n" \
  "s_nop 1\n" \
  "s_memrealtime s[92:93]\n" \
  "s_nop 1\n" \
  "s_memrealtime s[94:95]\n" \
  "s_nop 1\n" \
  "s_memrealtime s[100:101]\n" \
  "s_nop 1\n" \
  "s_memrealtime s[54:55]\n" \
  "s_nop 1\n" \
  "s_waitcnt lgkmcnt(0)\n" \
  "s_add_u32 s48, s48, s72\n" \
  "s_add_u32 s48, s48, s74\n" \
  "s_add_u32 s48, s48, s90\n" \
  "s_add_u32 s48, s48, s92\n" \
  "s_add_u32 s48, s48, s94\n" \
  "s_add_u32 s48, s48, s100\n" \
  "s_add_u32 s48, s48, s54\n" \
  "s_mul_i32 s48, s48, 3\n" \
  "s_sub_u32 s99, s48, s50\n" \
  "s_branch staged_%=\n" \
  "w3_%=:\n" \
  "s_waitcnt vmcnt(0)\n" \
  "s_sub_i32 s43, s40, 1\n" \
  "s_cmp_ge_u32 s43, 61\n" \
  "s_cbranch_scc1 staged_%=\n" \
  "s_add_i32 s70, s40, 1\n" \
  "v_cmp_ge_u32 vcc, s70, v17\n" \
  "s_cbranch_vccz fuok_%=\n" \
  "fu_%=:\n" \
  "buffer_load_dword v17, v11, %0, s47 offen sc1\n" \
  "s_waitcnt vmcnt(0)\n" \
  "v_cmp_ge_u32 vcc, s70, v17\n" \
  "s_cbranch_vccz fuok_%=\n" \
  "s_sleep 3\n" \
  "s_sub_u32 s41, s41, 1\n" \
  "s_cbranch_scc1 tmo_%=\n" \
  "s_branch fu_%=\n" \
  "fuok_%=:\n" \
  "s_mul_i32 s59, s70, 28\n" \
  "s_add_i32 s59, s59, %1\n" \
  "s_lshl_b32 s59, s59, 14\n" \
  "s_add_i32 s59, s59, 47185920\n" \
  "s_and_b32 s60, s70, 1\n" \
  "s_lshl_b32 s60, s60, 14\n" \
  "s_add_i32 s60, s60, %6\n" \
  "s_add_i32 s60, s60, 12288\n" \
  "staged_%=:\n" \
  "s_waitcnt lgkmcnt(0)\n" \
  "s_barrier\n" \
  "s_cmp_eq_u32 %2, 0\n" \
  "s_cbranch_scc1 um0_%=\n" \
  "s_cmp_lg_u32 %2, 3\n" \
  "s_cbranch_scc1 um1_%=\n" \
  "s_sub_i32 s43, s40, 1\n" \
  "s_cmp_ge_u32 s43, 61\n" \
  "s_cbranch_scc1 um1_%=\n" \
  "ds_read_b128 v[96:99], v14 offset:0\n" \
  "ds_read_b128 v[100:103], v14 offset:512\n" \
  "ds_read_b128 v[104:107], v14 offset:1024\n" \
  "s_waitcnt lgkmcnt(2)\n" \
  "v_smfmac_f32_16x16x64_bf16 v[152:155], v[96:99], a[0:7], v1\n" \
  "ds_read_b128 v[108:111], v14 offset:1536\n" \
  "v_smfmac_f32_16x16x64_bf16 v[156:159], v[96:99], a[112:119], v1\n" \
  "s_waitcnt lgkmcnt(2)\n" \
  "v_smfmac_f32_16x16x64_bf16 v[152:155], v[100:103], a[8:15], v1\n" \
  "ds_read_b128 v[112:115], v14 offset:2048\n" \
  "v_smfmac_f32_16x16x64_bf16 v[156:159], v[100:103], a[120:127], v1\n" \
  "s_waitcnt lgkmcnt(2)\n" \
  "v_smfmac_f32_16x16x64_bf16 v[152:155], v[104:107], a[16:23], v1\n" \
  "ds_read_b128 v[116:119], v14 offset:2560\n" \
  "v_smfmac_f32_16x16x64_bf16 v[156:159], v[104:107], a[128:135], v1\n" \
  "s_waitcnt lgkmcnt(2)\n" \
  "v_smfmac_f32_16x16x64_bf16 v[152:155], v[108:111], a[24:31], v1\n" \
  "ds_read_b128 v[120:123], v14 offset:3072\n" \
  "v_smfmac_f32_16x16x64_bf16 v[156:159], v[108:111], a[136:143], v1\n" \
  "s_waitcnt lgkmcnt(2)\n" \
  "v_smfmac_f32_16x16x64_bf16 v[152:155], v[112:115], a[32:39], v1\n" \
  "ds_read_b128 v[124:127], v14 offset:3584\n" \
  "v_smfmac_f32_16x16x64_bf16 v[156:159], v[112:115], a[144:151], v1\n" \
  "s_waitcnt lgkmcnt(2)\n" \
  "v_smfmac_f32_16x16x64_bf16 v[152:155], v[116:119], a[40:47], v1\n" \
  "ds_read_b128 v[128:131], v14 offset:4096\n" \
  "v_smfmac_f32_16x16x64_bf16 v[156:159], v[116:119], a[152:159], v1\n" \
  "s_waitcnt lgkmcnt(2)\n" \
  "v_smfmac_f32_16x16x64_bf16 v[152:155], v[120:123], a[48:55], v1\n" \
  "ds_read_b128 v[132:135], v14 offset:4608\n" \
  "v_smfmac_f32_16x16x64_bf16 v[156:159], v[120:123], a[160:167], v1\n" \
  "s_waitcnt lgkmcnt(2)\n" \
  "v_smfmac_f32_16x16x64_bf16 v[152:155], v[124:127], a[56:63], v1\n" \
  "ds_read_b128 v[136:139], v14 offset:5120\n" \
  "v_smfmac_f32_16x16x64_bf16 v[156:159], v[124:127], a[168:175], v1\n" \
  "s_waitcnt lgkmcnt(2)\n" \
  "v_smfmac_f32_16x16x64_bf16 v[152:155], v[128:131], a[64:71], v1\n" \
  "ds_read_b128 v[140:143], v14 offset:5632\n" \
  "v_smfmac_f32_16x16x64_bf16 v[156:159], v[128:131], a[176:183], v1\n" \
  "v_mov_b32 v6, s40\n" \
  "buffer_store_dword v6, v16, %0, s47 offen sc1\n" \
  "s_waitcnt lgkmcnt(2)\n" \
  "v_smfmac_f32_16x16x64_bf16 v[152:155], v[132:135], a[72:79], v1\n" \
  "ds_read_b128 v[144:147], v14 offset:6144\n" \
  "s_add_i32 m0, s60, 0\n" \
  "s_add_i32 s43, s59, 0\n" \
  "buffer_load_dwordx4 v0, %0, s43 offen offset:0 sc1 lds\n" \
  "v_smfmac_f32_16x16x64_bf16 v[156:159], v[132:135], a[184:191], v1\n" \
  "buffer_load_dwordx4 v0, %0, s43 offen offset:1024 sc1 lds\n" \
  "s_waitcnt lgkmcnt(2)\n" \
  "v_smfmac_f32_16x16x64_bf16 v[152:155], v[136:139], a[80:87], v1\n" \
  "ds_read_b128 v[148:151], v14 offset:6656\n" \
  "buffer_load_dwordx4 v0, %0, s43 offen offset:2048 sc1 lds\n" \
  "v_smfmac_f32_16x16x64_bf16 v[156:159], v[136:139], a[192:199], v1\n" \
  "buffer_load_dwordx4 v0, %0, s43 offen offset:3072 sc1 lds\n" \
  "s_waitcnt lgkmcnt(2)\n" \
  "v_smfmac_f32_16x16x64_bf16 v[152:155], v[140:143], a[88:95], v1\n" \
  "s_add_i32 m0, s60, 4096\n" \
  "s_add_i32 s43, s59, 4096\n" \
  "buffer_load_dwordx4 v0, %0, s43 offen offset:0 sc1 lds\n" \
  "v_smfmac_f32_16x16x64_bf16 v[156:159], v[140:143], a[200:207], v1\n" \
  "buffer_load_dwordx4 v0, %0, s43 offen offset:1024 sc1 lds\n" \
  "s_waitcnt lgkmcnt(1)\n" \
  "v_smfmac_f32_16x16x64_bf16 v[152:155], v[144:147], a[96:103], v1\n" \
  "buffer_load_dwordx4 v0, %0, s43 offen offset:2048 sc1 lds\n" \
  "v_smfmac_f32_16x16x64_bf16 v[156:159], v[144:147], a[208:215], v1\n" \
  "buffer_load_dwordx4 v0, %0, s43 offen offset:3072 sc1 lds\n" \
  "s_waitcnt lgkmcnt(0)\n" \
  "v_smfmac_f32_16x16x64_bf16 v[152:155], v[148:151], a[104:111], v1\n" \
  "s_add_i32 m0, s60, 8192\n" \
  "s_add_i32 s43, s59, 8192\n" \
  "buffer_load_dwordx4 v0, %0, s43 offen offset:0 sc1 lds\n" \
  "v_smfmac_f32_16x16x64_bf16 v[156:159], v[148:151], a[216:223], v1\n" \
  "buffer_load_dwordx4 v0, %0, s43 offen offset:1024 sc1 lds\n" \
  "buffer_load_dwordx4 v0, %0, s43 offen offset:2048 sc1 lds\n" \
  "buffer_load_dwordx4 v0, %0, s43 offen offset:3072 sc1 lds\n" \
  "s_add_i32 m0, s60, 12288\n" \
  "s_add_i32 s43, s59, 12288\n" \
  "buffer_load_dwordx4 v0, %0, s43 offen offset:0 sc1 lds\n" \
  "buffer_load_dwordx4 v0, %0, s43 offen offset:1024 sc1 lds\n" \
  "buffer_load_dwordx4 v0, %0, s43 offen offset:2048 sc1 lds\n" \
  "buffer_load_dwordx4 v0, %0, s43 offen offset:3072 sc1 lds\n" \
  "s_branch umd_%=\n" \
  "um0_%=:\n" \
  "ds_read_b128 v[96:99], v14 offset:0\n" \
  "ds_read_b128 v[100:103], v14 offset:512\n" \
  "ds_read_b128 v[104:107], v14 offset:1024\n" \
  "s_waitcnt lgkmcnt(2)\n" \
  "v_smfmac_f32_16x16x64_bf16 v[152:155], v[96:99], a[0:7], v1\n" \
  "ds_read_b128 v[108:111], v14 offset:1536\n" \
  "v_smfmac_f32_16x16x64_bf16 v[156:159], v[96:99], a[112:119], v1\n" \
  "ds_read_b128 v[16:19], v2 offset:0\n" \
  "s_waitcnt lgkmcnt(3)\n" \
  "v_smfmac_f32_16x16x64_bf16 v[152:155], v[100:103], a[8:15], v1\n" \
  "ds_read_b128 v[112:115], v14 offset:2048\n" \
  "v_smfmac_f32_16x16x64_bf16 v[156:159], v[100:103], a[120:127], v1\n" \
  "ds_read_b128 v[20:23], v2 offset:1024\n" \
  "s_waitcnt lgkmcnt(4)\n" \
  "v_smfmac_f32_16x16x64_bf16 v[152:155], v[104:107], a[16:23], v1\n" \
  "ds_read_b128 v[116:119], v14 offset:2560\n" \
  "v_smfmac_f32_16x16x64_bf16 v[156:159], v[104:107], a[128:135], v1\n" \
  "ds_read_b128 v[24:27], v2 offset:2048\n" \
  "s_waitcnt lgkmcnt(5)\n" \
  "v_smfmac_f32_16x16x64_bf16 v[152:155], v[108:111], a[24:31], v1\n" \
  "ds_read_b128 v[120:123], v14 offset:3072\n" \
  "v_smfmac_f32_16x16x64_bf16 v[156:159], v[108:111], a[136:143], v1\n" \
  "ds_read_b128 v[28:31], v2 offset:3072\n" \
  "s_waitcnt lgkmcnt(5)\n" \
  "v_smfmac_f32_16x16x64_bf16 v[152:155], v[112:115], a[32:39], v1\n" \
  "ds_read_b128 v[124:127], v14 offset:3584\n" \
  "v_smfmac_f32_16x16x64_bf16 v[156:159], v[112:115], a[144:151], v1\n" \
  "ds_read_b128 v[32:35], v2 offset:4096\n" \
  "s_waitcnt lgkmcnt(5)\n" \
  "v_smfmac_f32_16x16x64_bf16 v[152:155], v[116:119], a[40:47], v1\n" \
  "ds_read_b128 v[128:131], v14 offset:4096\n" \
  "v_smfmac_f32_16x16x64_bf16 v[156:159], v[116:119], a[152:159], v1\n" \
  "ds_read_b128 v[36:39], v2 offset:5120\n" \
  "s_waitcnt lgkmcnt(5)\n" \
  "v_smfmac_f32_16x16x64_bf16 v[152:155], v[120:123], a[48:55], v1\n" \
  "ds_read_b128 v[132:135], v14 offset:4608\n" \
  "v_smfmac_f32_16x16x64_bf16 v[156:159], v[120:123], a[160:167], v1\n" \
  "ds_read_b128 v[40:43], v2 offset:6144\n" \
  "s_waitcnt lgkmcnt(5)\n" \
  "v_smfmac_f32_16x16x64_bf16 v[152:155], v[124:127], a[56:63], v1\n" \
  "ds_read_b128 v[136:139], v14 offset:5120\n" \
  "v_smfmac_f32_16x16x64_bf16 v[156:159], v[124:127], a[168:175], v1\n" \
  "ds_read_b128 v[44:47], v2 offset:7168\n" \
  "s_waitcnt lgkmcnt(5)\n" \
  "v_smfmac_f32_16x16x64_bf16 v[152:155], v[128:131], a[64:71], v1\n" \
  "ds_read_b128 v[140:143], v14 offset:5632\n" \
  "buffer_load_dword v3, v4, %0, s98 offen sc1\n" \
  "v_smfmac_f32_16x16x64_bf16 v[156:159], v[128:131], a[176:183], v1\n" \
  "ds_read_b128 v[48:51], v2 offset:8192\n" \
  "s_waitcnt lgkmcnt(5)\n" \
  "v_smfmac_f32_16x16x64_bf16 v[152:155], v[132:135], a[72:79], v1\n" \
  "ds_read_b128 v[144:147], v14 offset:6144\n" \
  "v_smfmac_f32_16x16x64_bf16 v[156:159], v[132:135], a[184:191], v1\n" \
  "ds_read_b128 v[52:55], v2 offset:9216\n" \
  "s_waitcnt lgkmcnt(5)\n" \
  "v_smfmac_f32_16x16x64_bf16 v[152:155], v[136:139], a[80:87], v1\n" \
  "ds_read_b128 v[148:151], v14 offset:6656\n" \
  "v_smfmac_f32_16x16x64_bf16 v[156:159], v[136:139], a[192:199], v1\n" \
  "ds_read_b128 v[56:59], v2 offset:10240\n" \
  "s_waitcnt lgkmcnt(5)\n" \
  "v_smfmac_f32_16x16x64_bf16 v[152:155], v[140:143], a[88:95], v1\n" \
  "ds_read_b128 v[60:63], v2 offset:11264\n" \
  "v_smfmac_f32_16x16x64_bf16 v[156:159], v[140:143], a[200:207], v1\n" \
  "ds_read_b128 v[64:67], v2 offset:12288\n" \
  "s_waitcnt lgkmcnt(5)\n" \
  "v_smfmac_f32_16x16x64_bf16 v[152:155], v[144:147], a[96:103], v1\n" \
  "ds_read_b128 v[68:71], v2 offset:13312\n" \
  "v_smfmac_f32_16x16x64_bf16 v[156:159], v[144:147], a[208:215], v1\n" \
  "ds_read_b128 v[72:75], v2 offset:14336\n" \
  "s_waitcnt lgkmcnt(5)\n" \
  "v_smfmac_f32_16x16x64_bf16 v[152:155], v[148:151], a[104:111], v1\n" \
  "ds_read_b128 v[76:79], v2 offset:15360\n" \
  "v_smfmac_f32_16x16x64_bf16 v[156:159], v[148:151], a[216:223], v1\n" \
  "s_branch umd_%=\n" \
  "um1_%=:\n" \
  "ds_read_b128 v[96:99], v14 offset:0\n" \
  "ds_read_b128 v[100:103], v14 offset:512\n" \
  "ds_read_b128 v[104:107], v14 offset:1024\n" \
  "s_waitcnt lgkmcnt(2)\n" \
  "v_smfmac_f32_16x16x64_bf16 v[152:155], v[96:99], a[0:7], v1\n" \
  "ds_read_b128 v[108:111], v14 offset:1536\n" \
  "v_smfmac_f32_16x16x64_bf16 v[156:159], v[96:99], a[112:119], v1\n" \
  "s_waitcnt lgkmcnt(2)\n" \
  "v_smfmac_f32_16x16x64_bf16 v[152:155], v[100:103], a[8:15], v1\n" \
  "ds_read_b128 v[112:115], v14 offset:2048\n" \
  "v_smfmac_f32_16x16x64_bf16 v[156:159], v[100:103], a[120:127], v1\n" \
  "s_waitcnt lgkmcnt(2)\n" \
  "v_smfmac_f32_16x16x64_bf16 v[152:155], v[104:107], a[16:23], v1\n" \
  "ds_read_b128 v[116:119], v14 offset:2560\n" \
  "v_smfmac_f32_16x16x64_bf16 v[156:159], v[104:107], a[128:135], v1\n" \
  "s_waitcnt lgkmcnt(2)\n" \
  "v_smfmac_f32_16x16x64_bf16 v[152:155], v[108:111], a[24:31], v1\n" \
  "ds_read_b128 v[120:123], v14 offset:3072\n" \
  "v_smfmac_f32_16x16x64_bf16 v[156:159], v[108:111], a[136:143], v1\n" \
  "s_waitcnt lgkmcnt(2)\n" \
  "v_smfmac_f32_16x16x64_bf16 v[152:155], v[112:115], a[32:39], v1\n" \
  "ds_read_b128 v[124:127], v14 offset:3584\n" \
  "v_smfmac_f32_16x16x64_bf16 v[156:159], v[112:115], a[144:151], v1\n" \
  "s_waitcnt lgkmcnt(2)\n" \
  "v_smfmac_f32_16x16x64_bf16 v[152:155], v[116:119], a[40:47], v1\n" \
  "ds_read_b128 v[128:131], v14 offset:4096\n" \
  "v_smfmac_f32_16x16x64_bf16 v[156:159], v[116:119], a[152:159], v1\n" \
  "s_waitcnt lgkmcnt(2)\n" \
  "v_smfmac_f32_16x16x64_bf16 v[152:155], v[120:123], a[48:55], v1\n" \
  "ds_read_b128 v[132:135], v14 offset:4608\n" \
  "v_smfmac_f32_16x16x64_bf16 v[156:159], v[120:123], a[160:167], v1\n" \
  "s_waitcnt lgkmcnt(2)\n" \
  "v_smfmac_f32_16x16x64_bf16 v[152:155], v[124:127], a[56:63], v1\n" \
  "ds_read_b128 v[136:139], v14 offset:5120\n" \
  "v_smfmac_f32_16x16x64_bf16 v[156:159], v[124:127], a[168:175], v1\n" \
  "s_waitcnt lgkmcnt(2)\n" \
  "v_smfmac_f32_16x16x64_bf16 v[152:155], v[128:131], a[64:71], v1\n" \
  "ds_read_b128 v[140:143], v14 offset:5632\n" \
  "v_smfmac_f32_16x16x64_bf16 v[156:159], v[128:131], a[176:183], v1\n" \
  "v_mov_b32 v6, s40\n" \
  "buffer_store_dword v6, v16, %0, s47 offen sc1\n" \
  "s_waitcnt lgkmcnt(2)\n" \
  "v_smfmac_f32_16x16x64_bf16 v[152:155], v[132:135], a[72:79], v1\n" \
  "ds_read_b128 v[144:147], v14 offset:6144\n" \
  "v_smfmac_f32_16x16x64_bf16 v[156:159], v[132:135], a[184:191], v1\n" \
  "s_waitcnt lgkmcnt(2)\n" \
  "v_smfmac_f32_16x16x64_bf16 v[152:155], v[136:139], a[80:87], v1\n" \
  "ds_read_b128 v[148:151], v14 offset:6656\n" \
  "v_smfmac_f32_16x16x64_bf16 v[156:159], v[136:139], a[192:199], v1\n" \
  "s_waitcnt lgkmcnt(2)\n" \
  "v_smfmac_f32_16x16x64_bf16 v[152:155], v[140:143], a[88:95], v1\n" \
  "v_smfmac_f32_16x16x64_bf16 v[156:159], v[140:143], a[200:207], v1\n" \
  "s_waitcnt lgkmcnt(1)\n" \
  "v_smfmac_f32_16x16x64_bf16 v[152:155], v[144:147], a[96:103], v1\n" \
  "v_smfmac_f32_16x16x64_bf16 v[156:159], v[144:147], a[208:215], v1\n" \
  "s_waitcnt lgkmcnt(0)\n" \
  "v_smfmac_f32_16x16x64_bf16 v[152:155], v[148:151], a[104:111], v1\n" \
  "v_smfmac_f32_16x16x64_bf16 v[156:159], v[148:151], a[216:223], v1\n" \
  "umd_%=:\n" \
  "s_nop 7\n" \
  "v_cvt_pk_bf16_f32 v220, v152, v154\n" \
  "v_cvt_pk_bf16_f32 v221, v156, v158\n" \
  "v_cvt_pk_bf16_f32 v222, v153, v155\n" \
  "v_cvt_pk_bf16_f32 v223, v157, v159\n" \
  "ds_write_b128 v12, v[220:223]\n" \
  "s_waitcnt lgkmcnt(0)\n" \
  "s_cmp_lg_u32 %2, 0\n" \
  "s_cbranch_scc0 w0_%=\n" \
  "s_barrier\n" \
  "s_add_i32 s45, s40, 1\n" \
  "s_cmp_eq_u32 s40, 63\n" \
  "s_cbranch_scc1 sigok_%=\n" \
  "s_sleep 8\n" \
  "sig_%=:\n" \
  "ds_read_b64 v[18:19], v5\n" \
  "s_waitcnt lgkmcnt(0)\n" \
  "v_cmp_eq_u32 vcc, s45, v18\n" \
  "s_cbranch_vccnz sigok_%=\n" \
  "s_sleep 2\n" \
  "s_sub_u32 s41, s41, 1\n" \
  "s_cbranch_scc1 tmo_%=\n" \
  "s_branch sig_%=\n" \
  "sigok_%=:\n" \
  "v_readfirstlane_b32 s48, v19\n" \
  "s_mul_i32 s58, s40, 112\n" \
  "s_add_i32 s58, s58, s69\n" \
  "s_lshl_b32 s44, s58, 10\n" \
  "s_add_i32 s44, s44, 83886080\n" \
  "v_swap_b32 v221, v222\n" \
  "s_nop 0\n" \
  "buffer_store_dwordx4 v[220:223], v0, %0, s44 offen sc1\n" \
  "s_cmp_eq_u32 %2, 1\n" \
  "s_cbranch_scc0 pub1_%=\n" \
  "ds_read_b128 v[228:231], v13\n" \
  "s_sub_i32 s44, s44, 1024\n" \
  "s_waitcnt lgkmcnt(0)\n" \
  "v_swap_b32 v229, v230\n" \
  "s_nop 0\n" \
  "buffer_store_dwordx4 v[228:231], v0, %0, s44 offen sc1\n" \
  "pub1_%=:\n" \
  "s_cmp_lg_u32 %2, 3\n" \
  "s_cbranch_scc1 pub3_%=\n" \
  "s_cmp_lg_u32 s40, 0\n" \
  "s_cbranch_scc1 pub3_%=\n" \
  "s_add_i32 s70, s40, 1\n" \
  "v_cmp_ge_u32 vcc, s70, v17\n" \
  "s_cbranch_vccz fu1ok_%=\n" \
  "fu1_%=:\n" \
  "buffer_load_dword v17, v11, %0, s47 offen sc1\n" \
  "s_waitcnt vmcnt(0)\n" \
  "v_cmp_ge_u32 vcc, s70, v17\n" \
  "s_cbranch_vccz fu1ok_%=\n" \
  "s_sleep 3\n" \
  "s_sub_u32 s41, s41, 1\n" \
  "s_cbranch_scc1 tmo_%=\n" \
  "s_branch fu1_%=\n" \
  "fu1ok_%=:\n" \
  "s_mul_i32 s59, s70, 28\n" \
  "s_add_i32 s59, s59, %1\n" \
  "s_lshl_b32 s59, s59, 14\n" \
  "s_add_i32 s59, s59, 47185920\n" \
  "s_and_b32 s60, s70, 1\n" \
  "s_lshl_b32 s60, s60, 14\n" \
  "s_add_i32 s60, s60, %6\n" \
  "s_add_i32 s60, s60, 12288\n" \
  "s_add_i32 m0, s60, 0\n" \
  "s_add_i32 s43, s59, 0\n" \
  "buffer_load_dwordx4 v0, %0, s43 offen offset:0 sc1 lds\n" \
  "buffer_load_dwordx4 v0, %0, s43 offen offset:1024 sc1 lds\n" \
  "buffer_load_dwordx4 v0, %0, s43 offen offset:2048 sc1 lds\n" \
  "buffer_load_dwordx4 v0, %0, s43 offen offset:3072 sc1 lds\n" \
  "s_add_i32 m0, s60, 4096\n" \
  "s_add_i32 s43, s59, 4096\n" \
  "buffer_load_dwordx4 v0, %0, s43 offen offset:0 sc1 lds\n" \
  "buffer_load_dwordx4 v0, %0, s43 offen offset:1024 sc1 lds\n" \
  "buffer_load_dwordx4 v0, %0, s43 offen offset:2048 sc1 lds\n" \
  "buffer_load_dwordx4 v0, %0, s43 offen offset:3072 sc1 lds\n" \
  "s_add_i32 m0, s60, 8192\n" \
  "s_add_i32 s43, s59, 8192\n" \
  "buffer_load_dwordx4 v0, %0, s43 offen offset:0 sc1 lds\n" \
  "buffer_load_dwordx4 v0, %0, s43 offen offset:1024 sc1 lds\n" \
  "buffer_load_dwordx4 v0, %0, s43 offen offset:2048 sc1 lds\n" \
  "buffer_load_dwordx4 v0, %0, s43 offen offset:3072 sc1 lds\n" \
  "s_add_i32 m0, s60, 12288\n" \
  "s_add_i32 s43, s59, 12288\n" \
  "buffer_load_dwordx4 v0, %0, s43 offen offset:0 sc1 lds\n" \
  "buffer_load_dwordx4 v0, %0, s43 offen offset:1024 sc1 lds\n" \
  "buffer_load_dwordx4 v0, %0, s43 offen offset:2048 sc1 lds\n" \
  "buffer_load_dwordx4 v0, %0, s43 offen offset:3072 sc1 lds\n" \
  "pub3_%=:\n" \
  "s_cmp_eq_u32 s40, 63\n" \
  "s_cbranch_scc0 pub2_%=\n" \
  "s_waitcnt vmcnt(0)\n" \
  "v_mov_b32 v6, 64\n" \
  "buffer_store_dword v6, v16, %0, s47 offen sc1\n" \
  "pub2_%=:\n" \
  "s_add_i32 s40, s40, 1\n" \
  "s_cmp_lt_u32 s40, 64\n" \
  "s_cbranch_scc1 loop_%=\n" \
  "s_branch end_%=\n" \
  "w0_%=:\n" \
  "s_cmp_eq_u32 s40, 63\n" \
  "s_cbranch_scc0 w0go_%=\n" \
  "s_barrier\n" \
  "s_branch end_%=\n" \
  "w0go_%=:\n" \
  "s_waitcnt vmcnt(0)\n" \
  "bell_%=:\n" \
  "v_cmp_eq_u32 vcc, -1, v3\n" \
  "s_cbranch_vccz bellok_%=\n" \
  "s_sleep 1\n" \
  "s_sub_u32 s41, s41, 1\n" \
  "s_cbranch_scc1 tmo_%=\n" \
  "buffer_load_dword v3, v4, %0, s98 offen sc1\n" \
  "s_waitcnt vmcnt(0)\n" \
  "s_branch bell_%=\n" \
  "bellok_%=:\n" \
  "s_add_u32 s43, s50, s99\n" \
  "v_subrev_u32 v3, s43, v3\n" \
  "s_nop 1\n" \
  "v_max_i32_dpp v3, v3, v3 row_ror:8 row_mask:0xf bank_mask:0xf\n" \
  "s_nop 1\n" \
  "v_max_i32_dpp v3, v3, v3 row_ror:4 row_mask:0xf bank_mask:0xf\n" \
  "s_nop 1\n" \
  "v_max_i32_dpp v3, v3, v3 row_ror:2 row_mask:0xf bank_mask:0xf\n" \
  "s_nop 1\n" \
  "v_max_i32_dpp v3, v3, v3 row_ror:1 row_mask:0xf bank_mask:0xf\n" \
  "s_nop 0\n" \
  "v_readlane_b32 s43, v3, 0\n" \
  "v_readlane_b32 s44, v3, 16\n" \
  "s_max_i32 s43, s43, s44\n" \
  "s_min_i32 s43, s43, 0x2000\n" \
  "s_add_i32 s43, s43, s42\n" \
  "s_add_u32 s48, s43, s50\n" \
  "bellsp_%=:\n" \
  "s_memtime s[50:51]\n" \
  "s_waitcnt lgkmcnt(0)\n" \
  "s_sub_i32 s43, s48, s50\n" \
  "s_cmp_gt_i32 s43, 0x70\n" \
  "s_cbranch_scc1 bellsp_%=\n" \
  "s_max_i32 s43, s43, 0\n" \
  "bellcd_%=:\n" \
  "s_sub_u32 s43, s43, 16\n" \
  "s_cbranch_scc0 bellcd_%=\n" \
  "buffer_load_dwordx4 v[96:99], v8, %0, s56 offen offset:0 sc1\n" \
  "buffer_load_dwordx4 v[100:103], v8, %0, s56 offen offset:1024 sc1\n" \
  "buffer_load_dwordx4 v[104:107], v8, %0, s56 offen offset:2048 sc1\n" \
  "buffer_load_dwordx4 v[108:111], v8, %0, s56 offen offset:3072 sc1\n" \
  "buffer_load_dwordx4 v[112:115], v8, %0, s59 offen offset:0 sc1\n" \
  "buffer_load_dwordx4 v[116:119], v8, %0, s59 offen offset:1024 sc1\n" \
  "buffer_load_dwordx4 v[120:123], v8, %0, s59 offen offset:2048 sc1\n" \
  "buffer_load_dwordx4 v[124:127], v8, %0, s59 offen offset:3072 sc1\n" \
  "buffer_load_dwordx4 v[128:131], v9, %0, s62 offen offset:0 sc1\n" \
  "buffer_load_dwordx4 v[132:135], v9, %0, s62 offen offset:1024 sc1\n" \
  "buffer_load_dwordx4 v[136:139], v8, %0, s57 offen offset:0 sc1\n" \
  "buffer_load_dwordx4 v[140:143], v8, %0, s57 offen offset:1024 sc1\n" \
  "buffer_load_dwordx4 v[144:147], v8, %0, s57 offen offset:2048 sc1\n" \
  "buffer_load_dwordx4 v[148:151], v8, %0, s57 offen offset:3072 sc1\n" \
  "buffer_load_dwordx4 v[152:155], v8, %0, s60 offen offset:0 sc1\n" \
  "buffer_load_dwordx4 v[156:159], v8, %0, s60 offen offset:1024 sc1\n" \
  "buffer_load_dwordx4 v[160:163], v8, %0, s60 offen offset:2048 sc1\n" \
  "buffer_load_dwordx4 v[164:167], v8, %0, s60 offen offset:3072 sc1\n" \
  "buffer_load_dwordx4 v[168:171], v9, %0, s63 offen offset:0 sc1\n" \
  "buffer_load_dwordx4 v[172:175], v9, %0, s63 offen offset:1024 sc1\n" \
  "s_barrier\n" \
  "ds_read_b128 v[80:83], v13 offset:0\n" \
  "ds_read_b128 v[84:87], v13 offset:1024\n" \
  "ds_read_b128 v[88:91], v13 offset:2048\n" \
  "ds_read_b128 v[92:95], v13 offset:3072\n" \
  "s_waitcnt lgkmcnt(0)\n" \
  "s_waitcnt vmcnt(19)\n" \
  "v_smfmac_f32_16x16x64_bf16 v[216:219], v[96:99], v[16:23], v1\n" \
  "s_waitcnt vmcnt(18)\n" \
  "v_smfmac_f32_16x16x64_bf16 v[216:219], v[100:103], v[24:31], v1\n" \
  "s_waitcnt vmcnt(17)\n" \
  "v_smfmac_f32_16x16x64_bf16 v[216:219], v[104:107], v[32:39], v1\n" \
  "s_waitcnt vmcnt(16)\n" \
  "v_smfmac_f32_16x16x64_bf16 v[216:219], v[108:111], v[40:47], v1\n" \
  "s_waitcnt vmcnt(15)\n" \
  "v_smfmac_f32_16x16x64_bf16 v[216:219], v[112:115], v[48:55], v1\n" \
  "s_waitcnt vmcnt(14)\n" \
  "v_smfmac_f32_16x16x64_bf16 v[216:219], v[116:119], v[56:63], v1\n" \
  "s_waitcnt vmcnt(13)\n" \
  "v_smfmac_f32_16x16x64_bf16 v[216:219], v[120:123], v[64:71], v1\n" \
  "s_waitcnt vmcnt(12)\n" \
  "v_smfmac_f32_16x16x64_bf16 v[216:219], v[124:127], v[72:79], v1\n" \
  "s_waitcnt vmcnt(11)\n" \
  "v_smfmac_f32_16x16x64_bf16 v[216:219], v[128:131], v[80:87], v1\n" \
  "s_waitcnt vmcnt(10)\n" \
  "v_smfmac_f32_16x16x64_bf16 v[216:219], v[132:135], v[88:95], v1\n" \
  "s_nop 7\n" \
  "v_cmp_u_f32 vcc, v216, v217\n" \
  "v_cmp_u_f32 s[96:97], v218, v219\n" \
  "v_permlane32_swap_b32 v216, v217\n" \
  "s_or_b64 vcc, vcc, s[96:97]\n" \
  "v_permlane32_swap_b32 v218, v219\n" \
  "s_cbranch_vccz done_%=\n" \
  "s_add_i32 s46, s46, 1\n" \
  "v_mov_b32 v216, 0\n" \
  "v_mov_b32 v217, 0\n" \
  "v_mov_b32 v218, 0\n" \
  "v_mov_b32 v219, 0\n" \
  "s_nop 1\n" \
  "buffer_load_dwordx4 v[96:99], v8, %0, s58 offen offset:0 sc1\n" \
  "buffer_load_dwordx4 v[100:103], v8, %0, s58 offen offset:1024 sc1\n" \
  "buffer_load_dwordx4 v[104:107], v8, %0, s58 offen offset:2048 sc1\n" \
  "buffer_load_dwordx4 v[108:111], v8, %0, s58 offen offset:3072 sc1\n" \
  "buffer_load_dwordx4 v[112:115], v8, %0, s61 offen offset:0 sc1\n" \
  "buffer_load_dwordx4 v[116:119], v8, %0, s61 offen offset:1024 sc1\n" \
  "buffer_load_dwordx4 v[120:123], v8, %0, s61 offen offset:2048 sc1\n" \
  "buffer_load_dwordx4 v[124:127], v8, %0, s61 offen offset:3072 sc1\n" \
  "buffer_load_dwordx4 v[128:131], v9, %0, s64 offen offset:0 sc1\n" \
  "buffer_load_dwordx4 v[132:135], v9, %0, s64 offen offset:1024 sc1\n" \
  "s_waitcnt vmcnt(19)\n" \
  "v_smfmac_f32_16x16x64_bf16 v[216:219], v[136:139], v[16:23], v1\n" \
  "s_waitcnt vmcnt(18)\n" \
  "v_smfmac_f32_16x16x64_bf16 v[216:219], v[140:143], v[24:31], v1\n" \
  "s_waitcnt vmcnt(17)\n" \
  "v_smfmac_f32_16x16x64_bf16 v[216:219], v[144:147], v[32:39], v1\n" \
  "s_waitcnt vmcnt(16)\n" \
  "v_smfmac_f32_16x16x64_bf16 v[216:219], v[148:151], v[40:47], v1\n" \
  "s_waitcnt vmcnt(15)\n" \
  "v_smfmac_f32_16x16x64_bf16 v[216:219], v[152:155], v[48:55], v1\n" \
  "s_waitcnt vmcnt(14)\n" \
  "v_smfmac_f32_16x16x64_bf16 v[216:219], v[156:159], v[56:63], v1\n" \
  "s_waitcnt vmcnt(13)\n" \
  "v_smfmac_f32_16x16x64_bf16 v[216:219], v[160:163], v[64:71], v1\n" \
  "s_waitcnt vmcnt(12)\n" \
  "v_smfmac_f32_16x16x64_bf16 v[216:219], v[164:167], v[72:79], v1\n" \
  "s_waitcnt vmcnt(11)\n" \
  "v_smfmac_f32_16x16x64_bf16 v[216:219], v[168:171], v[80:87], v1\n" \
  "s_waitcnt vmcnt(10)\n" \
  "v_smfmac_f32_16x16x64_bf16 v[216:219], v[172:175], v[88:95], v1\n" \
  "s_nop 7\n" \
  "v_cmp_u_f32 vcc, v216, v217\n" \
  "v_cmp_u_f32 s[96:97], v218, v219\n" \
  "v_permlane32_swap_b32 v216, v217\n" \
  "s_or_b64 vcc, vcc, s[96:97]\n" \
  "v_permlane32_swap_b32 v218, v219\n" \
  "s_cbranch_vccz done_%=\n" \
  "s_add_i32 s46, s46, 1\n" \
  "v_mov_b32 v216, 0\n" \
  "v_mov_b32 v217, 0\n" \
  "v_mov_b32 v218, 0\n" \
  "v_mov_b32 v219, 0\n" \
  "s_nop 1\n" \
  "s_waitcnt vmcnt(9)\n" \
  "v_smfmac_f32_16x16x64_bf16 v[216:219], v[96:99], v[16:23], v1\n" \
  "s_waitcnt vmcnt(8)\n" \
  "v_smfmac_f32_16x16x64_bf16 v[216:219], v[100:103], v[24:31], v1\n" \
  "s_waitcnt vmcnt(7)\n" \
  "v_smfmac_f32_16x16x64_bf16 v[216:219], v[104:107], v[32:39], v1\n" \
  "s_waitcnt vmcnt(6)\n" \
  "v_smfmac_f32_16x16x64_bf16 v[216:219], v[108:111], v[40:47], v1\n" \
  "s_waitcnt vmcnt(5)\n" \
  "v_smfmac_f32_16x16x64_bf16 v[216:219], v[112:115], v[48:55], v1\n" \
  "s_waitcnt vmcnt(4)\n" \
  "v_smfmac_f32_16x16x64_bf16 v[216:219], v[116:119], v[56:63], v1\n" \
  "s_waitcnt vmcnt(3)\n" \
  "v_smfmac_f32_16x16x64_bf16 v[216:219], v[120:123], v[64:71], v1\n" \
  "s_waitcnt vmcnt(2)\n" \
  "v_smfmac_f32_16x16x64_bf16 v[216:219], v[124:127], v[72:79], v1\n" \
  "s_waitcnt vmcnt(1)\n" \
  "v_smfmac_f32_16x16x64_bf16 v[216:219], v[128:131], v[80:87], v1\n" \
  "s_waitcnt vmcnt(0)\n" \
  "v_smfmac_f32_16x16x64_bf16 v[216:219], v[132:135], v[88:95], v1\n" \
  "s_nop 7\n" \
  "v_cmp_u_f32 vcc, v216, v217\n" \
  "v_cmp_u_f32 s[96:97], v218, v219\n" \
  "v_permlane32_swap_b32 v216, v217\n" \
  "s_or_b64 vcc, vcc, s[96:97]\n" \
  "v_permlane32_swap_b32 v218, v219\n" \
  "s_cbranch_vccz done_%=\n" \
  "s_add_i32 s46, s46, 1\n" \
  "v_mov_b32 v216, 0\n" \
  "v_mov_b32 v217, 0\n" \
  "v_mov_b32 v218, 0\n" \
  "v_mov_b32 v219, 0\n" \
  "s_nop 1\n" \
  "poll_%=:\n" \
  "s_sub_u32 s41, s41, 1\n" \
  "s_cbranch_scc1 tmo_%=\n" \
  "buffer_load_dwordx4 v[96:99], v8, %0, s58 offen offset:0 sc1\n" \
  "buffer_load_dwordx4 v[100:103], v8, %0, s58 offen offset:1024 sc1\n" \
  "buffer_load_dwordx4 v[104:107], v8, %0, s58 offen offset:2048 sc1\n" \
  "buffer_load_dwordx4 v[108:111], v8, %0, s58 offen offset:3072 sc1\n" \
  "buffer_load_dwordx4 v[112:115], v8, %0, s61 offen offset:0 sc1\n" \
  "buffer_load_dwordx4 v[116:119], v8, %0, s61 offen offset:1024 sc1\n" \
  "buffer_load_dwordx4 v[120:123], v8, %0, s61 offen offset:2048 sc1\n" \
  "buffer_load_dwordx4 v[124:127], v8, %0, s61 offen offset:3072 sc1\n" \
  "buffer_load_dwordx4 v[128:131], v9, %0, s64 offen offset:0 sc1\n" \
  "buffer_load_dwordx4 v[132:135], v9, %0, s64 offen offset:1024 sc1\n" \
  "s_waitcnt vmcnt(9)\n" \
  "v_smfmac_f32_16x16x64_bf16 v[216:219], v[96:99], v[16:23], v1\n" \
  "s_waitcnt vmcnt(8)\n" \
  "v_smfmac_f32_16x16x64_bf16 v[216:219], v[100:103], v[24:31], v1\n" \
  "s_waitcnt vmcnt(7)\n" \
  "v_smfmac_f32_16x16x64_bf16 v[216:219], v[104:107], v[32:39], v1\n" \
  "s_waitcnt vmcnt(6)\n" \
  "v_smfmac_f32_16x16x64_bf16 v[216:219], v[108:111], v[40:47], v1\n" \
  "s_waitcnt vmcnt(5)\n" \
  "v_smfmac_f32_16x16x64_bf16 v[216:219], v[112:115], v[48:55], v1\n" \
  "s_waitcnt vmcnt(4)\n" \
  "v_smfmac_f32_16x16x64_bf16 v[216:219], v[116:119], v[56:63], v1\n" \
  "s_waitcnt vmcnt(3)\n" \
  "v_smfmac_f32_16x16x64_bf16 v[216:219], v[120:123], v[64:71], v1\n" \
  "s_waitcnt vmcnt(2)\n" \
  "v_smfmac_f32_16x16x64_bf16 v[216:219], v[124:127], v[72:79], v1\n" \
  "s_waitcnt vmcnt(1)\n" \
  "v_smfmac_f32_16x16x64_bf16 v[216:219], v[128:131], v[80:87], v1\n" \
  "s_waitcnt vmcnt(0)\n" \
  "v_smfmac_f32_16x16x64_bf16 v[216:219], v[132:135], v[88:95], v1\n" \
  "s_nop 7\n" \
  "v_cmp_u_f32 vcc, v216, v217\n" \
  "v_cmp_u_f32 s[96:97], v218, v219\n" \
  "v_permlane32_swap_b32 v216, v217\n" \
  "s_or_b64 vcc, vcc, s[96:97]\n" \
  "v_permlane32_swap_b32 v218, v219\n" \
  "s_cbranch_vccz done_%=\n" \
  "v_max3_u32 v6, v96, v97, 0\n" \
  "v_max3_u32 v6, v6, v98, v99\n" \
  "v_max3_u32 v6, v100, v101, v6\n" \
  "v_max3_u32 v6, v6, v102, v103\n" \
  "v_max3_u32 v6, v104, v105, v6\n" \
  "v_max3_u32 v6, v6, v106, v107\n" \
  "v_max3_u32 v6, v108, v109, v6\n" \
  "v_max3_u32 v6, v6, v110, v111\n" \
  "v_max3_u32 v6, v112, v113, v6\n" \
  "v_max3_u32 v6, v6, v114, v115\n" \
  "v_max3_u32 v6, v116, v117, v6\n" \
  "v_max3_u32 v6, v6, v118, v119\n" \
  "v_max3_u32 v6, v120, v121, v6\n" \
  "v_max3_u32 v6, v6, v122, v123\n" \
  "v_max3_u32 v6, v124, v125, v6\n" \
  "v_max3_u32 v6, v6, v126, v127\n" \
  "v_max3_u32 v6, v128, v129, v6\n" \
  "v_max3_u32 v6, v6, v130, v131\n" \
  "v_max3_u32 v6, v132, v133, v6\n" \
  "v_max3_u32 v6, v6, v134, v135\n" \
  "v_cmp_eq_u32 vcc, -1, v6\n" \
  "s_cbranch_vccz done_%=\n" \
  "s_add_i32 s46, s46, 1\n" \
  "v_mov_b32 v216, 0\n" \
  "v_mov_b32 v217, 0\n" \
  "v_mov_b32 v218, 0\n" \
  "v_mov_b32 v219, 0\n" \
  "s_nop 1\n" \
  "s_branch poll_%=\n" \
  "done_%=:\n" \
  "s_mov_b64 vcc, s[52:53]\n" \
  "v_add_f32 v216, v216, v217\n" \
  "v_add_f32 v218, v218, v219\n" \
  "s_nop 1\n" \
  "v_cndmask_b32_dpp v220, v218, v216, vcc quad_perm:[1,0,3,2] row_mask:0xf bank_mask:0xf\n" \
  "s_not_b64 vcc, vcc\n" \
  "v_cndmask_b32_dpp v221, v216, v218, vcc quad_perm:[1,0,3,2] row_mask:0xf bank_mask:0xf\n" \
  "v_cvt_pk_bf16_f32 v220, v220, v221\n" \
  "buffer_store_dword v220, v10, %0, s65 offen sc1\n" \
  "buffer_store_dword v220, v10, %0, s66 offen sc1\n" \
  "buffer_store_dword v220, v10, %0, s67 offen sc1\n" \
  "s_add_i32 s43, s40, 1\n" \
  "s_add_i32 s44, s48, 1300\n" \
  "v_mov_b32 v2, s43\n" \
  "v_mov_b32 v3, s44\n" \
  "ds_write_b64 v5, v[2:3]\n" \
  "s_cmp_eq_u32 s46, 0\n" \
  "s_cselect_b32 s43, -1, 14\n" \
  "s_cmp_eq_u32 s46, 2\n" \
  "s_cselect_b32 s43, 6, s43\n" \
  "s_cmp_gt_u32 s46, 2\n" \
  "s_cselect_b32 s43, 12, s43\n" \
  "s_add_i32 s42, s42, s43\n" \
  "s_waitcnt lgkmcnt(0)\n" \
  "s_add_i32 s40, s40, 1\n" \
  "s_branch loop_%=\n" \
  "s_branch end_%=\n" \
  "tmo_%=:\n" \
  "s_mov_b64 exec, 1\n" \
  "v_mov_b32 v7, 1\n" \
  "buffer_store_dword v7, off, %0, s47 sc1\n" \
  "s_mov_b64 exec, -1\n" \
  "end_%=:\n" \
  "s_waitcnt vmcnt(0) lgkmcnt(0)\n"

#define ASM_GUG \
  "v_mbcnt_lo_u32_b32 v7, -1, 0\n" \
  "v_mbcnt_hi_u32_b32 v7, -1, v7\n" \
  "v_lshlrev_b32 v0, 4, v7\n" \
  "v_and_b32 v6, 8, v7\n" \
  "v_mov_b32 v1, 0x4444\n" \
  "v_mov_b32 v2, 0xeeee\n" \
  "v_cmp_ne_u32 vcc, 0, v6\n" \
  "s_nop 1\n" \
  "v_cndmask_b32 v1, v1, v2, vcc\n" \
  "s_mov_b32 s40, 0\n" \
  "s_mov_b32 s41, 262144\n" \
  "s_mov_b32 s42, %7\n" \
  "s_mov_b32 s47, 104857600\n" \
  "s_mov_b32 s46, 0\n" \
  "s_lshl_b32 s56, %1, 12\n" \
  "s_add_i32 s56, s56, 17825792\n" \
  "s_lshl_b32 s57, %1, 7\n" \
  "s_add_i32 s57, s57, 104869888\n" \
  "s_mul_i32 s58, %2, 7168\n" \
  "s_add_i32 s59, s58, %6\n" \
  "s_add_i32 s58, s58, 13631488\n" \
  "s_add_i32 m0, s59, 0\n" \
  "s_add_i32 s44, s58, 0\n" \
  "buffer_load_dwordx4 v0, %0, s44 offen offset:0 sc1 lds\n" \
  "buffer_load_dwordx4 v0, %0, s44 offen offset:1024 sc1 lds\n" \
  "buffer_load_dwordx4 v0, %0, s44 offen offset:2048 sc1 lds\n" \
  "buffer_load_dwordx4 v0, %0, s44 offen offset:3072 sc1 lds\n" \
  "s_add_i32 m0, s59, 4096\n" \
  "s_add_i32 s44, s58, 4096\n" \
  "buffer_load_dwordx4 v0, %0, s44 offen offset:0 sc1 lds\n" \
  "buffer_load_dwordx4 v0, %0, s44 offen offset:1024 sc1 lds\n" \
  "buffer_load_dwordx4 v0, %0, s44 offen offset:2048 sc1 lds\n" \
  "s_add_i32 s58, s58, 28672\n" \
  "s_mul_i32 s45, %1, 57344\n" \
  "s_add_i32 s45, s45, 0\n" \
  "s_add_i32 s60, s45, 0\n" \
  "s_add_i32 s61, s45, 14336\n" \
  "s_add_i32 s62, s45, 28672\n" \
  "s_add_i32 s63, s45, 43008\n" \
  "buffer_load_dwordx4 v[128:131], v0, %0, s60 offen offset:0 sc1\n" \
  "buffer_load_dwordx4 v[132:135], v0, %0, s60 offen offset:1024 sc1\n" \
  "buffer_load_dwordx4 v[136:139], v0, %0, s62 offen offset:0 sc1\n" \
  "buffer_load_dwordx4 v[140:143], v0, %0, s62 offen offset:1024 sc1\n" \
  "buffer_load_dwordx4 v[144:147], v0, %0, s60 offen offset:2048 sc1\n" \
  "buffer_load_dwordx4 v[148:151], v0, %0, s60 offen offset:3072 sc1\n" \
  "buffer_load_dwordx4 v[152:155], v0, %0, s62 offen offset:2048 sc1\n" \
  "buffer_load_dwordx4 v[156:159], v0, %0, s62 offen offset:3072 sc1\n" \
  "s_add_i32 s60, s60, 4096\n" \
  "s_add_i32 s62, s62, 4096\n" \
  "buffer_load_dwordx4 v[160:163], v0, %0, s60 offen offset:0 sc1\n" \
  "buffer_load_dwordx4 v[164:167], v0, %0, s60 offen offset:1024 sc1\n" \
  "buffer_load_dwordx4 v[168:171], v0, %0, s62 offen offset:0 sc1\n" \
  "buffer_load_dwordx4 v[172:175], v0, %0, s62 offen offset:1024 sc1\n" \
  "buffer_load_dwordx4 v[176:179], v0, %0, s60 offen offset:2048 sc1\n" \
  "buffer_load_dwordx4 v[180:183], v0, %0, s60 offen offset:3072 sc1\n" \
  "buffer_load_dwordx4 v[184:187], v0, %0, s62 offen offset:2048 sc1\n" \
  "buffer_load_dwordx4 v[188:191], v0, %0, s62 offen offset:3072 sc1\n" \
  "s_add_i32 s60, s60, 4096\n" \
  "s_add_i32 s62, s62, 4096\n" \
  "buffer_load_dwordx4 v[192:195], v0, %0, s60 offen offset:0 sc1\n" \
  "buffer_load_dwordx4 v[196:199], v0, %0, s60 offen offset:1024 sc1\n" \
  "buffer_load_dwordx4 v[200:203], v0, %0, s62 offen offset:0 sc1\n" \
  "buffer_load_dwordx4 v[204:207], v0, %0, s62 offen offset:1024 sc1\n" \
  "buffer_load_dwordx4 v[208:211], v0, %0, s60 offen offset:2048 sc1\n" \
  "buffer_load_dwordx4 v[212:215], v0, %0, s60 offen offset:3072 sc1\n" \
  "buffer_load_dwordx4 v[216:219], v0, %0, s62 offen offset:2048 sc1\n" \
  "buffer_load_dwordx4 v[220:223], v0, %0, s62 offen offset:3072 sc1\n" \
  "s_add_i32 s60, s60, 4096\n" \
  "s_add_i32 s62, s62, 4096\n" \
  "buffer_load_dwordx4 v[224:227], v0, %0, s60 offen offset:0 sc1\n" \
  "buffer_load_dwordx4 v[228:231], v0, %0, s60 offen offset:1024 sc1\n" \
  "buffer_load_dwordx4 v[232:235], v0, %0, s62 offen offset:0 sc1\n" \
  "buffer_load_dwordx4 v[236:239], v0, %0, s62 offen offset:1024 sc1\n" \
  "buffer_load_dwordx4 v[240:243], v0, %0, s60 offen offset:2048 sc1\n" \
  "buffer_load_dwordx4 v[244:247], v0, %0, s60 offen offset:3072 sc1\n" \
  "buffer_load_dwordx4 v[248:251], v0, %0, s62 offen offset:2048 sc1\n" \
  "buffer_load_dwordx4 v[252:255], v0, %0, s62 offen offset:3072 sc1\n" \
  "s_add_i32 s60, s60, 4096\n" \
  "s_add_i32 s62, s62, 4096\n" \
  "v_add_u32 v2, %6, v0\n" \
  "s_waitcnt vmcnt(32)\n" \
  "s_barrier\n" \
  "ds_read_b128 v[8:11], v2 offset:0\n" \
  "ds_read_b128 v[12:15], v2 offset:14336\n" \
  "ds_read_b128 v[16:19], v2 offset:1024\n" \
  "ds_read_b128 v[20:23], v2 offset:15360\n" \
  "ds_read_b128 v[24:27], v2 offset:2048\n" \
  "ds_read_b128 v[28:31], v2 offset:16384\n" \
  "ds_read_b128 v[32:35], v2 offset:3072\n" \
  "s_waitcnt vmcnt(28)\n" \
  "v_accvgpr_write_b32 a0, v128\n" \
  "v_accvgpr_write_b32 a56, v129\n" \
  "v_accvgpr_write_b32 a1, v130\n" \
  "v_accvgpr_write_b32 a57, v131\n" \
  "v_accvgpr_write_b32 a2, v132\n" \
  "v_accvgpr_write_b32 a58, v133\n" \
  "v_accvgpr_write_b32 a3, v134\n" \
  "v_accvgpr_write_b32 a59, v135\n" \
  "v_accvgpr_write_b32 a112, v136\n" \
  "v_accvgpr_write_b32 a168, v137\n" \
  "v_accvgpr_write_b32 a113, v138\n" \
  "v_accvgpr_write_b32 a169, v139\n" \
  "v_accvgpr_write_b32 a114, v140\n" \
  "v_accvgpr_write_b32 a170, v141\n" \
  "v_accvgpr_write_b32 a115, v142\n" \
  "v_accvgpr_write_b32 a171, v143\n" \
  "buffer_load_dwordx4 v[128:131], v0, %0, s60 offen offset:0 sc1\n" \
  "buffer_load_dwordx4 v[132:135], v0, %0, s60 offen offset:1024 sc1\n" \
  "buffer_load_dwordx4 v[136:139], v0, %0, s62 offen offset:0 sc1\n" \
  "buffer_load_dwordx4 v[140:143], v0, %0, s62 offen offset:1024 sc1\n" \
  "s_nop 1\n" \
  "s_waitcnt lgkmcnt(6)\n" \
  "v_mfma_f32_16x16x32_bf16 v[48:51], a[0:3], v[8:11], 0\n" \
  "ds_read_b128 v[36:39], v2 offset:17408\n" \
  "v_mfma_f32_16x16x32_bf16 v[52:55], a[56:59], v[8:11], 0\n" \
  "v_mfma_f32_16x16x32_bf16 v[56:59], a[112:115], v[8:11], 0\n" \
  "v_mfma_f32_16x16x32_bf16 v[60:63], a[168:171], v[8:11], 0\n" \
  "s_waitcnt lgkmcnt(6)\n" \
  "v_mfma_f32_16x16x32_bf16 v[64:67], a[0:3], v[12:15], 0\n" \
  "ds_read_b128 v[8:11], v2 offset:4096\n" \
  "v_mfma_f32_16x16x32_bf16 v[68:71], a[56:59], v[12:15], 0\n" \
  "v_mfma_f32_16x16x32_bf16 v[72:75], a[112:115], v[12:15], 0\n" \
  "v_mfma_f32_16x16x32_bf16 v[76:79], a[168:171], v[12:15], 0\n" \
  "s_waitcnt vmcnt(28)\n" \
  "v_accvgpr_write_b32 a4, v144\n" \
  "v_accvgpr_write_b32 a60, v145\n" \
  "v_accvgpr_write_b32 a5, v146\n" \
  "v_accvgpr_write_b32 a61, v147\n" \
  "v_accvgpr_write_b32 a6, v148\n" \
  "v_accvgpr_write_b32 a62, v149\n" \
  "v_accvgpr_write_b32 a7, v150\n" \
  "v_accvgpr_write_b32 a63, v151\n" \
  "v_accvgpr_write_b32 a116, v152\n" \
  "v_accvgpr_write_b32 a172, v153\n" \
  "v_accvgpr_write_b32 a117, v154\n" \
  "v_accvgpr_write_b32 a173, v155\n" \
  "v_accvgpr_write_b32 a118, v156\n" \
  "v_accvgpr_write_b32 a174, v157\n" \
  "v_accvgpr_write_b32 a119, v158\n" \
  "v_accvgpr_write_b32 a175, v159\n" \
  "buffer_load_dwordx4 v[144:147], v0, %0, s60 offen offset:2048 sc1\n" \
  "buffer_load_dwordx4 v[148:151], v0, %0, s60 offen offset:3072 sc1\n" \
  "buffer_load_dwordx4 v[152:155], v0, %0, s62 offen offset:2048 sc1\n" \
  "buffer_load_dwordx4 v[156:159], v0, %0, s62 offen offset:3072 sc1\n" \
  "s_add_i32 s60, s60, 4096\n" \
  "s_add_i32 s62, s62, 4096\n" \
  "s_nop 1\n" \
  "s_waitcnt lgkmcnt(6)\n" \
  "v_mfma_f32_16x16x32_bf16 v[48:51], a[4:7], v[16:19], v[48:51]\n" \
  "ds_read_b128 v[12:15], v2 offset:18432\n" \
  "v_mfma_f32_16x16x32_bf16 v[52:55], a[60:63], v[16:19], v[52:55]\n" \
  "v_mfma_f32_16x16x32_bf16 v[56:59], a[116:119], v[16:19], v[56:59]\n" \
  "v_mfma_f32_16x16x32_bf16 v[60:63], a[172:175], v[16:19], v[60:63]\n" \
  "s_waitcnt lgkmcnt(6)\n" \
  "v_mfma_f32_16x16x32_bf16 v[64:67], a[4:7], v[20:23], v[64:67]\n" \
  "ds_read_b128 v[16:19], v2 offset:5120\n" \
  "v_mfma_f32_16x16x32_bf16 v[68:71], a[60:63], v[20:23], v[68:71]\n" \
  "v_mfma_f32_16x16x32_bf16 v[72:75], a[116:119], v[20:23], v[72:75]\n" \
  "v_mfma_f32_16x16x32_bf16 v[76:79], a[172:175], v[20:23], v[76:79]\n" \
  "s_waitcnt vmcnt(28)\n" \
  "v_accvgpr_write_b32 a8, v160\n" \
  "v_accvgpr_write_b32 a64, v161\n" \
  "v_accvgpr_write_b32 a9, v162\n" \
  "v_accvgpr_write_b32 a65, v163\n" \
  "v_accvgpr_write_b32 a10, v164\n" \
  "v_accvgpr_write_b32 a66, v165\n" \
  "v_accvgpr_write_b32 a11, v166\n" \
  "v_accvgpr_write_b32 a67, v167\n" \
  "v_accvgpr_write_b32 a120, v168\n" \
  "v_accvgpr_write_b32 a176, v169\n" \
  "v_accvgpr_write_b32 a121, v170\n" \
  "v_accvgpr_write_b32 a177, v171\n" \
  "v_accvgpr_write_b32 a122, v172\n" \
  "v_accvgpr_write_b32 a178, v173\n" \
  "v_accvgpr_write_b32 a123, v174\n" \
  "v_accvgpr_write_b32 a179, v175\n" \
  "buffer_load_dwordx4 v[160:163], v0, %0, s60 offen offset:0 sc1\n" \
  "buffer_load_dwordx4 v[164:167], v0, %0, s60 offen offset:1024 sc1\n" \
  "buffer_load_dwordx4 v[168:171], v0, %0, s62 offen offset:0 sc1\n" \
  "buffer_load_dwordx4 v[172:175], v0, %0, s62 offen offset:1024 sc1\n" \
  "s_nop 1\n" \
  "s_waitcnt lgkmcnt(6)\n" \
  "v_mfma_f32_16x16x32_bf16 v[48:51], a[8:11], v[24:27], v[48:51]\n" \
  "ds_read_b128 v[20:23], v2 offset:19456\n" \
  "v_mfma_f32_16x16x32_bf16 v[52:55], a[64:67], v[24:27], v[52:55]\n" \
  "v_mfma_f32_16x16x32_bf16 v[56:59], a[120:123], v[24:27], v[56:59]\n" \
  "v_mfma_f32_16x16x32_bf16 v[60:63], a[176:179], v[24:27], v[60:63]\n" \
  "s_waitcnt lgkmcnt(6)\n" \
  "v_mfma_f32_16x16x32_bf16 v[64:67], a[8:11], v[28:31], v[64:67]\n" \
  "ds_read_b128 v[24:27], v2 offset:6144\n" \
  "v_mfma_f32_16x16x32_bf16 v[68:71], a[64:67], v[28:31], v[68:71]\n" \
  "v_mfma_f32_16x16x32_bf16 v[72:75], a[120:123], v[28:31], v[72:75]\n" \
  "v_mfma_f32_16x16x32_bf16 v[76:79], a[176:179], v[28:31], v[76:79]\n" \
  "s_waitcnt vmcnt(28)\n" \
  "v_accvgpr_write_b32 a12, v176\n" \
  "v_accvgpr_write_b32 a68, v177\n" \
  "v_accvgpr_write_b32 a13, v178\n" \
  "v_accvgpr_write_b32 a69, v179\n" \
  "v_accvgpr_write_b32 a14, v180\n" \
  "v_accvgpr_write_b32 a70, v181\n" \
  "v_accvgpr_write_b32 a15, v182\n" \
  "v_accvgpr_write_b32 a71, v183\n" \
  "v_accvgpr_write_b32 a124, v184\n" \
  "v_accvgpr_write_b32 a180, v185\n" \
  "v_accvgpr_write_b32 a125, v186\n" \
  "v_accvgpr_write_b32 a181, v187\n" \
  "v_accvgpr_write_b32 a126, v188\n" \
  "v_accvgpr_write_b32 a182, v189\n" \
  "v_accvgpr_write_b32 a127, v190\n" \
  "v_accvgpr_write_b32 a183, v191\n" \
  "buffer_load_dwordx4 v[176:179], v0, %0, s60 offen offset:2048 sc1\n" \
  "buffer_load_dwordx4 v[180:183], v0, %0, s60 offen offset:3072 sc1\n" \
  "buffer_load_dwordx4 v[184:187], v0, %0, s62 offen offset:2048 sc1\n" \
  "buffer_load_dwordx4 v[188:191], v0, %0, s62 offen offset:3072 sc1\n" \
  "s_add_i32 s60, s60, 4096\n" \
  "s_add_i32 s62, s62, 4096\n" \
  "s_nop 1\n" \
  "s_waitcnt lgkmcnt(6)\n" \
  "v_mfma_f32_16x16x32_bf16 v[48:51], a[12:15], v[32:35], v[48:51]\n" \
  "ds_read_b128 v[28:31], v2 offset:20480\n" \
  "v_mfma_f32_16x16x32_bf16 v[52:55], a[68:71], v[32:35], v[52:55]\n" \
  "v_mfma_f32_16x16x32_bf16 v[56:59], a[124:127], v[32:35], v[56:59]\n" \
  "v_mfma_f32_16x16x32_bf16 v[60:63], a[180:183], v[32:35], v[60:63]\n" \
  "s_waitcnt lgkmcnt(6)\n" \
  "v_mfma_f32_16x16x32_bf16 v[64:67], a[12:15], v[36:39], v[64:67]\n" \
  "ds_read_b128 v[32:35], v2 offset:7168\n" \
  "v_mfma_f32_16x16x32_bf16 v[68:71], a[68:71], v[36:39], v[68:71]\n" \
  "v_mfma_f32_16x16x32_bf16 v[72:75], a[124:127], v[36:39], v[72:75]\n" \
  "v_mfma_f32_16x16x32_bf16 v[76:79], a[180:183], v[36:39], v[76:79]\n" \
  "s_waitcnt vmcnt(28)\n" \
  "v_accvgpr_write_b32 a16, v192\n" \
  "v_accvgpr_write_b32 a72, v193\n" \
  "v_accvgpr_write_b32 a17, v194\n" \
  "v_accvgpr_write_b32 a73, v195\n" \
  "v_accvgpr_write_b32 a18, v196\n" \
  "v_accvgpr_write_b32 a74, v197\n" \
  "v_accvgpr_write_b32 a19, v198\n" \
  "v_accvgpr_write_b32 a75, v199\n" \
  "v_accvgpr_write_b32 a128, v200\n" \
  "v_accvgpr_write_b32 a184, v201\n" \
  "v_accvgpr_write_b32 a129, v202\n" \
  "v_accvgpr_write_b32 a185, v203\n" \
  "v_accvgpr_write_b32 a130, v204\n" \
  "v_accvgpr_write_b32 a186, v205\n" \
  "v_accvgpr_write_b32 a131, v206\n" \
  "v_accvgpr_write_b32 a187, v207\n" \
  "buffer_load_dwordx4 v[192:195], v0, %0, s60 offen offset:0 sc1\n" \
  "buffer_load_dwordx4 v[196:199], v0, %0, s60 offen offset:1024 sc1\n" \
  "buffer_load_dwordx4 v[200:203], v0, %0, s62 offen offset:0 sc1\n" \
  "buffer_load_dwordx4 v[204:207], v0, %0, s62 offen offset:1024 sc1\n" \
  "s_nop 1\n" \
  "s_waitcnt lgkmcnt(6)\n" \
  "v_mfma_f32_16x16x32_bf16 v[48:51], a[16:19], v[8:11], v[48:51]\n" \
  "ds_read_b128 v[36:39], v2 offset:21504\n" \
  "v_mfma_f32_16x16x32_bf16 v[52:55], a[72:75], v[8:11], v[52:55]\n" \
  "v_mfma_f32_16x16x32_bf16 v[56:59], a[128:131], v[8:11], v[56:59]\n" \
  "v_mfma_f32_16x16x32_bf16 v[60:63], a[184:187], v[8:11], v[60:63]\n" \
  "s_waitcnt lgkmcnt(6)\n" \
  "v_mfma_f32_16x16x32_bf16 v[64:67], a[16:19], v[12:15], v[64:67]\n" \
  "ds_read_b128 v[8:11], v2 offset:8192\n" \
  "v_mfma_f32_16x16x32_bf16 v[68:71], a[72:75], v[12:15], v[68:71]\n" \
  "v_mfma_f32_16x16x32_bf16 v[72:75], a[128:131], v[12:15], v[72:75]\n" \
  "v_mfma_f32_16x16x32_bf16 v[76:79], a[184:187], v[12:15], v[76:79]\n" \
  "s_waitcnt vmcnt(28)\n" \
  "v_accvgpr_write_b32 a20, v208\n" \
  "v_accvgpr_write_b32 a76, v209\n" \
  "v_accvgpr_write_b32 a21, v210\n" \
  "v_accvgpr_write_b32 a77, v211\n" \
  "v_accvgpr_write_b32 a22, v212\n" \
  "v_accvgpr_write_b32 a78, v213\n" \
  "v_accvgpr_write_b32 a23, v214\n" \
  "v_accvgpr_write_b32 a79, v215\n" \
  "v_accvgpr_write_b32 a132, v216\n" \
  "v_accvgpr_write_b32 a188, v217\n" \
  "v_accvgpr_write_b32 a133, v218\n" \
  "v_accvgpr_write_b32 a189, v219\n" \
  "v_accvgpr_write_b32 a134, v220\n" \
  "v_accvgpr_write_b32 a190, v221\n" \
  "v_accvgpr_write_b32 a135, v222\n" \
  "v_accvgpr_write_b32 a191, v223\n" \
  "buffer_load_dwordx4 v[208:211], v0, %0, s60 offen offset:2048 sc1\n" \
  "buffer_load_dwordx4 v[212:215], v0, %0, s60 offen offset:3072 sc1\n" \
  "buffer_load_dwordx4 v[216:219], v0, %0, s62 offen offset:2048 sc1\n" \
  "buffer_load_dwordx4 v[220:223], v0, %0, s62 offen offset:3072 sc1\n" \
  "s_add_i32 s60, s60, 4096\n" \
  "s_add_i32 s62, s62, 4096\n" \
  "s_nop 1\n" \
  "s_waitcnt lgkmcnt(6)\n" \
  "v_mfma_f32_16x16x32_bf16 v[48:51], a[20:23], v[16:19], v[48:51]\n" \
  "ds_read_b128 v[12:15], v2 offset:22528\n" \
  "v_mfma_f32_16x16x32_bf16 v[52:55], a[76:79], v[16:19], v[52:55]\n" \
  "v_mfma_f32_16x16x32_bf16 v[56:59], a[132:135], v[16:19], v[56:59]\n" \
  "v_mfma_f32_16x16x32_bf16 v[60:63], a[188:191], v[16:19], v[60:63]\n" \
  "s_waitcnt lgkmcnt(6)\n" \
  "v_mfma_f32_16x16x32_bf16 v[64:67], a[20:23], v[20:23], v[64:67]\n" \
  "ds_read_b128 v[16:19], v2 offset:9216\n" \
  "v_mfma_f32_16x16x32_bf16 v[68:71], a[76:79], v[20:23], v[68:71]\n" \
  "v_mfma_f32_16x16x32_bf16 v[72:75], a[132:135], v[20:23], v[72:75]\n" \
  "v_mfma_f32_16x16x32_bf16 v[76:79], a[188:191], v[20:23], v[76:79]\n" \
  "s_waitcnt vmcnt(28)\n" \
  "v_accvgpr_write_b32 a24, v224\n" \
  "v_accvgpr_write_b32 a80, v225\n" \
  "v_accvgpr_write_b32 a25, v226\n" \
  "v_accvgpr_write_b32 a81, v227\n" \
  "v_accvgpr_write_b32 a26, v228\n" \
  "v_accvgpr_write_b32 a82, v229\n" \
  "v_accvgpr_write_b32 a27, v230\n" \
  "v_accvgpr_write_b32 a83, v231\n" \
  "v_accvgpr_write_b32 a136, v232\n" \
  "v_accvgpr_write_b32 a192, v233\n" \
  "v_accvgpr_write_b32 a137, v234\n" \
  "v_accvgpr_write_b32 a193, v235\n" \
  "v_accvgpr_write_b32 a138, v236\n" \
  "v_accvgpr_write_b32 a194, v237\n" \
  "v_accvgpr_write_b32 a139, v238\n" \
  "v_accvgpr_write_b32 a195, v239\n" \
  "s_nop 1\n" \
  "s_waitcnt lgkmcnt(6)\n" \
  "v_mfma_f32_16x16x32_bf16 v[48:51], a[24:27], v[24:27], v[48:51]\n" \
  "ds_read_b128 v[20:23], v2 offset:23552\n" \
  "v_mfma_f32_16x16x32_bf16 v[52:55], a[80:83], v[24:27], v[52:55]\n" \
  "v_mfma_f32_16x16x32_bf16 v[56:59], a[136:139], v[24:27], v[56:59]\n" \
  "v_mfma_f32_16x16x32_bf16 v[60:63], a[192:195], v[24:27], v[60:63]\n" \
  "s_waitcnt lgkmcnt(6)\n" \
  "v_mfma_f32_16x16x32_bf16 v[64:67], a[24:27], v[28:31], v[64:67]\n" \
  "ds_read_b128 v[24:27], v2 offset:10240\n" \
  "v_mfma_f32_16x16x32_bf16 v[68:71], a[80:83], v[28:31], v[68:71]\n" \
  "v_mfma_f32_16x16x32_bf16 v[72:75], a[136:139], v[28:31], v[72:75]\n" \
  "v_mfma_f32_16x16x32_bf16 v[76:79], a[192:195], v[28:31], v[76:79]\n" \
  "s_waitcnt vmcnt(24)\n" \
  "v_accvgpr_write_b32 a28, v240\n" \
  "v_accvgpr_write_b32 a84, v241\n" \
  "v_accvgpr_write_b32 a29, v242\n" \
  "v_accvgpr_write_b32 a85, v243\n" \
  "v_accvgpr_write_b32 a30, v244\n" \
  "v_accvgpr_write_b32 a86, v245\n" \
  "v_accvgpr_write_b32 a31, v246\n" \
  "v_accvgpr_write_b32 a87, v247\n" \
  "v_accvgpr_write_b32 a140, v248\n" \
  "v_accvgpr_write_b32 a196, v249\n" \
  "v_accvgpr_write_b32 a141, v250\n" \
  "v_accvgpr_write_b32 a197, v251\n" \
  "v_accvgpr_write_b32 a142, v252\n" \
  "v_accvgpr_write_b32 a198, v253\n" \
  "v_accvgpr_write_b32 a143, v254\n" \
  "v_accvgpr_write_b32 a199, v255\n" \
  "s_nop 1\n" \
  "s_waitcnt lgkmcnt(6)\n" \
  "v_mfma_f32_16x16x32_bf16 v[48:51], a[28:31], v[32:35], v[48:51]\n" \
  "ds_read_b128 v[28:31], v2 offset:24576\n" \
  "v_mfma_f32_16x16x32_bf16 v[52:55], a[84:87], v[32:35], v[52:55]\n" \
  "v_mfma_f32_16x16x32_bf16 v[56:59], a[140:143], v[32:35], v[56:59]\n" \
  "v_mfma_f32_16x16x32_bf16 v[60:63], a[196:199], v[32:35], v[60:63]\n" \
  "s_waitcnt lgkmcnt(6)\n" \
  "v_mfma_f32_16x16x32_bf16 v[64:67], a[28:31], v[36:39], v[64:67]\n" \
  "ds_read_b128 v[32:35], v2 offset:11264\n" \
  "v_mfma_f32_16x16x32_bf16 v[68:71], a[84:87], v[36:39], v[68:71]\n" \
  "v_mfma_f32_16x16x32_bf16 v[72:75], a[140:143], v[36:39], v[72:75]\n" \
  "v_mfma_f32_16x16x32_bf16 v[76:79], a[196:199], v[36:39], v[76:79]\n" \
  "s_waitcnt vmcnt(20)\n" \
  "v_accvgpr_write_b32 a32, v128\n" \
  "v_accvgpr_write_b32 a88, v129\n" \
  "v_accvgpr_write_b32 a33, v130\n" \
  "v_accvgpr_write_b32 a89, v131\n" \
  "v_accvgpr_write_b32 a34, v132\n" \
  "v_accvgpr_write_b32 a90, v133\n" \
  "v_accvgpr_write_b32 a35, v134\n" \
  "v_accvgpr_write_b32 a91, v135\n" \
  "v_accvgpr_write_b32 a144, v136\n" \
  "v_accvgpr_write_b32 a200, v137\n" \
  "v_accvgpr_write_b32 a145, v138\n" \
  "v_accvgpr_write_b32 a201, v139\n" \
  "v_accvgpr_write_b32 a146, v140\n" \
  "v_accvgpr_write_b32 a202, v141\n" \
  "v_accvgpr_write_b32 a147, v142\n" \
  "v_accvgpr_write_b32 a203, v143\n" \
  "s_nop 1\n" \
  "s_waitcnt lgkmcnt(6)\n" \
  "v_mfma_f32_16x16x32_bf16 v[48:51], a[32:35], v[8:11], v[48:51]\n" \
  "ds_read_b128 v[36:39], v2 offset:25600\n" \
  "v_mfma_f32_16x16x32_bf16 v[52:55], a[88:91], v[8:11], v[52:55]\n" \
  "v_mfma_f32_16x16x32_bf16 v[56:59], a[144:147], v[8:11], v[56:59]\n" \
  "v_mfma_f32_16x16x32_bf16 v[60:63], a[200:203], v[8:11], v[60:63]\n" \
  "s_waitcnt lgkmcnt(6)\n" \
  "v_mfma_f32_16x16x32_bf16 v[64:67], a[32:35], v[12:15], v[64:67]\n" \
  "ds_read_b128 v[8:11], v2 offset:12288\n" \
  "v_mfma_f32_16x16x32_bf16 v[68:71], a[88:91], v[12:15], v[68:71]\n" \
  "v_mfma_f32_16x16x32_bf16 v[72:75], a[144:147], v[12:15], v[72:75]\n" \
  "v_mfma_f32_16x16x32_bf16 v[76:79], a[200:203], v[12:15], v[76:79]\n" \
  "s_waitcnt vmcnt(16)\n" \
  "v_accvgpr_write_b32 a36, v144\n" \
  "v_accvgpr_write_b32 a92, v145\n" \
  "v_accvgpr_write_b32 a37, v146\n" \
  "v_accvgpr_write_b32 a93, v147\n" \
  "v_accvgpr_write_b32 a38, v148\n" \
  "v_accvgpr_write_b32 a94, v149\n" \
  "v_accvgpr_write_b32 a39, v150\n" \
  "v_accvgpr_write_b32 a95, v151\n" \
  "v_accvgpr_write_b32 a148, v152\n" \
  "v_accvgpr_write_b32 a204, v153\n" \
  "v_accvgpr_write_b32 a149, v154\n" \
  "v_accvgpr_write_b32 a205, v155\n" \
  "v_accvgpr_write_b32 a150, v156\n" \
  "v_accvgpr_write_b32 a206, v157\n" \
  "v_accvgpr_write_b32 a151, v158\n" \
  "v_accvgpr_write_b32 a207, v159\n" \
  "s_nop 1\n" \
  "s_waitcnt lgkmcnt(6)\n" \
  "v_mfma_f32_16x16x32_bf16 v[48:51], a[36:39], v[16:19], v[48:51]\n" \
  "ds_read_b128 v[12:15], v2 offset:26624\n" \
  "v_mfma_f32_16x16x32_bf16 v[52:55], a[92:95], v[16:19], v[52:55]\n" \
  "v_mfma_f32_16x16x32_bf16 v[56:59], a[148:151], v[16:19], v[56:59]\n" \
  "v_mfma_f32_16x16x32_bf16 v[60:63], a[204:207], v[16:19], v[60:63]\n" \
  "s_waitcnt lgkmcnt(6)\n" \
  "v_mfma_f32_16x16x32_bf16 v[64:67], a[36:39], v[20:23], v[64:67]\n" \
  "ds_read_b128 v[16:19], v2 offset:13312\n" \
  "v_mfma_f32_16x16x32_bf16 v[68:71], a[92:95], v[20:23], v[68:71]\n" \
  "v_mfma_f32_16x16x32_bf16 v[72:75], a[148:151], v[20:23], v[72:75]\n" \
  "v_mfma_f32_16x16x32_bf16 v[76:79], a[204:207], v[20:23], v[76:79]\n" \
  "s_waitcnt vmcnt(12)\n" \
  "v_accvgpr_write_b32 a40, v160\n" \
  "v_accvgpr_write_b32 a96, v161\n" \
  "v_accvgpr_write_b32 a41, v162\n" \
  "v_accvgpr_write_b32 a97, v163\n" \
  "v_accvgpr_write_b32 a42, v164\n" \
  "v_accvgpr_write_b32 a98, v165\n" \
  "v_accvgpr_write_b32 a43, v166\n" \
  "v_accvgpr_write_b32 a99, v167\n" \
  "v_accvgpr_write_b32 a152, v168\n" \
  "v_accvgpr_write_b32 a208, v169\n" \
  "v_accvgpr_write_b32 a153, v170\n" \
  "v_accvgpr_write_b32 a209, v171\n" \
  "v_accvgpr_write_b32 a154, v172\n" \
  "v_accvgpr_write_b32 a210, v173\n" \
  "v_accvgpr_write_b32 a155, v174\n" \
  "v_accvgpr_write_b32 a211, v175\n" \
  "s_nop 1\n" \
  "s_waitcnt lgkmcnt(6)\n" \
  "v_mfma_f32_16x16x32_bf16 v[48:51], a[40:43], v[24:27], v[48:51]\n" \
  "ds_read_b128 v[20:23], v2 offset:27648\n" \
  "v_mfma_f32_16x16x32_bf16 v[52:55], a[96:99], v[24:27], v[52:55]\n" \
  "v_mfma_f32_16x16x32_bf16 v[56:59], a[152:155], v[24:27], v[56:59]\n" \
  "v_mfma_f32_16x16x32_bf16 v[60:63], a[208:211], v[24:27], v[60:63]\n" \
  "s_waitcnt lgkmcnt(6)\n" \
  "v_mfma_f32_16x16x32_bf16 v[64:67], a[40:43], v[28:31], v[64:67]\n" \
  "v_mfma_f32_16x16x32_bf16 v[68:71], a[96:99], v[28:31], v[68:71]\n" \
  "v_mfma_f32_16x16x32_bf16 v[72:75], a[152:155], v[28:31], v[72:75]\n" \
  "v_mfma_f32_16x16x32_bf16 v[76:79], a[208:211], v[28:31], v[76:79]\n" \
  "s_waitcnt vmcnt(8)\n" \
  "v_accvgpr_write_b32 a44, v176\n" \
  "v_accvgpr_write_b32 a100, v177\n" \
  "v_accvgpr_write_b32 a45, v178\n" \
  "v_accvgpr_write_b32 a101, v179\n" \
  "v_accvgpr_write_b32 a46, v180\n" \
  "v_accvgpr_write_b32 a102, v181\n" \
  "v_accvgpr_write_b32 a47, v182\n" \
  "v_accvgpr_write_b32 a103, v183\n" \
  "v_accvgpr_write_b32 a156, v184\n" \
  "v_accvgpr_write_b32 a212, v185\n" \
  "v_accvgpr_write_b32 a157, v186\n" \
  "v_accvgpr_write_b32 a213, v187\n" \
  "v_accvgpr_write_b32 a158, v188\n" \
  "v_accvgpr_write_b32 a214, v189\n" \
  "v_accvgpr_write_b32 a159, v190\n" \
  "v_accvgpr_write_b32 a215, v191\n" \
  "s_nop 1\n" \
  "s_waitcnt lgkmcnt(5)\n" \
  "v_mfma_f32_16x16x32_bf16 v[48:51], a[44:47], v[32:35], v[48:51]\n" \
  "v_mfma_f32_16x16x32_bf16 v[52:55], a[100:103], v[32:35], v[52:55]\n" \
  "v_mfma_f32_16x16x32_bf16 v[56:59], a[156:159], v[32:35], v[56:59]\n" \
  "v_mfma_f32_16x16x32_bf16 v[60:63], a[212:215], v[32:35], v[60:63]\n" \
  "s_waitcnt lgkmcnt(4)\n" \
  "v_mfma_f32_16x16x32_bf16 v[64:67], a[44:47], v[36:39], v[64:67]\n" \
  "v_mfma_f32_16x16x32_bf16 v[68:71], a[100:103], v[36:39], v[68:71]\n" \
  "v_mfma_f32_16x16x32_bf16 v[72:75], a[156:159], v[36:39], v[72:75]\n" \
  "v_mfma_f32_16x16x32_bf16 v[76:79], a[212:215], v[36:39], v[76:79]\n" \
  "s_waitcnt vmcnt(4)\n" \
  "v_accvgpr_write_b32 a48, v192\n" \
  "v_accvgpr_write_b32 a104, v193\n" \
  "v_accvgpr_write_b32 a49, v194\n" \
  "v_accvgpr_write_b32 a105, v195\n" \
  "v_accvgpr_write_b32 a50, v196\n" \
  "v_accvgpr_write_b32 a106, v197\n" \
  "v_accvgpr_write_b32 a51, v198\n" \
  "v_accvgpr_write_b32 a107, v199\n" \
  "v_accvgpr_write_b32 a160, v200\n" \
  "v_accvgpr_write_b32 a216, v201\n" \
  "v_accvgpr_write_b32 a161, v202\n" \
  "v_accvgpr_write_b32 a217, v203\n" \
  "v_accvgpr_write_b32 a162, v204\n" \
  "v_accvgpr_write_b32 a218, v205\n" \
  "v_accvgpr_write_b32 a163, v206\n" \
  "v_accvgpr_write_b32 a219, v207\n" \
  "s_nop 1\n" \
  "s_waitcnt lgkmcnt(3)\n" \
  "v_mfma_f32_16x16x32_bf16 v[48:51], a[48:51], v[8:11], v[48:51]\n" \
  "v_mfma_f32_16x16x32_bf16 v[52:55], a[104:107], v[8:11], v[52:55]\n" \
  "v_mfma_f32_16x16x32_bf16 v[56:59], a[160:163], v[8:11], v[56:59]\n" \
  "v_mfma_f32_16x16x32_bf16 v[60:63], a[216:219], v[8:11], v[60:63]\n" \
  "s_waitcnt lgkmcnt(2)\n" \
  "v_mfma_f32_16x16x32_bf16 v[64:67], a[48:51], v[12:15], v[64:67]\n" \
  "v_mfma_f32_16x16x32_bf16 v[68:71], a[104:107], v[12:15], v[68:71]\n" \
  "v_mfma_f32_16x16x32_bf16 v[72:75], a[160:163], v[12:15], v[72:75]\n" \
  "v_mfma_f32_16x16x32_bf16 v[76:79], a[216:219], v[12:15], v[76:79]\n" \
  "s_waitcnt vmcnt(0)\n" \
  "v_accvgpr_write_b32 a52, v208\n" \
  "v_accvgpr_write_b32 a108, v209\n" \
  "v_accvgpr_write_b32 a53, v210\n" \
  "v_accvgpr_write_b32 a109, v211\n" \
  "v_accvgpr_write_b32 a54, v212\n" \
  "v_accvgpr_write_b32 a110, v213\n" \
  "v_accvgpr_write_b32 a55, v214\n" \
  "v_accvgpr_write_b32 a111, v215\n" \
  "v_accvgpr_write_b32 a164, v216\n" \
  "v_accvgpr_write_b32 a220, v217\n" \
  "v_accvgpr_write_b32 a165, v218\n" \
  "v_accvgpr_write_b32 a221, v219\n" \
  "v_accvgpr_write_b32 a166, v220\n" \
  "v_accvgpr_write_b32 a222, v221\n" \
  "v_accvgpr_write_b32 a167, v222\n" \
  "v_accvgpr_write_b32 a223, v223\n" \
  "s_nop 1\n" \
  "s_add_i32 m0, s59, 28672\n" \
  "s_add_i32 s44, s58, 0\n" \
  "buffer_load_dwordx4 v0, %0, s44 offen offset:0 sc1 lds\n" \
  "buffer_load_dwordx4 v0, %0, s44 offen offset:1024 sc1 lds\n" \
  "buffer_load_dwordx4 v0, %0, s44 offen offset:2048 sc1 lds\n" \
  "buffer_load_dwordx4 v0, %0, s44 offen offset:3072 sc1 lds\n" \
  "s_add_i32 m0, s59, 32768\n" \
  "s_add_i32 s44, s58, 4096\n" \
  "buffer_load_dwordx4 v0, %0, s44 offen offset:0 sc1 lds\n" \
  "buffer_load_dwordx4 v0, %0, s44 offen offset:1024 sc1 lds\n" \
  "buffer_load_dwordx4 v0, %0, s44 offen offset:2048 sc1 lds\n" \
  "s_add_i32 s58, s58, 28672\n" \
  "s_waitcnt lgkmcnt(1)\n" \
  "v_mfma_f32_16x16x32_bf16 v[48:51], a[52:55], v[16:19], v[48:51]\n" \
  "v_mfma_f32_16x16x32_bf16 v[52:55], a[108:111], v[16:19], v[52:55]\n" \
  "v_mfma_f32_16x16x32_bf16 v[56:59], a[164:167], v[16:19], v[56:59]\n" \
  "v_mfma_f32_16x16x32_bf16 v[60:63], a[220:223], v[16:19], v[60:63]\n" \
  "s_waitcnt lgkmcnt(0)\n" \
  "v_mfma_f32_16x16x32_bf16 v[64:67], a[52:55], v[20:23], v[64:67]\n" \
  "v_mfma_f32_16x16x32_bf16 v[68:71], a[108:111], v[20:23], v[68:71]\n" \
  "v_mfma_f32_16x16x32_bf16 v[72:75], a[164:167], v[20:23], v[72:75]\n" \
  "v_mfma_f32_16x16x32_bf16 v[76:79], a[220:223], v[20:23], v[76:79]\n" \
  "s_waitcnt vmcnt(0)\n" \
  "s_barrier\n" \
  "s_mov_b32 s40, 1\n" \
  "loop_%=:\n" \
  "ds_read_b128 v[8:11], v2 offset:28672\n" \
  "ds_read_b128 v[12:15], v2 offset:43008\n" \
  "ds_read_b128 v[16:19], v2 offset:29696\n" \
  "ds_read_b128 v[20:23], v2 offset:44032\n" \
  "ds_read_b128 v[24:27], v2 offset:30720\n" \
  "ds_read_b128 v[28:31], v2 offset:45056\n" \
  "ds_read_b128 v[32:35], v2 offset:31744\n" \
  "s_waitcnt lgkmcnt(6)\n" \
  "v_mfma_f32_16x16x32_bf16 v[80:83], a[0:3], v[8:11], 0\n" \
  "ds_read_b128 v[36:39], v2 offset:46080\n" \
  "v_mfma_f32_16x16x32_bf16 v[84:87], a[56:59], v[8:11], 0\n" \
  "v_mfma_f32_16x16x32_bf16 v[88:91], a[112:115], v[8:11], 0\n" \
  "v_mfma_f32_16x16x32_bf16 v[92:95], a[168:171], v[8:11], 0\n" \
  "v_cvt_pk_bf16_f32 v112, v48, v49\n" \
  "s_waitcnt lgkmcnt(6)\n" \
  "v_mfma_f32_16x16x32_bf16 v[96:99], a[0:3], v[12:15], 0\n" \
  "ds_read_b128 v[8:11], v2 offset:32768\n" \
  "v_cvt_pk_bf16_f32 v113, v64, v65\n" \
  "v_mfma_f32_16x16x32_bf16 v[100:103], a[56:59], v[12:15], 0\n" \
  "v_cvt_pk_bf16_f32 v114, v50, v51\n" \
  "v_mfma_f32_16x16x32_bf16 v[104:107], a[112:115], v[12:15], 0\n" \
  "v_cvt_pk_bf16_f32 v115, v66, v67\n" \
  "v_mfma_f32_16x16x32_bf16 v[108:111], a[168:171], v[12:15], 0\n" \
  "buffer_store_dwordx4 v[112:115], v0, %0, s56 offen offset:0 sc1\n" \
  "s_waitcnt lgkmcnt(6)\n" \
  "v_mfma_f32_16x16x32_bf16 v[80:83], a[4:7], v[16:19], v[80:83]\n" \
  "ds_read_b128 v[12:15], v2 offset:47104\n" \
  "v_cvt_pk_bf16_f32 v116, v52, v53\n" \
  "v_mfma_f32_16x16x32_bf16 v[84:87], a[60:63], v[16:19], v[84:87]\n" \
  "v_cvt_pk_bf16_f32 v117, v68, v69\n" \
  "v_mfma_f32_16x16x32_bf16 v[88:91], a[116:119], v[16:19], v[88:91]\n" \
  "v_cvt_pk_bf16_f32 v118, v54, v55\n" \
  "v_mfma_f32_16x16x32_bf16 v[92:95], a[172:175], v[16:19], v[92:95]\n" \
  "v_cvt_pk_bf16_f32 v119, v70, v71\n" \
  "s_waitcnt lgkmcnt(6)\n" \
  "v_mfma_f32_16x16x32_bf16 v[96:99], a[4:7], v[20:23], v[96:99]\n" \
  "ds_read_b128 v[16:19], v2 offset:33792\n" \
  "buffer_store_dwordx4 v[116:119], v0, %0, s56 offen offset:1024 sc1\n" \
  "v_mfma_f32_16x16x32_bf16 v[100:103], a[60:63], v[20:23], v[100:103]\n" \
  "v_cvt_pk_bf16_f32 v120, v56, v57\n" \
  "v_mfma_f32_16x16x32_bf16 v[104:107], a[116:119], v[20:23], v[104:107]\n" \
  "v_cvt_pk_bf16_f32 v121, v72, v73\n" \
  "v_mfma_f32_16x16x32_bf16 v[108:111], a[172:175], v[20:23], v[108:111]\n" \
  "v_cvt_pk_bf16_f32 v122, v58, v59\n" \
  "s_waitcnt lgkmcnt(6)\n" \
  "v_mfma_f32_16x16x32_bf16 v[80:83], a[8:11], v[24:27], v[80:83]\n" \
  "ds_read_b128 v[20:23], v2 offset:48128\n" \
  "v_cvt_pk_bf16_f32 v123, v74, v75\n" \
  "v_mfma_f32_16x16x32_bf16 v[84:87], a[64:67], v[24:27], v[84:87]\n" \
  "buffer_store_dwordx4 v[120:123], v0, %0, s56 offen offset:2048 sc1\n" \
  "v_mfma_f32_16x16x32_bf16 v[88:91], a[120:123], v[24:27], v[88:91]\n" \
  "v_cvt_pk_bf16_f32 v124, v60, v61\n" \
  "v_mfma_f32_16x16x32_bf16 v[92:95], a[176:179], v[24:27], v[92:95]\n" \
  "v_cvt_pk_bf16_f32 v125, v76, v77\n" \
  "s_waitcnt lgkmcnt(6)\n" \
  "v_mfma_f32_16x16x32_bf16 v[96:99], a[8:11], v[28:31], v[96:99]\n" \
  "ds_read_b128 v[24:27], v2 offset:34816\n" \
  "v_cvt_pk_bf16_f32 v126, v62, v63\n" \
  "v_mfma_f32_16x16x32_bf16 v[100:103], a[64:67], v[28:31], v[100:103]\n" \
  "v_cvt_pk_bf16_f32 v127, v78, v79\n" \
  "v_mfma_f32_16x16x32_bf16 v[104:107], a[120:123], v[28:31], v[104:107]\n" \
  "buffer_store_dwordx4 v[124:127], v0, %0, s56 offen offset:3072 sc1\n" \
  "v_mfma_f32_16x16x32_bf16 v[108:111], a[176:179], v[28:31], v[108:111]\n" \
  "s_add_i32 s56, s56, 458752\n" \
  "s_waitcnt lgkmcnt(6)\n" \
  "v_mfma_f32_16x16x32_bf16 v[80:83], a[12:15], v[32:35], v[80:83]\n" \
  "ds_read_b128 v[28:31], v2 offset:49152\n" \
  "v_mfma_f32_16x16x32_bf16 v[84:87], a[68:71], v[32:35], v[84:87]\n" \
  "v_mfma_f32_16x16x32_bf16 v[88:91], a[124:127], v[32:35], v[88:91]\n" \
  "s_add_i32 m0, s59, 0\n" \
  "s_add_i32 s44, s58, 0\n" \
  "v_mfma_f32_16x16x32_bf16 v[92:95], a[180:183], v[32:35], v[92:95]\n" \
  "buffer_load_dwordx4 v0, %0, s44 offen offset:0 sc1 lds\n" \
  "s_waitcnt lgkmcnt(6)\n" \
  "v_mfma_f32_16x16x32_bf16 v[96:99], a[12:15], v[36:39], v[96:99]\n" \
  "ds_read_b128 v[32:35], v2 offset:35840\n" \
  "buffer_load_dwordx4 v0, %0, s44 offen offset:1024 sc1 lds\n" \
  "v_mfma_f32_16x16x32_bf16 v[100:103], a[68:71], v[36:39], v[100:103]\n" \
  "buffer_load_dwordx4 v0, %0, s44 offen offset:2048 sc1 lds\n" \
  "v_mfma_f32_16x16x32_bf16 v[104:107], a[124:127], v[36:39], v[104:107]\n" \
  "buffer_load_dwordx4 v0, %0, s44 offen offset:3072 sc1 lds\n" \
  "v_mfma_f32_16x16x32_bf16 v[108:111], a[180:183], v[36:39], v[108:111]\n" \
  "s_add_i32 m0, s59, 4096\n" \
  "s_add_i32 s44, s58, 4096\n" \
  "s_waitcnt lgkmcnt(6)\n" \
  "v_mfma_f32_16x16x32_bf16 v[80:83], a[16:19], v[8:11], v[80:83]\n" \
  "ds_read_b128 v[36:39], v2 offset:50176\n" \
  "buffer_load_dwordx4 v0, %0, s44 offen offset:0 sc1 lds\n" \
  "v_mfma_f32_16x16x32_bf16 v[84:87], a[72:75], v[8:11], v[84:87]\n" \
  "buffer_load_dwordx4 v0, %0, s44 offen offset:1024 sc1 lds\n" \
  "v_mfma_f32_16x16x32_bf16 v[88:91], a[128:131], v[8:11], v[88:91]\n" \
  "buffer_load_dwordx4 v0, %0, s44 offen offset:2048 sc1 lds\n" \
  "v_mfma_f32_16x16x32_bf16 v[92:95], a[184:187], v[8:11], v[92:95]\n" \
  "s_add_i32 s58, s58, 28672\n" \
  "s_waitcnt lgkmcnt(6)\n" \
  "v_mfma_f32_16x16x32_bf16 v[96:99], a[16:19], v[12:15], v[96:99]\n" \
  "ds_read_b128 v[8:11], v2 offset:36864\n" \
  "v_mfma_f32_16x16x32_bf16 v[100:103], a[72:75], v[12:15], v[100:103]\n" \
  "v_mfma_f32_16x16x32_bf16 v[104:107], a[128:131], v[12:15], v[104:107]\n" \
  "v_mfma_f32_16x16x32_bf16 v[108:111], a[184:187], v[12:15], v[108:111]\n" \
  "s_waitcnt lgkmcnt(6)\n" \
  "v_mfma_f32_16x16x32_bf16 v[80:83], a[20:23], v[16:19], v[80:83]\n" \
  "ds_read_b128 v[12:15], v2 offset:51200\n" \
  "v_mfma_f32_16x16x32_bf16 v[84:87], a[76:79], v[16:19], v[84:87]\n" \
  "v_mfma_f32_16x16x32_bf16 v[88:91], a[132:135], v[16:19], v[88:91]\n" \
  "v_mfma_f32_16x16x32_bf16 v[92:95], a[188:191], v[16:19], v[92:95]\n" \
  "s_waitcnt lgkmcnt(6)\n" \
  "v_mfma_f32_16x16x32_bf16 v[96:99], a[20:23], v[20:23], v[96:99]\n" \
  "ds_read_b128 v[16:19], v2 offset:37888\n" \
  "v_mfma_f32_16x16x32_bf16 v[100:103], a[76:79], v[20:23], v[100:103]\n" \
  "v_mfma_f32_16x16x32_bf16 v[104:107], a[132:135], v[20:23], v[104:107]\n" \
  "v_mfma_f32_16x16x32_bf16 v[108:111], a[188:191], v[20:23], v[108:111]\n" \
  "s_waitcnt lgkmcnt(6)\n" \
  "v_mfma_f32_16x16x32_bf16 v[80:83], a[24:27], v[24:27], v[80:83]\n" \
  "ds_read_b128 v[20:23], v2 offset:52224\n" \
  "v_mfma_f32_16x16x32_bf16 v[84:87], a[80:83], v[24:27], v[84:87]\n" \
  "v_mfma_f32_16x16x32_bf16 v[88:91], a[136:139], v[24:27], v[88:91]\n" \
  "v_mfma_f32_16x16x32_bf16 v[92:95], a[192:195], v[24:27], v[92:95]\n" \
  "s_waitcnt lgkmcnt(6)\n" \
  "v_mfma_f32_16x16x32_bf16 v[96:99], a[24:27], v[28:31], v[96:99]\n" \
  "ds_read_b128 v[24:27], v2 offset:38912\n" \
  "v_mfma_f32_16x16x32_bf16 v[100:103], a[80:83], v[28:31], v[100:103]\n" \
  "v_mfma_f32_16x16x32_bf16 v[104:107], a[136:139], v[28:31], v[104:107]\n" \
  "v_mfma_f32_16x16x32_bf16 v[108:111], a[192:195], v[28:31], v[108:111]\n" \
  "s_waitcnt lgkmcnt(6)\n" \
  "v_mfma_f32_16x16x32_bf16 v[80:83], a[28:31], v[32:35], v[80:83]\n" \
  "ds_read_b128 v[28:31], v2 offset:53248\n" \
  "v_mfma_f32_16x16x32_bf16 v[84:87], a[84:87], v[32:35], v[84:87]\n" \
  "v_mfma_f32_16x16x32_bf16 v[88:91], a[140:143], v[32:35], v[88:91]\n" \
  "v_mfma_f32_16x16x32_bf16 v[92:95], a[196:199], v[32:35], v[92:95]\n" \
  "s_waitcnt lgkmcnt(6)\n" \
  "v_mfma_f32_16x16x32_bf16 v[96:99], a[28:31], v[36:39], v[96:99]\n" \
  "ds_read_b128 v[32:35], v2 offset:39936\n" \
  "v_mfma_f32_16x16x32_bf16 v[100:103], a[84:87], v[36:39], v[100:103]\n" \
  "v_mfma_f32_16x16x32_bf16 v[104:107], a[140:143], v[36:39], v[104:107]\n" \
  "v_mfma_f32_16x16x32_bf16 v[108:111], a[196:199], v[36:39], v[108:111]\n" \
  "s_waitcnt lgkmcnt(6)\n" \
  "v_mfma_f32_16x16x32_bf16 v[80:83], a[32:35], v[8:11], v[80:83]\n" \
  "ds_read_b128 v[36:39], v2 offset:54272\n" \
  "v_mfma_f32_16x16x32_bf16 v[84:87], a[88:91], v[8:11], v[84:87]\n" \
  "v_mfma_f32_16x16x32_bf16 v[88:91], a[144:147], v[8:11], v[88:91]\n" \
  "v_mfma_f32_16x16x32_bf16 v[92:95], a[200:203], v[8:11], v[92:95]\n" \
  "s_waitcnt lgkmcnt(6)\n" \
  "v_mfma_f32_16x16x32_bf16 v[96:99], a[32:35], v[12:15], v[96:99]\n" \
  "ds_read_b128 v[8:11], v2 offset:40960\n" \
  "v_mfma_f32_16x16x32_bf16 v[100:103], a[88:91], v[12:15], v[100:103]\n" \
  "v_mfma_f32_16x16x32_bf16 v[104:107], a[144:147], v[12:15], v[104:107]\n" \
  "v_mfma_f32_16x16x32_bf16 v[108:111], a[200:203], v[12:15], v[108:111]\n" \
  "s_waitcnt lgkmcnt(6)\n" \
  "v_mfma_f32_16x16x32_bf16 v[80:83], a[36:39], v[16:19], v[80:83]\n" \
  "ds_read_b128 v[12:15], v2 offset:55296\n" \
  "v_mfma_f32_16x16x32_bf16 v[84:87], a[92:95], v[16:19], v[84:87]\n" \
  "v_mfma_f32_16x16x32_bf16 v[88:91], a[148:151], v[16:19], v[88:91]\n" \
  "v_mfma_f32_16x16x32_bf16 v[92:95], a[204:207], v[16:19], v[92:95]\n" \
  "s_waitcnt lgkmcnt(6)\n" \
  "v_mfma_f32_16x16x32_bf16 v[96:99], a[36:39], v[20:23], v[96:99]\n" \
  "ds_read_b128 v[16:19], v2 offset:41984\n" \
  "v_mfma_f32_16x16x32_bf16 v[100:103], a[92:95], v[20:23], v[100:103]\n" \
  "v_mfma_f32_16x16x32_bf16 v[104:107], a[148:151], v[20:23], v[104:107]\n" \
  "v_mfma_f32_16x16x32_bf16 v[108:111], a[204:207], v[20:23], v[108:111]\n" \
  "s_waitcnt lgkmcnt(6)\n" \
  "v_mfma_f32_16x16x32_bf16 v[80:83], a[40:43], v[24:27], v[80:83]\n" \
  "ds_read_b128 v[20:23], v2 offset:56320\n" \
  "s_waitcnt vmcnt(7)\n" \
  "s_sub_i32 s43, s40, 0\n" \
  "v_mov_b32 v6, s43\n" \
  "buffer_store_dword v6, off, %0, s57 sc1\n" \
  "v_mfma_f32_16x16x32_bf16 v[84:87], a[96:99], v[24:27], v[84:87]\n" \
  "v_mfma_f32_16x16x32_bf16 v[88:91], a[152:155], v[24:27], v[88:91]\n" \
  "v_mfma_f32_16x16x32_bf16 v[92:95], a[208:211], v[24:27], v[92:95]\n" \
  "s_waitcnt lgkmcnt(6)\n" \
  "v_mfma_f32_16x16x32_bf16 v[96:99], a[40:43], v[28:31], v[96:99]\n" \
  "v_mfma_f32_16x16x32_bf16 v[100:103], a[96:99], v[28:31], v[100:103]\n" \
  "v_mfma_f32_16x16x32_bf16 v[104:107], a[152:155], v[28:31], v[104:107]\n" \
  "v_mfma_f32_16x16x32_bf16 v[108:111], a[208:211], v[28:31], v[108:111]\n" \
  "s_waitcnt lgkmcnt(5)\n" \
  "v_mfma_f32_16x16x32_bf16 v[80:83], a[44:47], v[32:35], v[80:83]\n" \
  "v_mfma_f32_16x16x32_bf16 v[84:87], a[100:103], v[32:35], v[84:87]\n" \
  "v_mfma_f32_16x16x32_bf16 v[88:91], a[156:159], v[32:35], v[88:91]\n" \
  "v_mfma_f32_16x16x32_bf16 v[92:95], a[212:215], v[32:35], v[92:95]\n" \
  "s_waitcnt lgkmcnt(4)\n" \
  "v_mfma_f32_16x16x32_bf16 v[96:99], a[44:47], v[36:39], v[96:99]\n" \
  "v_mfma_f32_16x16x32_bf16 v[100:103], a[100:103], v[36:39], v[100:103]\n" \
  "v_mfma_f32_16x16x32_bf16 v[104:107], a[156:159], v[36:39], v[104:107]\n" \
  "v_mfma_f32_16x16x32_bf16 v[108:111], a[212:215], v[36:39], v[108:111]\n" \
  "s_waitcnt lgkmcnt(3)\n" \
  "v_mfma_f32_16x16x32_bf16 v[80:83], a[48:51], v[8:11], v[80:83]\n" \
  "v_mfma_f32_16x16x32_bf16 v[84:87], a[104:107], v[8:11], v[84:87]\n" \
  "v_mfma_f32_16x16x32_bf16 v[88:91], a[160:163], v[8:11], v[88:91]\n" \
  "v_mfma_f32_16x16x32_bf16 v[92:95], a[216:219], v[8:11], v[92:95]\n" \
  "s_waitcnt lgkmcnt(2)\n" \
  "v_mfma_f32_16x16x32_bf16 v[96:99], a[48:51], v[12:15], v[96:99]\n" \
  "v_mfma_f32_16x16x32_bf16 v[100:103], a[104:107], v[12:15], v[100:103]\n" \
  "v_mfma_f32_16x16x32_bf16 v[104:107], a[160:163], v[12:15], v[104:107]\n" \
  "v_mfma_f32_16x16x32_bf16 v[108:111], a[216:219], v[12:15], v[108:111]\n" \
  "s_waitcnt lgkmcnt(1)\n" \
  "v_mfma_f32_16x16x32_bf16 v[80:83], a[52:55], v[16:19], v[80:83]\n" \
  "v_mfma_f32_16x16x32_bf16 v[84:87], a[108:111], v[16:19], v[84:87]\n" \
  "v_mfma_f32_16x16x32_bf16 v[88:91], a[164:167], v[16:19], v[88:91]\n" \
  "v_mfma_f32_16x16x32_bf16 v[92:95], a[220:223], v[16:19], v[92:95]\n" \
  "s_waitcnt lgkmcnt(0)\n" \
  "v_mfma_f32_16x16x32_bf16 v[96:99], a[52:55], v[20:23], v[96:99]\n" \
  "v_mfma_f32_16x16x32_bf16 v[100:103], a[108:111], v[20:23], v[100:103]\n" \
  "v_mfma_f32_16x16x32_bf16 v[104:107], a[164:167], v[20:23], v[104:107]\n" \
  "v_mfma_f32_16x16x32_bf16 v[108:111], a[220:223], v[20:23], v[108:111]\n" \
  "s_waitcnt vmcnt(1)\n" \
  "s_barrier\n" \
  "s_add_i32 s40, s40, 1\n" \
  "s_cmp_eq_u32 s40, 64\n" \
  "s_cbranch_scc1 last_%=\n" \
  "ds_read_b128 v[8:11], v2 offset:0\n" \
  "ds_read_b128 v[12:15], v2 offset:14336\n" \
  "ds_read_b128 v[16:19], v2 offset:1024\n" \
  "ds_read_b128 v[20:23], v2 offset:15360\n" \
  "ds_read_b128 v[24:27], v2 offset:2048\n" \
  "ds_read_b128 v[28:31], v2 offset:16384\n" \
  "ds_read_b128 v[32:35], v2 offset:3072\n" \
  "s_waitcnt lgkmcnt(6)\n" \
  "v_mfma_f32_16x16x32_bf16 v[48:51], a[0:3], v[8:11], 0\n" \
  "ds_read_b128 v[36:39], v2 offset:17408\n" \
  "v_mfma_f32_16x16x32_bf16 v[52:55], a[56:59], v[8:11], 0\n" \
  "v_mfma_f32_16x16x32_bf16 v[56:59], a[112:115], v[8:11], 0\n" \
  "v_mfma_f32_16x16x32_bf16 v[60:63], a[168:171], v[8:11], 0\n" \
  "v_cvt_pk_bf16_f32 v112, v80, v81\n" \
  "s_waitcnt lgkmcnt(6)\n" \
  "v_mfma_f32_16x16x32_bf16 v[64:67], a[0:3], v[12:15], 0\n" \
  "ds_read_b128 v[8:11], v2 offset:4096\n" \
  "v_cvt_pk_bf16_f32 v113, v96, v97\n" \
  "v_mfma_f32_16x16x32_bf16 v[68:71], a[56:59], v[12:15], 0\n" \
  "v_cvt_pk_bf16_f32 v114, v82, v83\n" \
  "v_mfma_f32_16x16x32_bf16 v[72:75], a[112:115], v[12:15], 0\n" \
  "v_cvt_pk_bf16_f32 v115, v98, v99\n" \
  "v_mfma_f32_16x16x32_bf16 v[76:79], a[168:171], v[12:15], 0\n" \
  "buffer_store_dwordx4 v[112:115], v0, %0, s56 offen offset:0 sc1\n" \
  "s_waitcnt lgkmcnt(6)\n" \
  "v_mfma_f32_16x16x32_bf16 v[48:51], a[4:7], v[16:19], v[48:51]\n" \
  "ds_read_b128 v[12:15], v2 offset:18432\n" \
  "v_cvt_pk_bf16_f32 v116, v84, v85\n" \
  "v_mfma_f32_16x16x32_bf16 v[52:55], a[60:63], v[16:19], v[52:55]\n" \
  "v_cvt_pk_bf16_f32 v117, v100, v101\n" \
  "v_mfma_f32_16x16x32_bf16 v[56:59], a[116:119], v[16:19], v[56:59]\n" \
  "v_cvt_pk_bf16_f32 v118, v86, v87\n" \
  "v_mfma_f32_16x16x32_bf16 v[60:63], a[172:175], v[16:19], v[60:63]\n" \
  "v_cvt_pk_bf16_f32 v119, v102, v103\n" \
  "s_waitcnt lgkmcnt(6)\n" \
  "v_mfma_f32_16x16x32_bf16 v[64:67], a[4:7], v[20:23], v[64:67]\n" \
  "ds_read_b128 v[16:19], v2 offset:5120\n" \
  "buffer_store_dwordx4 v[116:119], v0, %0, s56 offen offset:1024 sc1\n" \
  "v_mfma_f32_16x16x32_bf16 v[68:71], a[60:63], v[20:23], v[68:71]\n" \
  "v_cvt_pk_bf16_f32 v120, v88, v89\n" \
  "v_mfma_f32_16x16x32_bf16 v[72:75], a[116:119], v[20:23], v[72:75]\n" \
  "v_cvt_pk_bf16_f32 v121, v104, v105\n" \
  "v_mfma_f32_16x16x32_bf16 v[76:79], a[172:175], v[20:23], v[76:79]\n" \
  "v_cvt_pk_bf16_f32 v122, v90, v91\n" \
  "s_waitcnt lgkmcnt(6)\n" \
  "v_mfma_f32_16x16x32_bf16 v[48:51], a[8:11], v[24:27], v[48:51]\n" \
  "ds_read_b128 v[20:23], v2 offset:19456\n" \
  "v_cvt_pk_bf16_f32 v123, v106, v107\n" \
  "v_mfma_f32_16x16x32_bf16 v[52:55], a[64:67], v[24:27], v[52:55]\n" \
  "buffer_store_dwordx4 v[120:123], v0, %0, s56 offen offset:2048 sc1\n" \
  "v_mfma_f32_16x16x32_bf16 v[56:59], a[120:123], v[24:27], v[56:59]\n" \
  "v_cvt_pk_bf16_f32 v124, v92, v93\n" \
  "v_mfma_f32_16x16x32_bf16 v[60:63], a[176:179], v[24:27], v[60:63]\n" \
  "v_cvt_pk_bf16_f32 v125, v108, v109\n" \
  "s_waitcnt lgkmcnt(6)\n" \
  "v_mfma_f32_16x16x32_bf16 v[64:67], a[8:11], v[28:31], v[64:67]\n" \
  "ds_read_b128 v[24:27], v2 offset:6144\n" \
  "v_cvt_pk_bf16_f32 v126, v94, v95\n" \
  "v_mfma_f32_16x16x32_bf16 v[68:71], a[64:67], v[28:31], v[68:71]\n" \
  "v_cvt_pk_bf16_f32 v127, v110, v111\n" \
  "v_mfma_f32_16x16x32_bf16 v[72:75], a[120:123], v[28:31], v[72:75]\n" \
  "buffer_store_dwordx4 v[124:127], v0, %0, s56 offen offset:3072 sc1\n" \
  "v_mfma_f32_16x16x32_bf16 v[76:79], a[176:179], v[28:31], v[76:79]\n" \
  "s_add_i32 s56, s56, 458752\n" \
  "s_waitcnt lgkmcnt(6)\n" \
  "v_mfma_f32_16x16x32_bf16 v[48:51], a[12:15], v[32:35], v[48:51]\n" \
  "ds_read_b128 v[28:31], v2 offset:20480\n" \
  "v_mfma_f32_16x16x32_bf16 v[52:55], a[68:71], v[32:35], v[52:55]\n" \
  "v_mfma_f32_16x16x32_bf16 v[56:59], a[124:127], v[32:35], v[56:59]\n" \
  "s_add_i32 m0, s59, 28672\n" \
  "s_add_i32 s44, s58, 0\n" \
  "v_mfma_f32_16x16x32_bf16 v[60:63], a[180:183], v[32:35], v[60:63]\n" \
  "buffer_load_dwordx4 v0, %0, s44 offen offset:0 sc1 lds\n" \
  "s_waitcnt lgkmcnt(6)\n" \
  "v_mfma_f32_16x16x32_bf16 v[64:67], a[12:15], v[36:39], v[64:67]\n" \
  "ds_read_b128 v[32:35], v2 offset:7168\n" \
  "buffer_load_dwordx4 v0, %0, s44 offen offset:1024 sc1 lds\n" \
  "v_mfma_f32_16x16x32_bf16 v[68:71], a[68:71], v[36:39], v[68:71]\n" \
  "buffer_load_dwordx4 v0, %0, s44 offen offset:2048 sc1 lds\n" \
  "v_mfma_f32_16x16x32_bf16 v[72:75], a[124:127], v[36:39], v[72:75]\n" \
  "buffer_load_dwordx4 v0, %0, s44 offen offset:3072 sc1 lds\n" \
  "v_mfma_f32_16x16x32_bf16 v[76:79], a[180:183], v[36:39], v[76:79]\n" \
  "s_add_i32 m0, s59, 32768\n" \
  "s_add_i32 s44, s58, 4096\n" \
  "s_waitcnt lgkmcnt(6)\n" \
  "v_mfma_f32_16x16x32_bf16 v[48:51], a[16:19], v[8:11], v[48:51]\n" \
  "ds_read_b128 v[36:39], v2 offset:21504\n" \
  "buffer_load_dwordx4 v0, %0, s44 offen offset:0 sc1 lds\n" \
  "v_mfma_f32_16x16x32_bf16 v[52:55], a[72:75], v[8:11], v[52:55]\n" \
  "buffer_load_dwordx4 v0, %0, s44 offen offset:1024 sc1 lds\n" \
  "v_mfma_f32_16x16x32_bf16 v[56:59], a[128:131], v[8:11], v[56:59]\n" \
  "buffer_load_dwordx4 v0, %0, s44 offen offset:2048 sc1 lds\n" \
  "v_mfma_f32_16x16x32_bf16 v[60:63], a[184:187], v[8:11], v[60:63]\n" \
  "s_add_i32 s58, s58, 28672\n" \
  "s_waitcnt lgkmcnt(6)\n" \
  "v_mfma_f32_16x16x32_bf16 v[64:67], a[16:19], v[12:15], v[64:67]\n" \
  "ds_read_b128 v[8:11], v2 offset:8192\n" \
  "v_mfma_f32_16x16x32_bf16 v[68:71], a[72:75], v[12:15], v[68:71]\n" \
  "v_mfma_f32_16x16x32_bf16 v[72:75], a[128:131], v[12:15], v[72:75]\n" \
  "v_mfma_f32_16x16x32_bf16 v[76:79], a[184:187], v[12:15], v[76:79]\n" \
  "s_waitcnt lgkmcnt(6)\n" \
  "v_mfma_f32_16x16x32_bf16 v[48:51], a[20:23], v[16:19], v[48:51]\n" \
  "ds_read_b128 v[12:15], v2 offset:22528\n" \
  "v_mfma_f32_16x16x32_bf16 v[52:55], a[76:79], v[16:19], v[52:55]\n" \
  "v_mfma_f32_16x16x32_bf16 v[56:59], a[132:135], v[16:19], v[56:59]\n" \
  "v_mfma_f32_16x16x32_bf16 v[60:63], a[188:191], v[16:19], v[60:63]\n" \
  "s_waitcnt lgkmcnt(6)\n" \
  "v_mfma_f32_16x16x32_bf16 v[64:67], a[20:23], v[20:23], v[64:67]\n" \
  "ds_read_b128 v[16:19], v2 offset:9216\n" \
  "v_mfma_f32_16x16x32_bf16 v[68:71], a[76:79], v[20:23], v[68:71]\n" \
  "v_mfma_f32_16x16x32_bf16 v[72:75], a[132:135], v[20:23], v[72:75]\n" \
  "v_mfma_f32_16x16x32_bf16 v[76:79], a[188:191], v[20:23], v[76:79]\n" \
  "s_waitcnt lgkmcnt(6)\n" \
  "v_mfma_f32_16x16x32_bf16 v[48:51], a[24:27], v[24:27], v[48:51]\n" \
  "ds_read_b128 v[20:23], v2 offset:23552\n" \
  "v_mfma_f32_16x16x32_bf16 v[52:55], a[80:83], v[24:27], v[52:55]\n" \
  "v_mfma_f32_16x16x32_bf16 v[56:59], a[136:139], v[24:27], v[56:59]\n" \
  "v_mfma_f32_16x16x32_bf16 v[60:63], a[192:195], v[24:27], v[60:63]\n" \
  "s_waitcnt lgkmcnt(6)\n" \
  "v_mfma_f32_16x16x32_bf16 v[64:67], a[24:27], v[28:31], v[64:67]\n" \
  "ds_read_b128 v[24:27], v2 offset:10240\n" \
  "v_mfma_f32_16x16x32_bf16 v[68:71], a[80:83], v[28:31], v[68:71]\n" \
  "v_mfma_f32_16x16x32_bf16 v[72:75], a[136:139], v[28:31], v[72:75]\n" \
  "v_mfma_f32_16x16x32_bf16 v[76:79], a[192:195], v[28:31], v[76:79]\n" \
  "s_waitcnt lgkmcnt(6)\n" \
  "v_mfma_f32_16x16x32_bf16 v[48:51], a[28:31], v[32:35], v[48:51]\n" \
  "ds_read_b128 v[28:31], v2 offset:24576\n" \
  "v_mfma_f32_16x16x32_bf16 v[52:55], a[84:87], v[32:35], v[52:55]\n" \
  "v_mfma_f32_16x16x32_bf16 v[56:59], a[140:143], v[32:35], v[56:59]\n" \
  "v_mfma_f32_16x16x32_bf16 v[60:63], a[196:199], v[32:35], v[60:63]\n" \
  "s_waitcnt lgkmcnt(6)\n" \
  "v_mfma_f32_16x16x32_bf16 v[64:67], a[28:31], v[36:39], v[64:67]\n" \
  "ds_read_b128 v[32:35], v2 offset:11264\n" \
  "v_mfma_f32_16x16x32_bf16 v[68:71], a[84:87], v[36:39], v[68:71]\n" \
  "v_mfma_f32_16x16x32_bf16 v[72:75], a[140:143], v[36:39], v[72:75]\n" \
  "v_mfma_f32_16x16x32_bf16 v[76:79], a[196:199], v[36:39], v[76:79]\n" \
  "s_waitcnt lgkmcnt(6)\n" \
  "v_mfma_f32_16x16x32_bf16 v[48:51], a[32:35], v[8:11], v[48:51]\n" \
  "ds_read_b128 v[36:39], v2 offset:25600\n" \
  "v_mfma_f32_16x16x32_bf16 v[52:55], a[88:91], v[8:11], v[52:55]\n" \
  "v_mfma_f32_16x16x32_bf16 v[56:59], a[144:147], v[8:11], v[56:59]\n" \
  "v_mfma_f32_16x16x32_bf16 v[60:63], a[200:203], v[8:11], v[60:63]\n" \
  "s_waitcnt lgkmcnt(6)\n" \
  "v_mfma_f32_16x16x32_bf16 v[64:67], a[32:35], v[12:15], v[64:67]\n" \
  "ds_read_b128 v[8:11], v2 offset:12288\n" \
  "v_mfma_f32_16x16x32_bf16 v[68:71], a[88:91], v[12:15], v[68:71]\n" \
  "v_mfma_f32_16x16x32_bf16 v[72:75], a[144:147], v[12:15], v[72:75]\n" \
  "v_mfma_f32_16x16x32_bf16 v[76:79], a[200:203], v[12:15], v[76:79]\n" \
  "s_waitcnt lgkmcnt(6)\n" \
  "v_mfma_f32_16x16x32_bf16 v[48:51], a[36:39], v[16:19], v[48:51]\n" \
  "ds_read_b128 v[12:15], v2 offset:26624\n" \
  "v_mfma_f32_16x16x32_bf16 v[52:55], a[92:95], v[16:19], v[52:55]\n" \
  "v_mfma_f32_16x16x32_bf16 v[56:59], a[148:151], v[16:19], v[56:59]\n" \
  "v_mfma_f32_16x16x32_bf16 v[60:63], a[204:207], v[16:19], v[60:63]\n" \
  "s_waitcnt lgkmcnt(6)\n" \
  "v_mfma_f32_16x16x32_bf16 v[64:67], a[36:39], v[20:23], v[64:67]\n" \
  "ds_read_b128 v[16:19], v2 offset:13312\n" \
  "v_mfma_f32_16x16x32_bf16 v[68:71], a[92:95], v[20:23], v[68:71]\n" \
  "v_mfma_f32_16x16x32_bf16 v[72:75], a[148:151], v[20:23], v[72:75]\n" \
  "v_mfma_f32_16x16x32_bf16 v[76:79], a[204:207], v[20:23], v[76:79]\n" \
  "s_waitcnt lgkmcnt(6)\n" \
  "v_mfma_f32_16x16x32_bf16 v[48:51], a[40:43], v[24:27], v[48:51]\n" \
  "ds_read_b128 v[20:23], v2 offset:27648\n" \
  "s_waitcnt vmcnt(7)\n" \
  "s_sub_i32 s43, s40, 0\n" \
  "v_mov_b32 v6, s43\n" \
  "buffer_store_dword v6, off, %0, s57 sc1\n" \
  "v_mfma_f32_16x16x32_bf16 v[52:55], a[96:99], v[24:27], v[52:55]\n" \
  "v_mfma_f32_16x16x32_bf16 v[56:59], a[152:155], v[24:27], v[56:59]\n" \
  "v_mfma_f32_16x16x32_bf16 v[60:63], a[208:211], v[24:27], v[60:63]\n" \
  "s_waitcnt lgkmcnt(6)\n" \
  "v_mfma_f32_16x16x32_bf16 v[64:67], a[40:43], v[28:31], v[64:67]\n" \
  "v_mfma_f32_16x16x32_bf16 v[68:71], a[96:99], v[28:31], v[68:71]\n" \
  "v_mfma_f32_16x16x32_bf16 v[72:75], a[152:155], v[28:31], v[72:75]\n" \
  "v_mfma_f32_16x16x32_bf16 v[76:79], a[208:211], v[28:31], v[76:79]\n" \
  "s_waitcnt lgkmcnt(5)\n" \
  "v_mfma_f32_16x16x32_bf16 v[48:51], a[44:47], v[32:35], v[48:51]\n" \
  "v_mfma_f32_16x16x32_bf16 v[52:55], a[100:103], v[32:35], v[52:55]\n" \
  "v_mfma_f32_16x16x32_bf16 v[56:59], a[156:159], v[32:35], v[56:59]\n" \
  "v_mfma_f32_16x16x32_bf16 v[60:63], a[212:215], v[32:35], v[60:63]\n" \
  "s_waitcnt lgkmcnt(4)\n" \
  "v_mfma_f32_16x16x32_bf16 v[64:67], a[44:47], v[36:39], v[64:67]\n" \
  "v_mfma_f32_16x16x32_bf16 v[68:71], a[100:103], v[36:39], v[68:71]\n" \
  "v_mfma_f32_16x16x32_bf16 v[72:75], a[156:159], v[36:39], v[72:75]\n" \
  "v_mfma_f32_16x16x32_bf16 v[76:79], a[212:215], v[36:39], v[76:79]\n" \
  "s_waitcnt lgkmcnt(3)\n" \
  "v_mfma_f32_16x16x32_bf16 v[48:51], a[48:51], v[8:11], v[48:51]\n" \
  "v_mfma_f32_16x16x32_bf16 v[52:55], a[104:107], v[8:11], v[52:55]\n" \
  "v_mfma_f32_16x16x32_bf16 v[56:59], a[160:163], v[8:11], v[56:59]\n" \
  "v_mfma_f32_16x16x32_bf16 v[60:63], a[216:219], v[8:11], v[60:63]\n" \
  "s_waitcnt lgkmcnt(2)\n" \
  "v_mfma_f32_16x16x32_bf16 v[64:67], a[48:51], v[12:15], v[64:67]\n" \
  "v_mfma_f32_16x16x32_bf16 v[68:71], a[104:107], v[12:15], v[68:71]\n" \
  "v_mfma_f32_16x16x32_bf16 v[72:75], a[160:163], v[12:15], v[72:75]\n" \
  "v_mfma_f32_16x16x32_bf16 v[76:79], a[216:219], v[12:15], v[76:79]\n" \
  "s_waitcnt lgkmcnt(1)\n" \
  "v_mfma_f32_16x16x32_bf16 v[48:51], a[52:55], v[16:19], v[48:51]\n" \
  "v_mfma_f32_16x16x32_bf16 v[52:55], a[108:111], v[16:19], v[52:55]\n" \
  "v_mfma_f32_16x16x32_bf16 v[56:59], a[164:167], v[16:19], v[56:59]\n" \
  "v_mfma_f32_16x16x32_bf16 v[60:63], a[220:223], v[16:19], v[60:63]\n" \
  "s_waitcnt lgkmcnt(0)\n" \
  "v_mfma_f32_16x16x32_bf16 v[64:67], a[52:55], v[20:23], v[64:67]\n" \
  "v_mfma_f32_16x16x32_bf16 v[68:71], a[108:111], v[20:23], v[68:71]\n" \
  "v_mfma_f32_16x16x32_bf16 v[72:75], a[164:167], v[20:23], v[72:75]\n" \
  "v_mfma_f32_16x16x32_bf16 v[76:79], a[220:223], v[20:23], v[76:79]\n" \
  "s_waitcnt vmcnt(1)\n" \
  "s_barrier\n" \
  "s_add_i32 s40, s40, 1\n" \
  "s_branch loop_%=\n" \
  "last_%=:\n" \
  "s_nop 7\n" \
  "v_cvt_pk_bf16_f32 v112, v80, v81\n" \
  "v_cvt_pk_bf16_f32 v113, v96, v97\n" \
  "v_cvt_pk_bf16_f32 v114, v82, v83\n" \
  "v_cvt_pk_bf16_f32 v115, v98, v99\n" \
  "buffer_store_dwordx4 v[112:115], v0, %0, s56 offen offset:0 sc1\n" \
  "v_cvt_pk_bf16_f32 v116, v84, v85\n" \
  "v_cvt_pk_bf16_f32 v117, v100, v101\n" \
  "v_cvt_pk_bf16_f32 v118, v86, v87\n" \
  "v_cvt_pk_bf16_f32 v119, v102, v103\n" \
  "buffer_store_dwordx4 v[116:119], v0, %0, s56 offen offset:1024 sc1\n" \
  "v_cvt_pk_bf16_f32 v120, v88, v89\n" \
  "v_cvt_pk_bf16_f32 v121, v104, v105\n" \
  "v_cvt_pk_bf16_f32 v122, v90, v91\n" \
  "v_cvt_pk_bf16_f32 v123, v106, v107\n" \
  "buffer_store_dwordx4 v[120:123], v0, %0, s56 offen offset:2048 sc1\n" \
  "v_cvt_pk_bf16_f32 v124, v92, v93\n" \
  "v_cvt_pk_bf16_f32 v125, v108, v109\n" \
  "v_cvt_pk_bf16_f32 v126, v94, v95\n" \
  "v_cvt_pk_bf16_f32 v127, v110, v111\n" \
  "buffer_store_dwordx4 v[124:127], v0, %0, s56 offen offset:3072 sc1\n" \
  "s_add_i32 s56, s56, 458752\n" \
  "s_waitcnt vmcnt(0)\n" \
  "s_sub_i32 s43, s40, 0\n" \
  "v_mov_b32 v6, s43\n" \
  "buffer_store_dword v6, off, %0, s57 sc1\n" \
  "s_branch end_%=\n" \
  "tmo_%=:\n" \
  "s_mov_b64 exec, 1\n" \
  "v_mov_b32 v7, 1\n" \
  "buffer_store_dword v7, off, %0, s47 sc1\n" \
  "s_mov_b64 exec, -1\n" \
  "end_%=:\n" \
  "s_waitcnt vmcnt(0) lgkmcnt(0)\n"

#define ASM_GUU \
  "v_mbcnt_lo_u32_b32 v7, -1, 0\n" \
  "v_mbcnt_hi_u32_b32 v7, -1, v7\n" \
  "v_lshlrev_b32 v0, 4, v7\n" \
  "v_and_b32 v6, 8, v7\n" \
  "v_mov_b32 v1, 0x4444\n" \
  "v_mov_b32 v2, 0xeeee\n" \
  "v_cmp_ne_u32 vcc, 0, v6\n" \
  "s_nop 1\n" \
  "v_cndmask_b32 v1, v1, v2, vcc\n" \
  "s_mov_b32 s40, 0\n" \
  "s_mov_b32 s41, 262144\n" \
  "s_mov_b32 s42, %7\n" \
  "s_mov_b32 s47, 104857600\n" \
  "s_mov_b32 s46, 0\n" \
  "s_lshl_b32 s56, %1, 12\n" \
  "s_add_i32 s56, s56, 47185920\n" \
  "s_lshl_b32 s57, %1, 7\n" \
  "s_add_i32 s57, s57, 104884224\n" \
  "s_mul_i32 s58, %2, 7168\n" \
  "s_add_i32 s59, s58, %6\n" \
  "s_add_i32 s58, s58, 13631488\n" \
  "s_add_i32 m0, s59, 0\n" \
  "s_add_i32 s44, s58, 0\n" \
  "buffer_load_dwordx4 v0, %0, s44 offen offset:0 sc1 lds\n" \
  "buffer_load_dwordx4 v0, %0, s44 offen offset:1024 sc1 lds\n" \
  "buffer_load_dwordx4 v0, %0, s44 offen offset:2048 sc1 lds\n" \
  "buffer_load_dwordx4 v0, %0, s44 offen offset:3072 sc1 lds\n" \
  "s_add_i32 m0, s59, 4096\n" \
  "s_add_i32 s44, s58, 4096\n" \
  "buffer_load_dwordx4 v0, %0, s44 offen offset:0 sc1 lds\n" \
  "buffer_load_dwordx4 v0, %0, s44 offen offset:1024 sc1 lds\n" \
  "buffer_load_dwordx4 v0, %0, s44 offen offset:2048 sc1 lds\n" \
  "s_add_i32 s58, s58, 28672\n" \
  "s_mul_i32 s45, %1, 57344\n" \
  "s_add_i32 s45, s45, 6422528\n" \
  "s_add_i32 s60, s45, 0\n" \
  "s_add_i32 s61, s45, 14336\n" \
  "s_add_i32 s62, s45, 28672\n" \
  "s_add_i32 s63, s45, 43008\n" \
  "buffer_load_dwordx4 a[0:3], v0, %0, s60 offen offset:0 sc1\n" \
  "buffer_load_dwordx4 a[56:59], v0, %0, s61 offen offset:0 sc1\n" \
  "buffer_load_dwordx4 a[112:115], v0, %0, s62 offen offset:0 sc1\n" \
  "buffer_load_dwordx4 a[168:171], v0, %0, s63 offen offset:0 sc1\n" \
  "buffer_load_dwordx4 a[4:7], v0, %0, s60 offen offset:1024 sc1\n" \
  "buffer_load_dwordx4 a[60:63], v0, %0, s61 offen offset:1024 sc1\n" \
  "buffer_load_dwordx4 a[116:119], v0, %0, s62 offen offset:1024 sc1\n" \
  "buffer_load_dwordx4 a[172:175], v0, %0, s63 offen offset:1024 sc1\n" \
  "buffer_load_dwordx4 a[8:11], v0, %0, s60 offen offset:2048 sc1\n" \
  "buffer_load_dwordx4 a[64:67], v0, %0, s61 offen offset:2048 sc1\n" \
  "buffer_load_dwordx4 a[120:123], v0, %0, s62 offen offset:2048 sc1\n" \
  "buffer_load_dwordx4 a[176:179], v0, %0, s63 offen offset:2048 sc1\n" \
  "buffer_load_dwordx4 a[12:15], v0, %0, s60 offen offset:3072 sc1\n" \
  "buffer_load_dwordx4 a[68:71], v0, %0, s61 offen offset:3072 sc1\n" \
  "buffer_load_dwordx4 a[124:127], v0, %0, s62 offen offset:3072 sc1\n" \
  "buffer_load_dwordx4 a[180:183], v0, %0, s63 offen offset:3072 sc1\n" \
  "s_add_i32 s60, s60, 4096\n" \
  "s_add_i32 s61, s61, 4096\n" \
  "s_add_i32 s62, s62, 4096\n" \
  "s_add_i32 s63, s63, 4096\n" \
  "buffer_load_dwordx4 a[16:19], v0, %0, s60 offen offset:0 sc1\n" \
  "buffer_load_dwordx4 a[72:75], v0, %0, s61 offen offset:0 sc1\n" \
  "buffer_load_dwordx4 a[128:131], v0, %0, s62 offen offset:0 sc1\n" \
  "buffer_load_dwordx4 a[184:187], v0, %0, s63 offen offset:0 sc1\n" \
  "buffer_load_dwordx4 a[20:23], v0, %0, s60 offen offset:1024 sc1\n" \
  "buffer_load_dwordx4 a[76:79], v0, %0, s61 offen offset:1024 sc1\n" \
  "buffer_load_dwordx4 a[132:135], v0, %0, s62 offen offset:1024 sc1\n" \
  "buffer_load_dwordx4 a[188:191], v0, %0, s63 offen offset:1024 sc1\n" \
  "buffer_load_dwordx4 a[24:27], v0, %0, s60 offen offset:2048 sc1\n" \
  "buffer_load_dwordx4 a[80:83], v0, %0, s61 offen offset:2048 sc1\n" \
  "buffer_load_dwordx4 a[136:139], v0, %0, s62 offen offset:2048 sc1\n" \
  "buffer_load_dwordx4 a[192:195], v0, %0, s63 offen offset:2048 sc1\n" \
  "buffer_load_dwordx4 a[28:31], v0, %0, s60 offen offset:3072 sc1\n" \
  "buffer_load_dwordx4 a[84:87], v0, %0, s61 offen offset:3072 sc1\n" \
  "buffer_load_dwordx4 a[140:143], v0, %0, s62 offen offset:3072 sc1\n" \
  "buffer_load_dwordx4 a[196:199], v0, %0, s63 offen offset:3072 sc1\n" \
  "s_add_i32 s60, s60, 4096\n" \
  "s_add_i32 s61, s61, 4096\n" \
  "s_add_i32 s62, s62, 4096\n" \
  "s_add_i32 s63, s63, 4096\n" \
  "buffer_load_dwordx4 a[32:35], v0, %0, s60 offen offset:0 sc1\n" \
  "buffer_load_dwordx4 a[88:91], v0, %0, s61 offen offset:0 sc1\n" \
  "buffer_load_dwordx4 a[144:147], v0, %0, s62 offen offset:0 sc1\n" \
  "buffer_load_dwordx4 a[200:203], v0, %0, s63 offen offset:0 sc1\n" \
  "buffer_load_dwordx4 a[36:39], v0, %0, s60 offen offset:1024 sc1\n" \
  "buffer_load_dwordx4 a[92:95], v0, %0, s61 offen offset:1024 sc1\n" \
  "buffer_load_dwordx4 a[148:151], v0, %0, s62 offen offset:1024 sc1\n" \
  "buffer_load_dwordx4 a[204:207], v0, %0, s63 offen offset:1024 sc1\n" \
  "buffer_load_dwordx4 a[40:43], v0, %0, s60 offen offset:2048 sc1\n" \
  "buffer_load_dwordx4 a[96:99], v0, %0, s61 offen offset:2048 sc1\n" \
  "buffer_load_dwordx4 a[152:155], v0, %0, s62 offen offset:2048 sc1\n" \
  "buffer_load_dwordx4 a[208:211], v0, %0, s63 offen offset:2048 sc1\n" \
  "buffer_load_dwordx4 a[44:47], v0, %0, s60 offen offset:3072 sc1\n" \
  "buffer_load_dwordx4 a[100:103], v0, %0, s61 offen offset:3072 sc1\n" \
  "buffer_load_dwordx4 a[156:159], v0, %0, s62 offen offset:3072 sc1\n" \
  "buffer_load_dwordx4 a[212:215], v0, %0, s63 offen offset:3072 sc1\n" \
  "s_add_i32 s60, s60, 4096\n" \
  "s_add_i32 s61, s61, 4096\n" \
  "s_add_i32 s62, s62, 4096\n" \
  "s_add_i32 s63, s63, 4096\n" \
  "buffer_load_dwordx4 a[48:51], v0, %0, s60 offen offset:0 sc1\n" \
  "buffer_load_dwordx4 a[104:107], v0, %0, s61 offen offset:0 sc1\n" \
  "buffer_load_dwordx4 a[160:163], v0, %0, s62 offen offset:0 sc1\n" \
  "buffer_load_dwordx4 a[216:219], v0, %0, s63 offen offset:0 sc1\n" \
  "buffer_load_dwordx4 a[52:55], v0, %0, s60 offen offset:1024 sc1\n" \
  "buffer_load_dwordx4 a[108:111], v0, %0, s61 offen offset:1024 sc1\n" \
  "buffer_load_dwordx4 a[164:167], v0, %0, s62 offen offset:1024 sc1\n" \
  "buffer_load_dwordx4 a[220:223], v0, %0, s63 offen offset:1024 sc1\n" \
  "v_add_u32 v2, %6, v0\n" \
  "s_waitcnt vmcnt(56)\n" \
  "s_barrier\n" \
  "ds_read_b128 v[8:11], v2 offset:0\n" \
  "ds_read_b128 v[12:15], v2 offset:14336\n" \
  "ds_read_b128 v[16:19], v2 offset:1024\n" \
  "ds_read_b128 v[20:23], v2 offset:15360\n" \
  "ds_read_b128 v[24:27], v2 offset:2048\n" \
  "ds_read_b128 v[28:31], v2 offset:16384\n" \
  "ds_read_b128 v[32:35], v2 offset:3072\n" \
  "s_waitcnt vmcnt(52)\n" \
  "s_waitcnt lgkmcnt(6)\n" \
  "v_mfma_f32_16x16x32_bf16 v[48:51], v[8:11], a[0:3], 0\n" \
  "ds_read_b128 v[36:39], v2 offset:17408\n" \
  "v_mfma_f32_16x16x32_bf16 v[52:55], v[8:11], a[56:59], 0\n" \
  "v_mfma_f32_16x16x32_bf16 v[56:59], v[8:11], a[112:115], 0\n" \
  "v_mfma_f32_16x16x32_bf16 v[60:63], v[8:11], a[168:171], 0\n" \
  "s_waitcnt lgkmcnt(6)\n" \
  "v_mfma_f32_16x16x32_bf16 v[64:67], v[12:15], a[0:3], 0\n" \
  "ds_read_b128 v[8:11], v2 offset:4096\n" \
  "v_mfma_f32_16x16x32_bf16 v[68:71], v[12:15], a[56:59], 0\n" \
  "v_mfma_f32_16x16x32_bf16 v[72:75], v[12:15], a[112:115], 0\n" \
  "v_mfma_f32_16x16x32_bf16 v[76:79], v[12:15], a[168:171], 0\n" \
  "s_waitcnt vmcnt(48)\n" \
  "s_waitcnt lgkmcnt(6)\n" \
  "v_mfma_f32_16x16x32_bf16 v[48:51], v[16:19], a[4:7], v[48:51]\n" \
  "ds_read_b128 v[12:15], v2 offset:18432\n" \
  "v_mfma_f32_16x16x32_bf16 v[52:55], v[16:19], a[60:63], v[52:55]\n" \
  "v_mfma_f32_16x16x32_bf16 v[56:59], v[16:19], a[116:119], v[56:59]\n" \
  "v_mfma_f32_16x16x32_bf16 v[60:63], v[16:19], a[172:175], v[60:63]\n" \
  "s_waitcnt lgkmcnt(6)\n" \
  "v_mfma_f32_16x16x32_bf16 v[64:67], v[20:23], a[4:7], v[64:67]\n" \
  "ds_read_b128 v[16:19], v2 offset:5120\n" \
  "v_mfma_f32_16x16x32_bf16 v[68:71], v[20:23], a[60:63], v[68:71]\n" \
  "v_mfma_f32_16x16x32_bf16 v[72:75], v[20:23], a[116:119], v[72:75]\n" \
  "v_mfma_f32_16x16x32_bf16 v[76:79], v[20:23], a[172:175], v[76:79]\n" \
  "s_waitcnt vmcnt(44)\n" \
  "s_waitcnt lgkmcnt(6)\n" \
  "v_mfma_f32_16x16x32_bf16 v[48:51], v[24:27], a[8:11], v[48:51]\n" \
  "ds_read_b128 v[20:23], v2 offset:19456\n" \
  "v_mfma_f32_16x16x32_bf16 v[52:55], v[24:27], a[64:67], v[52:55]\n" \
  "v_mfma_f32_16x16x32_bf16 v[56:59], v[24:27], a[120:123], v[56:59]\n" \
  "v_mfma_f32_16x16x32_bf16 v[60:63], v[24:27], a[176:179], v[60:63]\n" \
  "s_waitcnt lgkmcnt(6)\n" \
  "v_mfma_f32_16x16x32_bf16 v[64:67], v[28:31], a[8:11], v[64:67]\n" \
  "ds_read_b128 v[24:27], v2 offset:6144\n" \
  "v_mfma_f32_16x16x32_bf16 v[68:71], v[28:31], a[64:67], v[68:71]\n" \
  "v_mfma_f32_16x16x32_bf16 v[72:75], v[28:31], a[120:123], v[72:75]\n" \
  "v_mfma_f32_16x16x32_bf16 v[76:79], v[28:31], a[176:179], v[76:79]\n" \
  "s_waitcnt vmcnt(40)\n" \
  "s_waitcnt lgkmcnt(6)\n" \
  "v_mfma_f32_16x16x32_bf16 v[48:51], v[32:35], a[12:15], v[48:51]\n" \
  "ds_read_b128 v[28:31], v2 offset:20480\n" \
  "v_mfma_f32_16x16x32_bf16 v[52:55], v[32:35], a[68:71], v[52:55]\n" \
  "v_mfma_f32_16x16x32_bf16 v[56:59], v[32:35], a[124:127], v[56:59]\n" \
  "v_mfma_f32_16x16x32_bf16 v[60:63], v[32:35], a[180:183], v[60:63]\n" \
  "s_waitcnt lgkmcnt(6)\n" \
  "v_mfma_f32_16x16x32_bf16 v[64:67], v[36:39], a[12:15], v[64:67]\n" \
  "ds_read_b128 v[32:35], v2 offset:7168\n" \
  "v_mfma_f32_16x16x32_bf16 v[68:71], v[36:39], a[68:71], v[68:71]\n" \
  "v_mfma_f32_16x16x32_bf16 v[72:75], v[36:39], a[124:127], v[72:75]\n" \
  "v_mfma_f32_16x16x32_bf16 v[76:79], v[36:39], a[180:183], v[76:79]\n" \
  "s_waitcnt vmcnt(36)\n" \
  "s_waitcnt lgkmcnt(6)\n" \
  "v_mfma_f32_16x16x32_bf16 v[48:51], v[8:11], a[16:19], v[48:51]\n" \
  "ds_read_b128 v[36:39], v2 offset:21504\n" \
  "v_mfma_f32_16x16x32_bf16 v[52:55], v[8:11], a[72:75], v[52:55]\n" \
  "v_mfma_f32_16x16x32_bf16 v[56:59], v[8:11], a[128:131], v[56:59]\n" \
  "v_mfma_f32_16x16x32_bf16 v[60:63], v[8:11], a[184:187], v[60:63]\n" \
  "s_waitcnt lgkmcnt(6)\n" \
  "v_mfma_f32_16x16x32_bf16 v[64:67], v[12:15], a[16:19], v[64:67]\n" \
  "ds_read_b128 v[8:11], v2 offset:8192\n" \
  "v_mfma_f32_16x16x32_bf16 v[68:71], v[12:15], a[72:75], v[68:71]\n" \
  "v_mfma_f32_16x16x32_bf16 v[72:75], v[12:15], a[128:131], v[72:75]\n" \
  "v_mfma_f32_16x16x32_bf16 v[76:79], v[12:15], a[184:187], v[76:79]\n" \
  "s_waitcnt vmcnt(32)\n" \
  "s_waitcnt lgkmcnt(6)\n" \
  "v_mfma_f32_16x16x32_bf16 v[48:51], v[16:19], a[20:23], v[48:51]\n" \
  "ds_read_b128 v[12:15], v2 offset:22528\n" \
  "v_mfma_f32_16x16x32_bf16 v[52:55], v[16:19], a[76:79], v[52:55]\n" \
  "v_mfma_f32_16x16x32_bf16 v[56:59], v[16:19], a[132:135], v[56:59]\n" \
  "v_mfma_f32_16x16x32_bf16 v[60:63], v[16:19], a[188:191], v[60:63]\n" \
  "s_waitcnt lgkmcnt(6)\n" \
  "v_mfma_f32_16x16x32_bf16 v[64:67], v[20:23], a[20:23], v[64:67]\n" \
  "ds_read_b128 v[16:19], v2 offset:9216\n" \
  "v_mfma_f32_16x16x32_bf16 v[68:71], v[20:23], a[76:79], v[68:71]\n" \
  "v_mfma_f32_16x16x32_bf16 v[72:75], v[20:23], a[132:135], v[72:75]\n" \
  "v_mfma_f32_16x16x32_bf16 v[76:79], v[20:23], a[188:191], v[76:79]\n" \
  "s_waitcnt vmcnt(28)\n" \
  "s_waitcnt lgkmcnt(6)\n" \
  "v_mfma_f32_16x16x32_bf16 v[48:51], v[24:27], a[24:27], v[48:51]\n" \
  "ds_read_b128 v[20:23], v2 offset:23552\n" \
  "v_mfma_f32_16x16x32_bf16 v[52:55], v[24:27], a[80:83], v[52:55]\n" \
  "v_mfma_f32_16x16x32_bf16 v[56:59], v[24:27], a[136:139], v[56:59]\n" \
  "v_mfma_f32_16x16x32_bf16 v[60:63], v[24:27], a[192:195], v[60:63]\n" \
  "s_waitcnt lgkmcnt(6)\n" \
  "v_mfma_f32_16x16x32_bf16 v[64:67], v[28:31], a[24:27], v[64:67]\n" \
  "ds_read_b128 v[24:27], v2 offset:10240\n" \
  "v_mfma_f32_16x16x32_bf16 v[68:71], v[28:31], a[80:83], v[68:71]\n" \
  "v_mfma_f32_16x16x32_bf16 v[72:75], v[28:31], a[136:139], v[72:75]\n" \
  "v_mfma_f32_16x16x32_bf16 v[76:79], v[28:31], a[192:195], v[76:79]\n" \
  "s_waitcnt vmcnt(24)\n" \
  "s_waitcnt lgkmcnt(6)\n" \
  "v_mfma_f32_16x16x32_bf16 v[48:51], v[32:35], a[28:31], v[48:51]\n" \
  "ds_read_b128 v[28:31], v2 offset:24576\n" \
  "v_mfma_f32_16x16x32_bf16 v[52:55], v[32:35], a[84:87], v[52:55]\n" \
  "v_mfma_f32_16x16x32_bf16 v[56:59], v[32:35], a[140:143], v[56:59]\n" \
  "v_mfma_f32_16x16x32_bf16 v[60:63], v[32:35], a[196:199], v[60:63]\n" \
  "s_waitcnt lgkmcnt(6)\n" \
  "v_mfma_f32_16x16x32_bf16 v[64:67], v[36:39], a[28:31], v[64:67]\n" \
  "ds_read_b128 v[32:35], v2 offset:11264\n" \
  "v_mfma_f32_16x16x32_bf16 v[68:71], v[36:39], a[84:87], v[68:71]\n" \
  "v_mfma_f32_16x16x32_bf16 v[72:75], v[36:39], a[140:143], v[72:75]\n" \
  "v_mfma_f32_16x16x32_bf16 v[76:79], v[36:39], a[196:199], v[76:79]\n" \
  "s_waitcnt vmcnt(20)\n" \
  "s_waitcnt lgkmcnt(6)\n" \
  "v_mfma_f32_16x16x32_bf16 v[48:51], v[8:11], a[32:35], v[48:51]\n" \
  "ds_read_b128 v[36:39], v2 offset:25600\n" \
  "v_mfma_f32_16x16x32_bf16 v[52:55], v[8:11], a[88:91], v[52:55]\n" \
  "v_mfma_f32_16x16x32_bf16 v[56:59], v[8:11], a[144:147], v[56:59]\n" \
  "v_mfma_f32_16x16x32_bf16 v[60:63], v[8:11], a[200:203], v[60:63]\n" \
  "s_waitcnt lgkmcnt(6)\n" \
  "v_mfma_f32_16x16x32_bf16 v[64:67], v[12:15], a[32:35], v[64:67]\n" \
  "ds_read_b128 v[8:11], v2 offset:12288\n" \
  "v_mfma_f32_16x16x32_bf16 v[68:71], v[12:15], a[88:91], v[68:71]\n" \
  "v_mfma_f32_16x16x32_bf16 v[72:75], v[12:15], a[144:147], v[72:75]\n" \
  "v_mfma_f32_16x16x32_bf16 v[76:79], v[12:15], a[200:203], v[76:79]\n" \
  "s_waitcnt vmcnt(16)\n" \
  "s_waitcnt lgkmcnt(6)\n" \
  "v_mfma_f32_16x16x32_bf16 v[48:51], v[16:19], a[36:39], v[48:51]\n" \
  "ds_read_b128 v[12:15], v2 offset:26624\n" \
  "v_mfma_f32_16x16x32_bf16 v[52:55], v[16:19], a[92:95], v[52:55]\n" \
  "v_mfma_f32_16x16x32_bf16 v[56:59], v[16:19], a[148:151], v[56:59]\n" \
  "v_mfma_f32_16x16x32_bf16 v[60:63], v[16:19], a[204:207], v[60:63]\n" \
  "s_waitcnt lgkmcnt(6)\n" \
  "v_mfma_f32_16x16x32_bf16 v[64:67], v[20:23], a[36:39], v[64:67]\n" \
  "ds_read_b128 v[16:19], v2 offset:13312\n" \
  "v_mfma_f32_16x16x32_bf16 v[68:71], v[20:23], a[92:95], v[68:71]\n" \
  "v_mfma_f32_16x16x32_bf16 v[72:75], v[20:23], a[148:151], v[72:75]\n" \
  "v_mfma_f32_16x16x32_bf16 v[76:79], v[20:23], a[204:207], v[76:79]\n" \
  "s_waitcnt vmcnt(12)\n" \
  "s_waitcnt lgkmcnt(6)\n" \
  "v_mfma_f32_16x16x32_bf16 v[48:51], v[24:27], a[40:43], v[48:51]\n" \
  "ds_read_b128 v[20:23], v2 offset:27648\n" \
  "v_mfma_f32_16x16x32_bf16 v[52:55], v[24:27], a[96:99], v[52:55]\n" \
  "v_mfma_f32_16x16x32_bf16 v[56:59], v[24:27], a[152:155], v[56:59]\n" \
  "v_mfma_f32_16x16x32_bf16 v[60:63], v[24:27], a[208:211], v[60:63]\n" \
  "s_waitcnt lgkmcnt(6)\n" \
  "v_mfma_f32_16x16x32_bf16 v[64:67], v[28:31], a[40:43], v[64:67]\n" \
  "v_mfma_f32_16x16x32_bf16 v[68:71], v[28:31], a[96:99], v[68:71]\n" \
  "v_mfma_f32_16x16x32_bf16 v[72:75], v[28:31], a[152:155], v[72:75]\n" \
  "v_mfma_f32_16x16x32_bf16 v[76:79], v[28:31], a[208:211], v[76:79]\n" \
  "s_waitcnt vmcnt(8)\n" \
  "s_waitcnt lgkmcnt(5)\n" \
  "v_mfma_f32_16x16x32_bf16 v[48:51], v[32:35], a[44:47], v[48:51]\n" \
  "v_mfma_f32_16x16x32_bf16 v[52:55], v[32:35], a[100:103], v[52:55]\n" \
  "v_mfma_f32_16x16x32_bf16 v[56:59], v[32:35], a[156:159], v[56:59]\n" \
  "v_mfma_f32_16x16x32_bf16 v[60:63], v[32:35], a[212:215], v[60:63]\n" \
  "s_waitcnt lgkmcnt(4)\n" \
  "v_mfma_f32_16x16x32_bf16 v[64:67], v[36:39], a[44:47], v[64:67]\n" \
  "v_mfma_f32_16x16x32_bf16 v[68:71], v[36:39], a[100:103], v[68:71]\n" \
  "v_mfma_f32_16x16x32_bf16 v[72:75], v[36:39], a[156:159], v[72:75]\n" \
  "v_mfma_f32_16x16x32_bf16 v[76:79], v[36:39], a[212:215], v[76:79]\n" \
  "s_waitcnt vmcnt(4)\n" \
  "s_waitcnt lgkmcnt(3)\n" \
  "v_mfma_f32_16x16x32_bf16 v[48:51], v[8:11], a[48:51], v[48:51]\n" \
  "v_mfma_f32_16x16x32_bf16 v[52:55], v[8:11], a[104:107], v[52:55]\n" \
  "v_mfma_f32_16x16x32_bf16 v[56:59], v[8:11], a[160:163], v[56:59]\n" \
  "v_mfma_f32_16x16x32_bf16 v[60:63], v[8:11], a[216:219], v[60:63]\n" \
  "s_waitcnt lgkmcnt(2)\n" \
  "v_mfma_f32_16x16x32_bf16 v[64:67], v[12:15], a[48:51], v[64:67]\n" \
  "v_mfma_f32_16x16x32_bf16 v[68:71], v[12:15], a[104:107], v[68:71]\n" \
  "v_mfma_f32_16x16x32_bf16 v[72:75], v[12:15], a[160:163], v[72:75]\n" \
  "v_mfma_f32_16x16x32_bf16 v[76:79], v[12:15], a[216:219], v[76:79]\n" \
  "s_waitcnt vmcnt(0)\n" \
  "s_add_i32 m0, s59, 28672\n" \
  "s_add_i32 s44, s58, 0\n" \
  "buffer_load_dwordx4 v0, %0, s44 offen offset:0 sc1 lds\n" \
  "buffer_load_dwordx4 v0, %0, s44 offen offset:1024 sc1 lds\n" \
  "buffer_load_dwordx4 v0, %0, s44 offen offset:2048 sc1 lds\n" \
  "buffer_load_dwordx4 v0, %0, s44 offen offset:3072 sc1 lds\n" \
  "s_add_i32 m0, s59, 32768\n" \
  "s_add_i32 s44, s58, 4096\n" \
  "buffer_load_dwordx4 v0, %0, s44 offen offset:0 sc1 lds\n" \
  "buffer_load_dwordx4 v0, %0, s44 offen offset:1024 sc1 lds\n" \
  "buffer_load_dwordx4 v0, %0, s44 offen offset:2048 sc1 lds\n" \
  "s_add_i32 s58, s58, 28672\n" \
  "s_waitcnt lgkmcnt(1)\n" \
  "v_mfma_f32_16x16x32_bf16 v[48:51], v[16:19], a[52:55], v[48:51]\n" \
  "v_mfma_f32_16x16x32_bf16 v[52:55], v[16:19], a[108:111], v[52:55]\n" \
  "v_mfma_f32_16x16x32_bf16 v[56:59], v[16:19], a[164:167], v[56:59]\n" \
  "v_mfma_f32_16x16x32_bf16 v[60:63], v[16:19], a[220:223], v[60:63]\n" \
  "s_waitcnt lgkmcnt(0)\n" \
  "v_mfma_f32_16x16x32_bf16 v[64:67], v[20:23], a[52:55], v[64:67]\n" \
  "v_mfma_f32_16x16x32_bf16 v[68:71], v[20:23], a[108:111], v[68:71]\n" \
  "v_mfma_f32_16x16x32_bf16 v[72:75], v[20:23], a[164:167], v[72:75]\n" \
  "v_mfma_f32_16x16x32_bf16 v[76:79], v[20:23], a[220:223], v[76:79]\n" \
  "s_waitcnt vmcnt(0)\n" \
  "s_barrier\n" \
  "s_mov_b32 s40, 1\n" \
  "loop_%=:\n" \
  "ds_read_b128 v[8:11], v2 offset:28672\n" \
  "ds_read_b128 v[12:15], v2 offset:43008\n" \
  "ds_read_b128 v[16:19], v2 offset:29696\n" \
  "ds_read_b128 v[20:23], v2 offset:44032\n" \
  "ds_read_b128 v[24:27], v2 offset:30720\n" \
  "ds_read_b128 v[28:31], v2 offset:45056\n" \
  "ds_read_b128 v[32:35], v2 offset:31744\n" \
  "s_waitcnt lgkmcnt(6)\n" \
  "v_mfma_f32_16x16x32_bf16 v[80:83], v[8:11], a[0:3], 0\n" \
  "ds_read_b128 v[36:39], v2 offset:46080\n" \
  "v_mfma_f32_16x16x32_bf16 v[84:87], v[8:11], a[56:59], 0\n" \
  "v_mfma_f32_16x16x32_bf16 v[88:91], v[8:11], a[112:115], 0\n" \
  "v_mfma_f32_16x16x32_bf16 v[92:95], v[8:11], a[168:171], 0\n" \
  "v_cvt_pk_bf16_f32 v112, v48, v49\n" \
  "s_waitcnt lgkmcnt(6)\n" \
  "v_mfma_f32_16x16x32_bf16 v[96:99], v[12:15], a[0:3], 0\n" \
  "ds_read_b128 v[8:11], v2 offset:32768\n" \
  "v_cvt_pk_bf16_f32 v113, v52, v53\n" \
  "v_mfma_f32_16x16x32_bf16 v[100:103], v[12:15], a[56:59], 0\n" \
  "v_cvt_pk_bf16_f32 v114, v50, v51\n" \
  "v_mfma_f32_16x16x32_bf16 v[104:107], v[12:15], a[112:115], 0\n" \
  "v_cvt_pk_bf16_f32 v115, v54, v55\n" \
  "v_mfma_f32_16x16x32_bf16 v[108:111], v[12:15], a[168:171], 0\n" \
  "buffer_store_dwordx4 v[112:115], v0, %0, s56 offen offset:0 sc1\n" \
  "s_waitcnt lgkmcnt(6)\n" \
  "v_mfma_f32_16x16x32_bf16 v[80:83], v[16:19], a[4:7], v[80:83]\n" \
  "ds_read_b128 v[12:15], v2 offset:47104\n" \
  "v_cvt_pk_bf16_f32 v116, v64, v65\n" \
  "v_mfma_f32_16x16x32_bf16 v[84:87], v[16:19], a[60:63], v[84:87]\n" \
  "v_cvt_pk_bf16_f32 v117, v68, v69\n" \
  "v_mfma_f32_16x16x32_bf16 v[88:91], v[16:19], a[116:119], v[88:91]\n" \
  "v_cvt_pk_bf16_f32 v118, v66, v67\n" \
  "v_mfma_f32_16x16x32_bf16 v[92:95], v[16:19], a[172:175], v[92:95]\n" \
  "v_cvt_pk_bf16_f32 v119, v70, v71\n" \
  "s_waitcnt lgkmcnt(6)\n" \
  "v_mfma_f32_16x16x32_bf16 v[96:99], v[20:23], a[4:7], v[96:99]\n" \
  "ds_read_b128 v[16:19], v2 offset:33792\n" \
  "buffer_store_dwordx4 v[116:119], v0, %0, s56 offen offset:1024 sc1\n" \
  "v_mfma_f32_16x16x32_bf16 v[100:103], v[20:23], a[60:63], v[100:103]\n" \
  "v_cvt_pk_bf16_f32 v120, v56, v57\n" \
  "v_mfma_f32_16x16x32_bf16 v[104:107], v[20:23], a[116:119], v[104:107]\n" \
  "v_cvt_pk_bf16_f32 v121, v60, v61\n" \
  "v_mfma_f32_16x16x32_bf16 v[108:111], v[20:23], a[172:175], v[108:111]\n" \
  "v_cvt_pk_bf16_f32 v122, v58, v59\n" \
  "s_waitcnt lgkmcnt(6)\n" \
  "v_mfma_f32_16x16x32_bf16 v[80:83], v[24:27], a[8:11], v[80:83]\n" \
  "ds_read_b128 v[20:23], v2 offset:48128\n" \
  "v_cvt_pk_bf16_f32 v123, v62, v63\n" \
  "v_mfma_f32_16x16x32_bf16 v[84:87], v[24:27], a[64:67], v[84:87]\n" \
  "buffer_store_dwordx4 v[120:123], v0, %0, s56 offen offset:2048 sc1\n" \
  "v_mfma_f32_16x16x32_bf16 v[88:91], v[24:27], a[120:123], v[88:91]\n" \
  "v_cvt_pk_bf16_f32 v124, v72, v73\n" \
  "v_mfma_f32_16x16x32_bf16 v[92:95], v[24:27], a[176:179], v[92:95]\n" \
  "v_cvt_pk_bf16_f32 v125, v76, v77\n" \
  "s_waitcnt lgkmcnt(6)\n" \
  "v_mfma_f32_16x16x32_bf16 v[96:99], v[28:31], a[8:11], v[96:99]\n" \
  "ds_read_b128 v[24:27], v2 offset:34816\n" \
  "v_cvt_pk_bf16_f32 v126, v74, v75\n" \
  "v_mfma_f32_16x16x32_bf16 v[100:103], v[28:31], a[64:67], v[100:103]\n" \
  "v_cvt_pk_bf16_f32 v127, v78, v79\n" \
  "v_mfma_f32_16x16x32_bf16 v[104:107], v[28:31], a[120:123], v[104:107]\n" \
  "buffer_store_dwordx4 v[124:127], v0, %0, s56 offen offset:3072 sc1\n" \
  "v_mfma_f32_16x16x32_bf16 v[108:111], v[28:31], a[176:179], v[108:111]\n" \
  "s_add_i32 s56, s56, 458752\n" \
  "s_waitcnt lgkmcnt(6)\n" \
  "v_mfma_f32_16x16x32_bf16 v[80:83], v[32:35], a[12:15], v[80:83]\n" \
  "ds_read_b128 v[28:31], v2 offset:49152\n" \
  "v_mfma_f32_16x16x32_bf16 v[84:87], v[32:35], a[68:71], v[84:87]\n" \
  "v_mfma_f32_16x16x32_bf16 v[88:91], v[32:35], a[124:127], v[88:91]\n" \
  "s_add_i32 m0, s59, 0\n" \
  "s_add_i32 s44, s58, 0\n" \
  "v_mfma_f32_16x16x32_bf16 v[92:95], v[32:35], a[180:183], v[92:95]\n" \
  "buffer_load_dwordx4 v0, %0, s44 offen offset:0 sc1 lds\n" \
  "s_waitcnt lgkmcnt(6)\n" \
  "v_mfma_f32_16x16x32_bf16 v[96:99], v[36:39], a[12:15], v[96:99]\n" \
  "ds_read_b128 v[32:35], v2 offset:35840\n" \
  "buffer_load_dwordx4 v0, %0, s44 offen offset:1024 sc1 lds\n" \
  "v_mfma_f32_16x16x32_bf16 v[100:103], v[36:39], a[68:71], v[100:103]\n" \
  "buffer_load_dwordx4 v0, %0, s44 offen offset:2048 sc1 lds\n" \
  "v_mfma_f32_16x16x32_bf16 v[104:107], v[36:39], a[124:127], v[104:107]\n" \
  "buffer_load_dwordx4 v0, %0, s44 offen offset:3072 sc1 lds\n" \
  "v_mfma_f32_16x16x32_bf16 v[108:111], v[36:39], a[180:183], v[108:111]\n" \
  "s_add_i32 m0, s59, 4096\n" \
  "s_add_i32 s44, s58, 4096\n" \
  "s_waitcnt lgkmcnt(6)\n" \
  "v_mfma_f32_16x16x32_bf16 v[80:83], v[8:11], a[16:19], v[80:83]\n" \
  "ds_read_b128 v[36:39], v2 offset:50176\n" \
  "buffer_load_dwordx4 v0, %0, s44 offen offset:0 sc1 lds\n" \
  "v_mfma_f32_16x16x32_bf16 v[84:87], v[8:11], a[72:75], v[84:87]\n" \
  "buffer_load_dwordx4 v0, %0, s44 offen offset:1024 sc1 lds\n" \
  "v_mfma_f32_16x16x32_bf16 v[88:91], v[8:11], a[128:131], v[88:91]\n" \
  "buffer_load_dwordx4 v0, %0, s44 offen offset:2048 sc1 lds\n" \
  "v_mfma_f32_16x16x32_bf16 v[92:95], v[8:11], a[184:187], v[92:95]\n" \
  "s_add_i32 s58, s58, 28672\n" \
  "s_waitcnt lgkmcnt(6)\n" \
  "v_mfma_f32_16x16x32_bf16 v[96:99], v[12:15], a[16:19], v[96:99]\n" \
  "ds_read_b128 v[8:11], v2 offset:36864\n" \
  "v_mfma_f32_16x16x32_bf16 v[100:103], v[12:15], a[72:75], v[100:103]\n" \
  "v_mfma_f32_16x16x32_bf16 v[104:107], v[12:15], a[128:131], v[104:107]\n" \
  "v_mfma_f32_16x16x32_bf16 v[108:111], v[12:15], a[184:187], v[108:111]\n" \
  "s_waitcnt lgkmcnt(6)\n" \
  "v_mfma_f32_16x16x32_bf16 v[80:83], v[16:19], a[20:23], v[80:83]\n" \
  "ds_read_b128 v[12:15], v2 offset:51200\n" \
  "v_mfma_f32_16x16x32_bf16 v[84:87], v[16:19], a[76:79], v[84:87]\n" \
  "v_mfma_f32_16x16x32_bf16 v[88:91], v[16:19], a[132:135], v[88:91]\n" \
  "v_mfma_f32_16x16x32_bf16 v[92:95], v[16:19], a[188:191], v[92:95]\n" \
  "s_waitcnt lgkmcnt(6)\n" \
  "v_mfma_f32_16x16x32_bf16 v[96:99], v[20:23], a[20:23], v[96:99]\n" \
  "ds_read_b128 v[16:19], v2 offset:37888\n" \
  "v_mfma_f32_16x16x32_bf16 v[100:103], v[20:23], a[76:79], v[100:103]\n" \
  "v_mfma_f32_16x16x32_bf16 v[104:107], v[20:23], a[132:135], v[104:107]\n" \
  "v_mfma_f32_16x16x32_bf16 v[108:111], v[20:23], a[188:191], v[108:111]\n" \
  "s_waitcnt lgkmcnt(6)\n" \
  "v_mfma_f32_16x16x32_bf16 v[80:83], v[24:27], a[24:27], v[80:83]\n" \
  "ds_read_b128 v[20:23], v2 offset:52224\n" \
  "v_mfma_f32_16x16x32_bf16 v[84:87], v[24:27], a[80:83], v[84:87]\n" \
  "v_mfma_f32_16x16x32_bf16 v[88:91], v[24:27], a[136:139], v[88:91]\n" \
  "v_mfma_f32_16x16x32_bf16 v[92:95], v[24:27], a[192:195], v[92:95]\n" \
  "s_waitcnt lgkmcnt(6)\n" \
  "v_mfma_f32_16x16x32_bf16 v[96:99], v[28:31], a[24:27], v[96:99]\n" \
  "ds_read_b128 v[24:27], v2 offset:38912\n" \
  "v_mfma_f32_16x16x32_bf16 v[100:103], v[28:31], a[80:83], v[100:103]\n" \
  "v_mfma_f32_16x16x32_bf16 v[104:107], v[28:31], a[136:139], v[104:107]\n" \
  "v_mfma_f32_16x16x32_bf16 v[108:111], v[28:31], a[192:195], v[108:111]\n" \
  "s_waitcnt lgkmcnt(6)\n" \
  "v_mfma_f32_16x16x32_bf16 v[80:83], v[32:35], a[28:31], v[80:83]\n" \
  "ds_read_b128 v[28:31], v2 offset:53248\n" \
  "v_mfma_f32_16x16x32_bf16 v[84:87], v[32:35], a[84:87], v[84:87]\n" \
  "v_mfma_f32_16x16x32_bf16 v[88:91], v[32:35], a[140:143], v[88:91]\n" \
  "v_mfma_f32_16x16x32_bf16 v[92:95], v[32:35], a[196:199], v[92:95]\n" \
  "s_waitcnt lgkmcnt(6)\n" \
  "v_mfma_f32_16x16x32_bf16 v[96:99], v[36:39], a[28:31], v[96:99]\n" \
  "ds_read_b128 v[32:35], v2 offset:39936\n" \
  "v_mfma_f32_16x16x32_bf16 v[100:103], v[36:39], a[84:87], v[100:103]\n" \
  "v_mfma_f32_16x16x32_bf16 v[104:107], v[36:39], a[140:143], v[104:107]\n" \
  "v_mfma_f32_16x16x32_bf16 v[108:111], v[36:39], a[196:199], v[108:111]\n" \
  "s_waitcnt lgkmcnt(6)\n" \
  "v_mfma_f32_16x16x32_bf16 v[80:83], v[8:11], a[32:35], v[80:83]\n" \
  "ds_read_b128 v[36:39], v2 offset:54272\n" \
  "v_mfma_f32_16x16x32_bf16 v[84:87], v[8:11], a[88:91], v[84:87]\n" \
  "v_mfma_f32_16x16x32_bf16 v[88:91], v[8:11], a[144:147], v[88:91]\n" \
  "v_mfma_f32_16x16x32_bf16 v[92:95], v[8:11], a[200:203], v[92:95]\n" \
  "s_waitcnt lgkmcnt(6)\n" \
  "v_mfma_f32_16x16x32_bf16 v[96:99], v[12:15], a[32:35], v[96:99]\n" \
  "ds_read_b128 v[8:11], v2 offset:40960\n" \
  "v_mfma_f32_16x16x32_bf16 v[100:103], v[12:15], a[88:91], v[100:103]\n" \
  "v_mfma_f32_16x16x32_bf16 v[104:107], v[12:15], a[144:147], v[104:107]\n" \
  "v_mfma_f32_16x16x32_bf16 v[108:111], v[12:15], a[200:203], v[108:111]\n" \
  "s_waitcnt lgkmcnt(6)\n" \
  "v_mfma_f32_16x16x32_bf16 v[80:83], v[16:19], a[36:39], v[80:83]\n" \
  "ds_read_b128 v[12:15], v2 offset:55296\n" \
  "v_mfma_f32_16x16x32_bf16 v[84:87], v[16:19], a[92:95], v[84:87]\n" \
  "v_mfma_f32_16x16x32_bf16 v[88:91], v[16:19], a[148:151], v[88:91]\n" \
  "v_mfma_f32_16x16x32_bf16 v[92:95], v[16:19], a[204:207], v[92:95]\n" \
  "s_waitcnt lgkmcnt(6)\n" \
  "v_mfma_f32_16x16x32_bf16 v[96:99], v[20:23], a[36:39], v[96:99]\n" \
  "ds_read_b128 v[16:19], v2 offset:41984\n" \
  "v_mfma_f32_16x16x32_bf16 v[100:103], v[20:23], a[92:95], v[100:103]\n" \
  "v_mfma_f32_16x16x32_bf16 v[104:107], v[20:23], a[148:151], v[104:107]\n" \
  "v_mfma_f32_16x16x32_bf16 v[108:111], v[20:23], a[204:207], v[108:111]\n" \
  "s_waitcnt lgkmcnt(6)\n" \
  "v_mfma_f32_16x16x32_bf16 v[80:83], v[24:27], a[40:43], v[80:83]\n" \
  "ds_read_b128 v[20:23], v2 offset:56320\n" \
  "s_waitcnt vmcnt(7)\n" \
  "s_sub_i32 s43, s40, 0\n" \
  "v_mov_b32 v6, s43\n" \
  "buffer_store_dword v6, off, %0, s57 sc1\n" \
  "v_mfma_f32_16x16x32_bf16 v[84:87], v[24:27], a[96:99], v[84:87]\n" \
  "v_mfma_f32_16x16x32_bf16 v[88:91], v[24:27], a[152:155], v[88:91]\n" \
  "v_mfma_f32_16x16x32_bf16 v[92:95], v[24:27], a[208:211], v[92:95]\n" \
  "s_waitcnt lgkmcnt(6)\n" \
  "v_mfma_f32_16x16x32_bf16 v[96:99], v[28:31], a[40:43], v[96:99]\n" \
  "v_mfma_f32_16x16x32_bf16 v[100:103], v[28:31], a[96:99], v[100:103]\n" \
  "v_mfma_f32_16x16x32_bf16 v[104:107], v[28:31], a[152:155], v[104:107]\n" \
  "v_mfma_f32_16x16x32_bf16 v[108:111], v[28:31], a[208:211], v[108:111]\n" \
  "s_waitcnt lgkmcnt(5)\n" \
  "v_mfma_f32_16x16x32_bf16 v[80:83], v[32:35], a[44:47], v[80:83]\n" \
  "v_mfma_f32_16x16x32_bf16 v[84:87], v[32:35], a[100:103], v[84:87]\n" \
  "v_mfma_f32_16x16x32_bf16 v[88:91], v[32:35], a[156:159], v[88:91]\n" \
  "v_mfma_f32_16x16x32_bf16 v[92:95], v[32:35], a[212:215], v[92:95]\n" \
  "s_waitcnt lgkmcnt(4)\n" \
  "v_mfma_f32_16x16x32_bf16 v[96:99], v[36:39], a[44:47], v[96:99]\n" \
  "v_mfma_f32_16x16x32_bf16 v[100:103], v[36:39], a[100:103], v[100:103]\n" \
  "v_mfma_f32_16x16x32_bf16 v[104:107], v[36:39], a[156:159], v[104:107]\n" \
  "v_mfma_f32_16x16x32_bf16 v[108:111], v[36:39], a[212:215], v[108:111]\n" \
  "s_waitcnt lgkmcnt(3)\n" \
  "v_mfma_f32_16x16x32_bf16 v[80:83], v[8:11], a[48:51], v[80:83]\n" \
  "v_mfma_f32_16x16x32_bf16 v[84:87], v[8:11], a[104:107], v[84:87]\n" \
  "v_mfma_f32_16x16x32_bf16 v[88:91], v[8:11], a[160:163], v[88:91]\n" \
  "v_mfma_f32_16x16x32_bf16 v[92:95], v[8:11], a[216:219], v[92:95]\n" \
  "s_waitcnt lgkmcnt(2)\n" \
  "v_mfma_f32_16x16x32_bf16 v[96:99], v[12:15], a[48:51], v[96:99]\n" \
  "v_mfma_f32_16x16x32_bf16 v[100:103], v[12:15], a[104:107], v[100:103]\n" \
  "v_mfma_f32_16x16x32_bf16 v[104:107], v[12:15], a[160:163], v[104:107]\n" \
  "v_mfma_f32_16x16x32_bf16 v[108:111], v[12:15], a[216:219], v[108:111]\n" \
  "s_waitcnt lgkmcnt(1)\n" \
  "v_mfma_f32_16x16x32_bf16 v[80:83], v[16:19], a[52:55], v[80:83]\n" \
  "v_mfma_f32_16x16x32_bf16 v[84:87], v[16:19], a[108:111], v[84:87]\n" \
  "v_mfma_f32_16x16x32_bf16 v[88:91], v[16:19], a[164:167], v[88:91]\n" \
  "v_mfma_f32_16x16x32_bf16 v[92:95], v[16:19], a[220:223], v[92:95]\n" \
  "s_waitcnt lgkmcnt(0)\n" \
  "v_mfma_f32_16x16x32_bf16 v[96:99], v[20:23], a[52:55], v[96:99]\n" \
  "v_mfma_f32_16x16x32_bf16 v[100:103], v[20:23], a[108:111], v[100:103]\n" \
  "v_mfma_f32_16x16x32_bf16 v[104:107], v[20:23], a[164:167], v[104:107]\n" \
  "v_mfma_f32_16x16x32_bf16 v[108:111], v[20:23], a[220:223], v[108:111]\n" \
  "s_waitcnt vmcnt(1)\n" \
  "s_barrier\n" \
  "s_add_i32 s40, s40, 1\n" \
  "s_cmp_eq_u32 s40, 64\n" \
  "s_cbranch_scc1 last_%=\n" \
  "ds_read_b128 v[8:11], v2 offset:0\n" \
  "ds_read_b128 v[12:15], v2 offset:14336\n" \
  "ds_read_b128 v[16:19], v2 offset:1024\n" \
  "ds_read_b128 v[20:23], v2 offset:15360\n" \
  "ds_read_b128 v[24:27], v2 offset:2048\n" \
  "ds_read_b128 v[28:31], v2 offset:16384\n" \
  "ds_read_b128 v[32:35], v2 offset:3072\n" \
  "s_waitcnt lgkmcnt(6)\n" \
  "v_mfma_f32_16x16x32_bf16 v[48:51], v[8:11], a[0:3], 0\n" \
  "ds_read_b128 v[36:39], v2 offset:17408\n" \
  "v_mfma_f32_16x16x32_bf16 v[52:55], v[8:11], a[56:59], 0\n" \
  "v_mfma_f32_16x16x32_bf16 v[56:59], v[8:11], a[112:115], 0\n" \
  "v_mfma_f32_16x16x32_bf16 v[60:63], v[8:11], a[168:171], 0\n" \
  "v_cvt_pk_bf16_f32 v112, v80, v81\n" \
  "s_waitcnt lgkmcnt(6)\n" \
  "v_mfma_f32_16x16x32_bf16 v[64:67], v[12:15], a[0:3], 0\n" \
  "ds_read_b128 v[8:11], v2 offset:4096\n" \
  "v_cvt_pk_bf16_f32 v113, v84, v85\n" \
  "v_mfma_f32_16x16x32_bf16 v[68:71], v[12:15], a[56:59], 0\n" \
  "v_cvt_pk_bf16_f32 v114, v82, v83\n" \
  "v_mfma_f32_16x16x32_bf16 v[72:75], v[12:15], a[112:115], 0\n" \
  "v_cvt_pk_bf16_f32 v115, v86, v87\n" \
  "v_mfma_f32_16x16x32_bf16 v[76:79], v[12:15], a[168:171], 0\n" \
  "buffer_store_dwordx4 v[112:115], v0, %0, s56 offen offset:0 sc1\n" \
  "s_waitcnt lgkmcnt(6)\n" \
  "v_mfma_f32_16x16x32_bf16 v[48:51], v[16:19], a[4:7], v[48:51]\n" \
  "ds_read_b128 v[12:15], v2 offset:18432\n" \
  "v_cvt_pk_bf16_f32 v116, v96, v97\n" \
  "v_mfma_f32_16x16x32_bf16 v[52:55], v[16:19], a[60:63], v[52:55]\n" \
  "v_cvt_pk_bf16_f32 v117, v100, v101\n" \
  "v_mfma_f32_16x16x32_bf16 v[56:59], v[16:19], a[116:119], v[56:59]\n" \
  "v_cvt_pk_bf16_f32 v118, v98, v99\n" \
  "v_mfma_f32_16x16x32_bf16 v[60:63], v[16:19], a[172:175], v[60:63]\n" \
  "v_cvt_pk_bf16_f32 v119, v102, v103\n" \
  "s_waitcnt lgkmcnt(6)\n" \
  "v_mfma_f32_16x16x32_bf16 v[64:67], v[20:23], a[4:7], v[64:67]\n" \
  "ds_read_b128 v[16:19], v2 offset:5120\n" \
  "buffer_store_dwordx4 v[116:119], v0, %0, s56 offen offset:1024 sc1\n" \
  "v_mfma_f32_16x16x32_bf16 v[68:71], v[20:23], a[60:63], v[68:71]\n" \
  "v_cvt_pk_bf16_f32 v120, v88, v89\n" \
  "v_mfma_f32_16x16x32_bf16 v[72:75], v[20:23], a[116:119], v[72:75]\n" \
  "v_cvt_pk_bf16_f32 v121, v92, v93\n" \
  "v_mfma_f32_16x16x32_bf16 v[76:79], v[20:23], a[172:175], v[76:79]\n" \
  "v_cvt_pk_bf16_f32 v122, v90, v91\n" \
  "s_waitcnt lgkmcnt(6)\n" \
  "v_mfma_f32_16x16x32_bf16 v[48:51], v[24:27], a[8:11], v[48:51]\n" \
  "ds_read_b128 v[20:23], v2 offset:19456\n" \
  "v_cvt_pk_bf16_f32 v123, v94, v95\n" \
  "v_mfma_f32_16x16x32_bf16 v[52:55], v[24:27], a[64:67], v[52:55]\n" \
  "buffer_store_dwordx4 v[120:123], v0, %0, s56 offen offset:2048 sc1\n" \
  "v_mfma_f32_16x16x32_bf16 v[56:59], v[24:27], a[120:123], v[56:59]\n" \
  "v_cvt_pk_bf16_f32 v124, v104, v105\n" \
  "v_mfma_f32_16x16x32_bf16 v[60:63], v[24:27], a[176:179], v[60:63]\n" \
  "v_cvt_pk_bf16_f32 v125, v108, v109\n" \
  "s_waitcnt lgkmcnt(6)\n" \
  "v_mfma_f32_16x16x32_bf16 v[64:67], v[28:31], a[8:11], v[64:67]\n" \
  "ds_read_b128 v[24:27], v2 offset:6144\n" \
  "v_cvt_pk_bf16_f32 v126, v106, v107\n" \
  "v_mfma_f32_16x16x32_bf16 v[68:71], v[28:31], a[64:67], v[68:71]\n" \
  "v_cvt_pk_bf16_f32 v127, v110, v111\n" \
  "v_mfma_f32_16x16x32_bf16 v[72:75], v[28:31], a[120:123], v[72:75]\n" \
  "buffer_store_dwordx4 v[124:127], v0, %0, s56 offen offset:3072 sc1\n" \
  "v_mfma_f32_16x16x32_bf16 v[76:79], v[28:31], a[176:179], v[76:79]\n" \
  "s_add_i32 s56, s56, 458752\n" \
  "s_waitcnt lgkmcnt(6)\n" \
  "v_mfma_f32_16x16x32_bf16 v[48:51], v[32:35], a[12:15], v[48:51]\n" \
  "ds_read_b128 v[28:31], v2 offset:20480\n" \
  "v_mfma_f32_16x16x32_bf16 v[52:55], v[32:35], a[68:71], v[52:55]\n" \
  "v_mfma_f32_16x16x32_bf16 v[56:59], v[32:35], a[124:127], v[56:59]\n" \
  "s_add_i32 m0, s59, 28672\n" \
  "s_add_i32 s44, s58, 0\n" \
  "v_mfma_f32_16x16x32_bf16 v[60:63], v[32:35], a[180:183], v[60:63]\n" \
  "buffer_load_dwordx4 v0, %0, s44 offen offset:0 sc1 lds\n" \
  "s_waitcnt lgkmcnt(6)\n" \
  "v_mfma_f32_16x16x32_bf16 v[64:67], v[36:39], a[12:15], v[64:67]\n" \
  "ds_read_b128 v[32:35], v2 offset:7168\n" \
  "buffer_load_dwordx4 v0, %0, s44 offen offset:1024 sc1 lds\n" \
  "v_mfma_f32_16x16x32_bf16 v[68:71], v[36:39], a[68:71], v[68:71]\n" \
  "buffer_load_dwordx4 v0, %0, s44 offen offset:2048 sc1 lds\n" \
  "v_mfma_f32_16x16x32_bf16 v[72:75], v[36:39], a[124:127], v[72:75]\n" \
  "buffer_load_dwordx4 v0, %0, s44 offen offset:3072 sc1 lds\n" \
  "v_mfma_f32_16x16x32_bf16 v[76:79], v[36:39], a[180:183], v[76:79]\n" \
  "s_add_i32 m0, s59, 32768\n" \
  "s_add_i32 s44, s58, 4096\n" \
  "s_waitcnt lgkmcnt(6)\n" \
  "v_mfma_f32_16x16x32_bf16 v[48:51], v[8:11], a[16:19], v[48:51]\n" \
  "ds_read_b128 v[36:39], v2 offset:21504\n" \
  "buffer_load_dwordx4 v0, %0, s44 offen offset:0 sc1 lds\n" \
  "v_mfma_f32_16x16x32_bf16 v[52:55], v[8:11], a[72:75], v[52:55]\n" \
  "buffer_load_dwordx4 v0, %0, s44 offen offset:1024 sc1 lds\n" \
  "v_mfma_f32_16x16x32_bf16 v[56:59], v[8:11], a[128:131], v[56:59]\n" \
  "buffer_load_dwordx4 v0, %0, s44 offen offset:2048 sc1 lds\n" \
  "v_mfma_f32_16x16x32_bf16 v[60:63], v[8:11], a[184:187], v[60:63]\n" \
  "s_add_i32 s58, s58, 28672\n" \
  "s_waitcnt lgkmcnt(6)\n" \
  "v_mfma_f32_16x16x32_bf16 v[64:67], v[12:15], a[16:19], v[64:67]\n" \
  "ds_read_b128 v[8:11], v2 offset:8192\n" \
  "v_mfma_f32_16x16x32_bf16 v[68:71], v[12:15], a[72:75], v[68:71]\n" \
  "v_mfma_f32_16x16x32_bf16 v[72:75], v[12:15], a[128:131], v[72:75]\n" \
  "v_mfma_f32_16x16x32_bf16 v[76:79], v[12:15], a[184:187], v[76:79]\n" \
  "s_waitcnt lgkmcnt(6)\n" \
  "v_mfma_f32_16x16x32_bf16 v[48:51], v[16:19], a[20:23], v[48:51]\n" \
  "ds_read_b128 v[12:15], v2 offset:22528\n" \
  "v_mfma_f32_16x16x32_bf16 v[52:55], v[16:19], a[76:79], v[52:55]\n" \
  "v_mfma_f32_16x16x32_bf16 v[56:59], v[16:19], a[132:135], v[56:59]\n" \
  "v_mfma_f32_16x16x32_bf16 v[60:63], v[16:19], a[188:191], v[60:63]\n" \
  "s_waitcnt lgkmcnt(6)\n" \
  "v_mfma_f32_16x16x32_bf16 v[64:67], v[20:23], a[20:23], v[64:67]\n" \
  "ds_read_b128 v[16:19], v2 offset:9216\n" \
  "v_mfma_f32_16x16x32_bf16 v[68:71], v[20:23], a[76:79], v[68:71]\n" \
  "v_mfma_f32_16x16x32_bf16 v[72:75], v[20:23], a[132:135], v[72:75]\n" \
  "v_mfma_f32_16x16x32_bf16 v[76:79], v[20:23], a[188:191], v[76:79]\n" \
  "s_waitcnt lgkmcnt(6)\n" \
  "v_mfma_f32_16x16x32_bf16 v[48:51], v[24:27], a[24:27], v[48:51]\n" \
  "ds_read_b128 v[20:23], v2 offset:23552\n" \
  "v_mfma_f32_16x16x32_bf16 v[52:55], v[24:27], a[80:83], v[52:55]\n" \
  "v_mfma_f32_16x16x32_bf16 v[56:59], v[24:27], a[136:139], v[56:59]\n" \
  "v_mfma_f32_16x16x32_bf16 v[60:63], v[24:27], a[192:195], v[60:63]\n" \
  "s_waitcnt lgkmcnt(6)\n" \
  "v_mfma_f32_16x16x32_bf16 v[64:67], v[28:31], a[24:27], v[64:67]\n" \
  "ds_read_b128 v[24:27], v2 offset:10240\n" \
  "v_mfma_f32_16x16x32_bf16 v[68:71], v[28:31], a[80:83], v[68:71]\n" \
  "v_mfma_f32_16x16x32_bf16 v[72:75], v[28:31], a[136:139], v[72:75]\n" \
  "v_mfma_f32_16x16x32_bf16 v[76:79], v[28:31], a[192:195], v[76:79]\n" \
  "s_waitcnt lgkmcnt(6)\n" \
  "v_mfma_f32_16x16x32_bf16 v[48:51], v[32:35], a[28:31], v[48:51]\n" \
  "ds_read_b128 v[28:31], v2 offset:24576\n" \
  "v_mfma_f32_16x16x32_bf16 v[52:55], v[32:35], a[84:87], v[52:55]\n" \
  "v_mfma_f32_16x16x32_bf16 v[56:59], v[32:35], a[140:143], v[56:59]\n" \
  "v_mfma_f32_16x16x32_bf16 v[60:63], v[32:35], a[196:199], v[60:63]\n" \
  "s_waitcnt lgkmcnt(6)\n" \
  "v_mfma_f32_16x16x32_bf16 v[64:67], v[36:39], a[28:31], v[64:67]\n" \
  "ds_read_b128 v[32:35], v2 offset:11264\n" \
  "v_mfma_f32_16x16x32_bf16 v[68:71], v[36:39], a[84:87], v[68:71]\n" \
  "v_mfma_f32_16x16x32_bf16 v[72:75], v[36:39], a[140:143], v[72:75]\n" \
  "v_mfma_f32_16x16x32_bf16 v[76:79], v[36:39], a[196:199], v[76:79]\n" \
  "s_waitcnt lgkmcnt(6)\n" \
  "v_mfma_f32_16x16x32_bf16 v[48:51], v[8:11], a[32:35], v[48:51]\n" \
  "ds_read_b128 v[36:39], v2 offset:25600\n" \
  "v_mfma_f32_16x16x32_bf16 v[52:55], v[8:11], a[88:91], v[52:55]\n" \
  "v_mfma_f32_16x16x32_bf16 v[56:59], v[8:11], a[144:147], v[56:59]\n" \
  "v_mfma_f32_16x16x32_bf16 v[60:63], v[8:11], a[200:203], v[60:63]\n" \
  "s_waitcnt lgkmcnt(6)\n" \
  "v_mfma_f32_16x16x32_bf16 v[64:67], v[12:15], a[32:35], v[64:67]\n" \
  "ds_read_b128 v[8:11], v2 offset:12288\n" \
  "v_mfma_f32_16x16x32_bf16 v[68:71], v[12:15], a[88:91], v[68:71]\n" \
  "v_mfma_f32_16x16x32_bf16 v[72:75], v[12:15], a[144:147], v[72:75]\n" \
  "v_mfma_f32_16x16x32_bf16 v[76:79], v[12:15], a[200:203], v[76:79]\n" \
  "s_waitcnt lgkmcnt(6)\n" \
  "v_mfma_f32_16x16x32_bf16 v[48:51], v[16:19], a[36:39], v[48:51]\n" \
  "ds_read_b128 v[12:15], v2 offset:26624\n" \
  "v_mfma_f32_16x16x32_bf16 v[52:55], v[16:19], a[92:95], v[52:55]\n" \
  "v_mfma_f32_16x16x32_bf16 v[56:59], v[16:19], a[148:151], v[56:59]\n" \
  "v_mfma_f32_16x16x32_bf16 v[60:63], v[16:19], a[204:207], v[60:63]\n" \
  "s_waitcnt lgkmcnt(6)\n" \
  "v_mfma_f32_16x16x32_bf16 v[64:67], v[20:23], a[36:39], v[64:67]\n" \
  "ds_read_b128 v[16:19], v2 offset:13312\n" \
  "v_mfma_f32_16x16x32_bf16 v[68:71], v[20:23], a[92:95], v[68:71]\n" \
  "v_mfma_f32_16x16x32_bf16 v[72:75], v[20:23], a[148:151], v[72:75]\n" \
  "v_mfma_f32_16x16x32_bf16 v[76:79], v[20:23], a[204:207], v[76:79]\n" \
  "s_waitcnt lgkmcnt(6)\n" \
  "v_mfma_f32_16x16x32_bf16 v[48:51], v[24:27], a[40:43], v[48:51]\n" \
  "ds_read_b128 v[20:23], v2 offset:27648\n" \
  "s_waitcnt vmcnt(7)\n" \
  "s_sub_i32 s43, s40, 0\n" \
  "v_mov_b32 v6, s43\n" \
  "buffer_store_dword v6, off, %0, s57 sc1\n" \
  "v_mfma_f32_16x16x32_bf16 v[52:55], v[24:27], a[96:99], v[52:55]\n" \
  "v_mfma_f32_16x16x32_bf16 v[56:59], v[24:27], a[152:155], v[56:59]\n" \
  "v_mfma_f32_16x16x32_bf16 v[60:63], v[24:27], a[208:211], v[60:63]\n" \
  "s_waitcnt lgkmcnt(6)\n" \
  "v_mfma_f32_16x16x32_bf16 v[64:67], v[28:31], a[40:43], v[64:67]\n" \
  "v_mfma_f32_16x16x32_bf16 v[68:71], v[28:31], a[96:99], v[68:71]\n" \
  "v_mfma_f32_16x16x32_bf16 v[72:75], v[28:31], a[152:155], v[72:75]\n" \
  "v_mfma_f32_16x16x32_bf16 v[76:79], v[28:31], a[208:211], v[76:79]\n" \
  "s_waitcnt lgkmcnt(5)\n" \
  "v_mfma_f32_16x16x32_bf16 v[48:51], v[32:35], a[44:47], v[48:51]\n" \
  "v_mfma_f32_16x16x32_bf16 v[52:55], v[32:35], a[100:103], v[52:55]\n" \
  "v_mfma_f32_16x16x32_bf16 v[56:59], v[32:35], a[156:159], v[56:59]\n" \
  "v_mfma_f32_16x16x32_bf16 v[60:63], v[32:35], a[212:215], v[60:63]\n" \
  "s_waitcnt lgkmcnt(4)\n" \
  "v_mfma_f32_16x16x32_bf16 v[64:67], v[36:39], a[44:47], v[64:67]\n" \
  "v_mfma_f32_16x16x32_bf16 v[68:71], v[36:39], a[100:103], v[68:71]\n" \
  "v_mfma_f32_16x16x32_bf16 v[72:75], v[36:39], a[156:159], v[72:75]\n" \
  "v_mfma_f32_16x16x32_bf16 v[76:79], v[36:39], a[212:215], v[76:79]\n" \
  "s_waitcnt lgkmcnt(3)\n" \
  "v_mfma_f32_16x16x32_bf16 v[48:51], v[8:11], a[48:51], v[48:51]\n" \
  "v_mfma_f32_16x16x32_bf16 v[52:55], v[8:11], a[104:107], v[52:55]\n" \
  "v_mfma_f32_16x16x32_bf16 v[56:59], v[8:11], a[160:163], v[56:59]\n" \
  "v_mfma_f32_16x16x32_bf16 v[60:63], v[8:11], a[216:219], v[60:63]\n" \
  "s_waitcnt lgkmcnt(2)\n" \
  "v_mfma_f32_16x16x32_bf16 v[64:67], v[12:15], a[48:51], v[64:67]\n" \
  "v_mfma_f32_16x16x32_bf16 v[68:71], v[12:15], a[104:107], v[68:71]\n" \
  "v_mfma_f32_16x16x32_bf16 v[72:75], v[12:15], a[160:163], v[72:75]\n" \
  "v_mfma_f32_16x16x32_bf16 v[76:79], v[12:15], a[216:219], v[76:79]\n" \
  "s_waitcnt lgkmcnt(1)\n" \
  "v_mfma_f32_16x16x32_bf16 v[48:51], v[16:19], a[52:55], v[48:51]\n" \
  "v_mfma_f32_16x16x32_bf16 v[52:55], v[16:19], a[108:111], v[52:55]\n" \
  "v_mfma_f32_16x16x32_bf16 v[56:59], v[16:19], a[164:167], v[56:59]\n" \
  "v_mfma_f32_16x16x32_bf16 v[60:63], v[16:19], a[220:223], v[60:63]\n" \
  "s_waitcnt lgkmcnt(0)\n" \
  "v_mfma_f32_16x16x32_bf16 v[64:67], v[20:23], a[52:55], v[64:67]\n" \
  "v_mfma_f32_16x16x32_bf16 v[68:71], v[20:23], a[108:111], v[68:71]\n" \
  "v_mfma_f32_16x16x32_bf16 v[72:75], v[20:23], a[164:167], v[72:75]\n" \
  "v_mfma_f32_16x16x32_bf16 v[76:79], v[20:23], a[220:223], v[76:79]\n" \
  "s_waitcnt vmcnt(1)\n" \
  "s_barrier\n" \
  "s_add_i32 s40, s40, 1\n" \
  "s_branch loop_%=\n" \
  "last_%=:\n" \
  "s_nop 7\n" \
  "v_cvt_pk_bf16_f32 v112, v80, v81\n" \
  "v_cvt_pk_bf16_f32 v113, v84, v85\n" \
  "v_cvt_pk_bf16_f32 v114, v82, v83\n" \
  "v_cvt_pk_bf16_f32 v115, v86, v87\n" \
  "buffer_store_dwordx4 v[112:115], v0, %0, s56 offen offset:0 sc1\n" \
  "v_cvt_pk_bf16_f32 v116, v96, v97\n" \
  "v_cvt_pk_bf16_f32 v117, v100, v101\n" \
  "v_cvt_pk_bf16_f32 v118, v98, v99\n" \
  "v_cvt_pk_bf16_f32 v119, v102, v103\n" \
  "buffer_store_dwordx4 v[116:119], v0, %0, s56 offen offset:1024 sc1\n" \
  "v_cvt_pk_bf16_f32 v120, v88, v89\n" \
  "v_cvt_pk_bf16_f32 v121, v92, v93\n" \
  "v_cvt_pk_bf16_f32 v122, v90, v91\n" \
  "v_cvt_pk_bf16_f32 v123, v94, v95\n" \
  "buffer_store_dwordx4 v[120:123], v0, %0, s56 offen offset:2048 sc1\n" \
  "v_cvt_pk_bf16_f32 v124, v104, v105\n" \
  "v_cvt_pk_bf16_f32 v125, v108, v109\n" \
  "v_cvt_pk_bf16_f32 v126, v106, v107\n" \
  "v_cvt_pk_bf16_f32 v127, v110, v111\n" \
  "buffer_store_dwordx4 v[124:127], v0, %0, s56 offen offset:3072 sc1\n" \
  "s_add_i32 s56, s56, 458752\n" \
  "s_waitcnt vmcnt(0)\n" \
  "s_sub_i32 s43, s40, 0\n" \
  "v_mov_b32 v6, s43\n" \
  "buffer_store_dword v6, off, %0, s57 sc1\n" \
  "s_branch end_%=\n" \
  "tmo_%=:\n" \
  "s_mov_b64 exec, 1\n" \
  "v_mov_b32 v7, 1\n" \
  "buffer_store_dword v7, off, %0, s47 sc1\n" \
  "s_mov_b64 exec, -1\n" \
  "end_%=:\n" \
  "s_waitcnt vmcnt(0) lgkmcnt(0)\n"

template <int type>
DEV void fold(const unsigned short* Wa, const unsigned short* Wb, int h, int jj, rsrc_t ws, char* smem, int tid, int b) {
  const int lane = tid & 63, wave = __builtin_amdgcn_readfirstlane(tid >> 6), g = lane >> 4, c = lane & 15;
  (void)b;
  const rsrc_t ra = __builtin_amdgcn_make_buffer_rsrc((void*)Wa, 0, D * NH * DH * 2, 0x00020000);
  const rsrc_t rb = __builtin_amdgcn_make_buffer_rsrc((void*)Wb, 0, D * NH * DH * 2, 0x00020000);
  const unsigned lbase = (unsigned)(size_t)smem;
#pragma unroll
  for (int n = 0; n < 8; ++n) {
    const int piece = wave * 8 + n;
    int voff;
    if (type == 0) { const int r = 2 * piece + (lane >> 5), i = r >> 4, cc = r & 15;
      const int f = 64 * jj + 32 * (i >> 1) + 8 * (cc >> 2) + 4 * (i & 1) + (cc & 3);
      voff = (f * 4096 + h * 256) * 2 + (((lane & 31) ^ cc) << 4); }
    else { const int e = 8 * piece + (lane >> 3); const int swz = (((e >> 1) & 1) | (((e >> 3) & 1) << 1)) << 1;
      voff = ((h * 256 + e) * D + 64 * jj) * 2 + (((lane & 7) ^ swz) << 4); }
    dma16(lbase + piece * 1024, voff, rb, 0);
  }
  const int va = (lane >> 5) * 8192 + (((lane & 31) ^ (lane >> 5)) << 4), sa = wave * 112 * 8192 + h * 512;
  const unsigned abase = lbase + 32768 + wave * 32768;
#pragma unroll
  for (int n = 0; n < 32; ++n) { const int t = (jj + n / 8) % 7; dma16(abase + n * 1024, va ^ ((n & 7) << 5), ra, sa + t * 131072 + (n & 7) * 16384); }
  asm volatile("s_waitcnt vmcnt(24)" ::: "memory");
  bar();
  unsigned br[4];
#pragma unroll
  for (int i = 0; i < 4; ++i) {
    const int q = c >> 2, p = c & 3, swz = (((q >> 1) & 1) | ((g & 1) << 1)) << 1;
    br[i] = type == 0 ? lbase + c * 512 + (((4 * i + g) ^ c) << 4) : lbase + (8 * g + q) * 128 + (((2 * i + (p >> 1)) ^ swz) << 4) + (p & 1) * 8;
  }
  const int vst = (g & 1) * (type == 0 ? 1024 : 16 * 14336) + ((g >> 1) * 16 + c) * 16;
  const int sst = OFF_WF + (type == 0 ? (h * 14 + 2 * jj) * 28672 : (448 + 64 * jj + h) * 14336);
#define FOLD_OPERANDS "s"(ws), "s"(ra), "v"(va), "s"(sa), "s"(abase), "s"(jj), "v"(abase + c * 512 + ((g ^ c) << 4)), \
                      "v"(br[0]), "v"(br[1]), "v"(br[2]), "v"(br[3]), "v"(vst), "s"(sst), "s"(wave * 7), "s"(lbase + wave * 8192)
  if (type == 0) asm volatile(ASM_FOLDG :: FOLD_OPERANDS : FOLD_CLOBBERS);
  else asm volatile(ASM_FOLDU :: FOLD_OPERANDS : FOLD_CLOBBERS);
}

DEV unsigned prep_rows(const unsigned short* x, char* wsp, rsrc_t ws, char* smem, int tid, int b) {
  typedef float f32x2 __attribute__((ext_vector_type(2)));
  const int lane = tid & 63, wave = __builtin_amdgcn_readfirstlane(tid >> 6), g = lane >> 4, c = lane & 15;
  unsigned cls = 0;
  {
    const int qt = (b - 224) * 4 + wave;
    const rsrc_t rx = __builtin_amdgcn_make_buffer_rsrc((void*)x, 0, 2048 * D * 2, 0x00020000);
#pragma unroll
    for (int k = 0; k < 14; ++k) dma16((unsigned)(size_t)smem + wave * 14336 + k * 1024, lane * 16, rx, qt * 14336 + k * 1024);
    drain();
    f32x2 f[56], s = {0.f, 0.f};
#pragma unroll
    for (int k = 0; k < 14; ++k) {
      const u32x4 v = lds16(smem, wave * 14336 + c * 896 + k * 64 + g * 16);
#pragma unroll
      for (int e = 0; e < 4; ++e) { f[4 * k + e] = (f32x2){bf_lo(v[e]), bf_hi(v[e])}; s += f[4 * k + e]; }
    }
    const float mu = sum_x32(sum_x16(s.x + s.y)) * (1.0f / D);
    f32x2 q2 = {0.f, 0.f};
#pragma unroll
    for (int i = 0; i < 56; ++i) { f[i] -= mu; q2 += f[i] * f[i]; }
    const float rs = __builtin_amdgcn_rsqf(sum_x32(sum_x16(q2.x + q2.y)) * (1.0f / D) + 1e-5f);
#pragma unroll
    for (int k = 0; k < 14; ++k) {
      u32x4 o;
#pragma unroll
      for (int e = 0; e < 4; ++e) { const f32x2 y = f[4 * k + e] * rs; o[e] = pk(y.x, y.y); }
      std16(o, ws, lane * 16, OFF_XN + qt * 14336 + k * 1024);
    }
    for (int i = (b - 224) * 256 + tid; i < C_END / 16; i += 32 * 256) std16((u32x4){0, 0, 0, 0}, ws, i * 16, OFF_CTL);
    K1STAMP(2);
    if (wave == 0 && b - 224 < 8) {
      const int w = b - 224, eb = ex_base(wsp);
      const unsigned lowbits = ((unsigned)(size_t)wsp + (unsigned)eb) & 0x1FFFFFu;
      const int wstart = w == 0 ? eb : eb + (int)(((unsigned)w << 21) - lowbits);
      unsigned best[2] = {~0u, ~0u};
      drain();
      for (int t = 0; t < 3; ++t)
#pragma unroll
        for (int k = 0; k < 2; ++k) {
          const unsigned t0 = clk();
          __builtin_amdgcn_sched_barrier(0);
          if (lane == 0) std4(SENT, ws, k * 8192 + t * 256, wstart);
          drain();
          __builtin_amdgcn_sched_barrier(0);
          best[k] = min(best[k], clk() - t0);
        }
      cls = (best[1] < best[0] ? 1u : 0u) ^ ((xcc_id() >> 2) & 1u);
    }
  }
  return cls;
}

#define STAMP(slot) do {} while (0)
struct Tm { DEV void start() {} DEV void lap(int) {} DEV void dump(rsrc_t, int, int) {} };
#define SEGSTAMP(id, s) do {} while (0)
#define TAU_S 1200
#define TAU_V 150

#define CHAIN_OPERANDS(id, tau) "s"(ws), "s"(id), "s"(wave), "s"(ex.base), "s"(ex.w0 & 0x1FFFFFu), "s"(ex.par), "s"((unsigned)(size_t)smem), "s"(tau)
#define ROLE_END() __builtin_amdgcn_endpgm()
#define ASM_ROLE(text, id, tau) do { asm volatile(text :: CHAIN_OPERANDS(id, tau) : CHAIN_CLOBBERS); ROLE_END(); } while (0)

DEV int fresh_lane() { int l; asm volatile("v_mbcnt_lo_u32_b32 %0, -1, 0\n\tv_mbcnt_hi_u32_b32 %0, -1, %0" : "=v"(l)); return l & 63; }

typedef GAS unsigned short* gptr;
DEV gptr late_arg(int i) {
  const __attribute__((address_space(4))) unsigned long long* ka = (const __attribute__((address_space(4))) unsigned long long*)__builtin_amdgcn_kernarg_segment_ptr();
  asm volatile("" : "+s"(ka));
  return (gptr)ka[i];
}

DEV void role_init(int id, int wave, int lane, rsrc_t ws, const Ex& ex, gptr mem0) {
  const u32x4 sv = {SENT, SENT, SENT, SENT};
  const int tid = wave * 64 + lane;
  for (int n = id; n < NSEG * CPS; n += N_INIT) {
    const int o = ex_chunk(ex, n);
    if (n >= R && n < 2 * R) {
      for (int ch = tid; ch < 448; ch += 256) {
        const int ks = ch >> 5, ga = (ch >> 3) & 3;
        const GAS unsigned* src = (const GAS unsigned*)(mem0 + (ch & 7) * D + 32 * ks + 16 * (ga & 1) + 4 * (ga >> 1));
        std16((u32x4){src[0], src[1], src[4], src[5]}, ws, ch * 16, o);
      }
    } else { std16(sv, ws, tid * 16, o); std16(sv, ws, tid * 16 + 4096, o); }
  }
  drain();
  bar();
  if (tid == 0) std4(1u, ws, C_FI + id * FL, OFF_CTL);
}

DEV void role_xs(int h, int wave, int lane, rsrc_t ws) {
  const int g = lane >> 4, c = lane & 15;
  const int voff = lane < 7 ? C_FG + (h * 7 + lane) * FL : C_FGM + h * FL;
  unsigned seen = 0, last = clk() - (1u << 20);
  Tm tm; tm.start();
  for (int s = wave; s < NSEG; s += 4) {
    if (!wait_cached2(ws, voff, lane < 7 ? s : -1, 8, lane, seen)) return;
    u32x4 xq[2][14], ga[14], gb[14];
    const int go = OFF_G + (s * 16 + h) * 28672;
#pragma unroll
    for (int k = 0; k < 14; ++k) {
      xq[0][k] = ldd(ws, lane * 16, OFF_XN + s * 28672 + k * 1024); xq[1][k] = ldd(ws, lane * 16, OFF_XN + s * 28672 + 14336 + k * 1024);
      ga[k] = ldd(ws, lane * 16, go + k * 2048); gb[k] = ldd(ws, lane * 16, go + k * 2048 + 1024);
    }
    __builtin_amdgcn_sched_barrier(0);
    f32x4 s00 = {0, 0, 0, 0}, s10 = {0, 0, 0, 0}, s11 = {0, 0, 0, 0};
#pragma unroll
    for (int k = 0; k < 14; ++k) {
      const u32x4 k0 = {ga[k].x, ga[k].z, gb[k].x, gb[k].z}, k1 = {ga[k].y, ga[k].w, gb[k].y, gb[k].w};
      s00 = mfma(k0, xq[0][k], s00); s10 = mfma(k0, xq[1][k], s10); s11 = mfma(k1, xq[1][k], s11);
    }
    float e[2][8], mx[2];
#pragma unroll
    for (int r = 0; r < 4; ++r) {
      const bool vis = 4 * g + r <= c;
      e[0][r] = vis ? s00[r] : -INFINITY; e[0][4 + r] = -INFINITY;
      e[1][r] = s10[r]; e[1][4 + r] = vis ? s11[r] : -INFINITY;
    }
#pragma unroll
    for (int t = 0; t < 2; ++t) {
      mx[t] = fmaxf(fmaxf(fmaxf(e[t][0], e[t][1]), fmaxf(e[t][2], e[t][3])), fmaxf(fmaxf(e[t][4], e[t][5]), fmaxf(e[t][6], e[t][7])));
      asm volatile("" : "+v"(mx[t]));
    }
    tm.lap(1);
    if (!wait_paced(ws, voff, lane < 7 ? -1 : s, 8, lane, seen, last, s < NSEG - 4 ? 4 * QUIET + 1500 : 4 * QUIET, s == NSEG - 1)) return;
    tm.lap(2);
    u32x4 gm[7];
#pragma unroll
    for (int j = 0; j < 7; ++j) gm[j] = ldd(ws, lane * 16, OFF_GM + (s * 16 + h) * 7168 + j * 1024);
    __builtin_amdgcn_sched_barrier(0);
    f32x4 m[2][2] = {{{0, 0, 0, 0}, {0, 0, 0, 0}}, {{0, 0, 0, 0}, {0, 0, 0, 0}}};
#pragma unroll
    for (int j = 0; j < 7; ++j)
#pragma unroll
      for (int t = 0; t < 2; ++t) { m[t][0] = mfma(gm[j], xq[t][2 * j], m[t][0]); m[t][1] = mfma(gm[j], xq[t][2 * j + 1], m[t][1]); }
    tm.lap(3);
#pragma unroll
    for (int t = 0; t < 2; ++t) {
      float y[4];
#pragma unroll
      for (int r = 0; r < 4; ++r) y[r] = sum_x32(g < 2 ? m[t][0][r] : m[t][1][r]);
      const float mm = max_x32(max_x16(fmaxf(mx[t], fmaxf(fmaxf(y[0], y[1]), fmaxf(y[2], y[3])))));
      float sx = 0.f, sy = 0.f;
#pragma unroll
      for (int r = 0; r < 8; ++r) { e[t][r] = (t || r < 4) ? __builtin_amdgcn_exp2f(e[t][r] - mm) : 0.f; sx += e[t][r]; }
#pragma unroll
      for (int r = 0; r < 4; ++r) { y[r] = __builtin_amdgcn_exp2f(y[r] - mm); sy += y[r]; }
      const float inv = __builtin_amdgcn_rcpf(sum_x16(sum_x32(sx) + sy));
      std16((u32x4){pk(e[t][0] * inv, e[t][1] * inv), pk(e[t][2] * inv, e[t][3] * inv), pk(e[t][4] * inv, e[t][5] * inv), pk(e[t][6] * inv, e[t][7] * inv)},
            ws, lane * 16, OFF_PXX + ((s * 2 + t) * 16 + h) * 1024);
      std8((u32x2){pk(y[0] * inv, y[2] * inv), pk(y[1] * inv, y[3] * inv)}, ws, (lane & 31) * 8, OFF_PXM + ((s * 2 + t) * 16 + h) * 256);
    }
    __builtin_amdgcn_sched_barrier(0);
    SEGSTAMP(h, s);
    tm.lap(4);
    set_flag(ws, C_FP + (wave * 16 + h) * FL, s, lane);
    tm.lap(0);
  }
  tm.dump(ws, 336 + h * 4 + wave, lane);
}

DEV void reduce4(f32x4& a0, f32x4& a1, char* buf, int wave, int lane) {
  if (wave) { *(f32x4*)(buf + (wave - 1) * 2048 + lane * 16) = a0; *(f32x4*)(buf + (wave - 1) * 2048 + 1024 + lane * 16) = a1; }
  bar();
  if (!wave) {
#pragma unroll
    for (int w = 0; w < 3; ++w) { a0 += *(const f32x4*)(buf + w * 2048 + lane * 16); a1 += *(const f32x4*)(buf + w * 2048 + 1024 + lane * 16); }
  }
}

DEV void role_xv(int id, int wave, int lane, rsrc_t ws, char* smem, gptr x) {
  const int cg = id >> 1, par = id & 1, g = lane >> 4, c = lane & 15;
  const int voff = lane < 16 ? ((lane & 8) ? C_FU : C_FUM) + (8 * cg + (lane & 7)) * FL : C_FP + ((((lane - 16) >> 4) * 2 + par) * 16 + (lane & 15)) * FL;
  const rsrc_t rx = __builtin_amdgcn_make_buffer_rsrc((void*)(unsigned short*)x, 0, 2048 * D * 2, 0x00020000);
  const int xoff = (((wave & 1) * 16 + c) * D + cg * 32 + 4 * g) * 2;
  unsigned seen = 0, last = clk() - (1u << 20);
  Tm tm; tm.start();
  for (int s = par; s < NSEG; s += 2) {
    if (wave == 2 && !wait_cached2(ws, voff, (lane & 8) && lane < 16 ? s : -1, 48, lane, seen)) return;
    bar();
    u32x4 r[8];
#pragma unroll
    for (int i = 0; i < 8; ++i) r[i] = ldd(ws, lane * 16, OFF_U + (s * 28 + 2 * cg + (i >> 2)) * 16384 + (wave * 4 + (i & 3)) * 1024);
    const u32x2 xa = __builtin_amdgcn_raw_buffer_load_b64(rx, xoff, s * 32 * D * 2, 0), xb = __builtin_amdgcn_raw_buffer_load_b64(rx, xoff + 32, s * 32 * D * 2, 0);
    __builtin_amdgcn_sched_barrier(0);
    tm.lap(0);
    if (wave == 2 && !wait_paced(ws, voff, lane < 8 || (lane >= 16 && (lane >> 5) == ((s >> 1) & 1)) ? s : -1, 48, lane, seen, last, s < NSEG - 2 ? 2 * QUIET + 500 : QUIET, s == NSEG - 1)) return;
    bar();
    tm.lap(1);
    u32x4 p[2][4], um[2];
    u32x2 lo[2], hi[2];
#pragma unroll
    for (int t = 0; t < 2; ++t) {
#pragma unroll
      for (int i = 0; i < 4; ++i) p[t][i] = ldd(ws, lane * 16, OFF_PXX + ((s * 2 + t) * 16 + wave * 4 + i) * 1024);
      const int o = OFF_PXM + ((s * 2 + t) * 16 + wave * 4) * 256, vo = (lane >> 5) * 256 + (lane & 31) * 8;
      lo[t] = __builtin_amdgcn_raw_buffer_load_b64(ws, vo, o, SC1); hi[t] = __builtin_amdgcn_raw_buffer_load_b64(ws, vo, o + 512, SC1);
      um[t] = ldd(ws, lane * 16, OFF_UM + ((s * 28 + 2 * cg + t) * 4 + wave) * 1024);
    }
    __builtin_amdgcn_sched_barrier(0);
    f32x4 a[2][2] = {{{0, 0, 0, 0}, {0, 0, 0, 0}}, {{0, 0, 0, 0}, {0, 0, 0, 0}}};
#pragma unroll
    for (int q = 0; q < 2; ++q)
#pragma unroll
      for (int t = 0; t < 2; ++t) {
#pragma unroll
        for (int i = 0; i < 4; i += 2) {
          const u32x4 e = r[4 * t + i], f = r[4 * t + i + 1];
          a[q][t] = mfma((u32x4){e.x, e.z, f.x, f.z}, p[q][i], a[q][t]); a[q][t] = mfma((u32x4){e.y, e.w, f.y, f.w}, p[q][i + 1], a[q][t]);
        }
        a[q][t] = mfma(um[t], (u32x4){lo[q].x, lo[q].y, hi[q].x, hi[q].y}, a[q][t]);
      }
    tm.lap(2);
#pragma unroll
    for (int q = 0; q < 2; ++q)
#pragma unroll
      for (int t = 0; t < 2; ++t) *(f32x4*)(smem + ((q * 4 + wave) * 2 + t) * 1024 + lane * 16) = a[q][t];
    bar();
    if (wave < 2) {
      f32x4 a0 = {bf_lo(xa.x), bf_hi(xa.x), bf_lo(xa.y), bf_hi(xa.y)}, a1 = {bf_lo(xb.x), bf_hi(xb.x), bf_lo(xb.y), bf_hi(xb.y)};
#pragma unroll
      for (int w = 0; w < 4; ++w) { a0 += *(const f32x4*)(smem + (wave * 4 + w) * 2048 + lane * 16); a1 += *(const f32x4*)(smem + (wave * 4 + w) * 2048 + 1024 + lane * 16); }
      std16((u32x4){pk(a0[0], a0[1]), pk(a0[2], a0[3]), pk(a1[0], a1[1]), pk(a1[2], a1[3])}, ws, lane * 16, OFF_H + ((s * 2 + wave) * 14 + cg) * 1024);
      __builtin_amdgcn_sched_barrier(0);
      if (!wave) SEGSTAMP(32 + cg, s);
      tm.lap(3);
      set_flag(ws, C_FH + ((par * 2 + wave) * 16 + cg) * FL, s, lane);
    }
    tm.lap(4);
  }
  tm.dump(ws, 224 + id * 4 + wave, lane);
}

DEV u32x4 gather_frag(gptr w, int ld, int k0, int n, int g) {
  u32x4 o;
#pragma unroll
  for (int e = 0; e < 4; ++e) { const int k = k0 + (e >> 1) * 16 + 4 * g + 2 * (e & 1); o[e] = (unsigned)w[k * ld + n] | ((unsigned)w[(k + 1) * ld + n] << 16); }
  return o;
}

DEV void role_m1(int id, int par, int wave, int lane, rsrc_t ws, char* smem, gptr w1, gptr b1) {
  const int fg = id >> 1, qt = id & 1, g = lane >> 4, c = lane & 15;
  if (!wait_flags(ws, C_FG, 1, 0, lane)) return;
  u32x4 W[2][14];
  float bias[8];
#pragma unroll
  for (int t = 0; t < 2; ++t) {
#pragma unroll
    for (int ks = 0; ks < 14; ++ks) W[t][ks] = gather_frag(w1, FF, 32 * ks, 128 * fg + 32 * wave + 16 * t + c, g);
#pragma unroll
    for (int r = 0; r < 4; ++r) bias[4 * t + r] = bf_lo(b1[128 * fg + 32 * wave + 16 * t + 4 * g + r]);
  }
  const int k0 = wave < 2 ? 4 * wave : 8 + 3 * (wave - 2), nk = wave < 2 ? 4 : 3;
  float* red = (float*)(smem + 32768);
  const float rn = wave < 2 ? 1.0f / 128.0f : 1.0f / 96.0f;
  unsigned seen = 0, last = clk() - (1u << 20);
  Tm tm; tm.start();
  for (int s = par; s < NSEG; s += 2) {
    if (wave == 0 && !wait_paced(ws, C_FH + ((par * 2 + qt) * 16 + lane) * FL, s, 14, lane, seen, last, s < NSEG - 2 ? 2 * QUIET + 500 : QUIET, s == NSEG - 1)) return;
    bar();
    tm.lap(0);
    u32x4 hv[4];
#pragma unroll
    for (int k = 0; k < 4; ++k) hv[k] = k < nk ? ldd(ws, lane * 16, OFF_H + ((s * 2 + qt) * 14 + k0 + k) * 1024) : (u32x4){0, 0, 0, 0};
    float f[32], s1 = 0.f;
#pragma unroll
    for (int k = 0; k < 4; ++k)
#pragma unroll
      for (int e = 0; e < 4; ++e) { f[8 * k + 2 * e] = bf_lo(hv[k][e]); f[8 * k + 2 * e + 1] = bf_hi(hv[k][e]); s1 += f[8 * k + 2 * e] + f[8 * k + 2 * e + 1]; }
    const float mw = sum_x32(sum_x16(s1)) * rn;
    float s2 = 0.f, s3 = 0.f;
#pragma unroll
    for (int i = 0; i < 24; ++i) { const float d = f[i] - mw; s2 += d * d; }
#pragma unroll
    for (int i = 24; i < 32; ++i) { const float d = f[i] - mw; s3 += d * d; }
    s2 = sum_x32(sum_x16(nk == 4 ? s2 + s3 : s2));
    tm.lap(1);
    if (g == 0) *(float2*)(red + (s & 2) * 64 + wave * 32 + c * 2) = make_float2(mw, s2);
    bar();
    const float2 q0 = *(const float2*)(red + (s & 2) * 64 + c * 2), q1 = *(const float2*)(red + (s & 2) * 64 + 32 + c * 2),
                 q2 = *(const float2*)(red + (s & 2) * 64 + 64 + c * 2), q3 = *(const float2*)(red + (s & 2) * 64 + 96 + c * 2);
    const float mu = ((q0.x + q1.x) * 128.0f + (q2.x + q3.x) * 96.0f) * (1.0f / D);
    const float d0 = q0.x - mu, d1 = q1.x - mu, d2 = q2.x - mu, d3 = q3.x - mu;
    const float var = (q0.y + q1.y + q2.y + q3.y + (d0 * d0 + d1 * d1) * 128.0f + (d2 * d2 + d3 * d3) * 96.0f) * (1.0f / D);
    const float rs = __builtin_amdgcn_rsqf(var + 1e-5f * (var + 1e-5f));
#pragma unroll
    for (int k = 0; k < 4; ++k)
      if (k < nk) {
        u32x4 o;
#pragma unroll
        for (int e = 0; e < 4; ++e) o[e] = pk((f[8 * k + 2 * e] - mu) * rs, (f[8 * k + 2 * e + 1] - mu) * rs);
        *(u32x4*)(smem + (s & 2) * 7168 + (k0 + k) * 1024 + lane * 16) = o;
      }
    bar();
    tm.lap(2);
    f32x4 a0 = {bias[0], bias[1], bias[2], bias[3]}, a1 = {bias[4], bias[5], bias[6], bias[7]};
#pragma unroll
    for (int ks = 0; ks < 14; ++ks) { const u32x4 y = lds16(smem, (s & 2) * 7168 + ks * 1024 + lane * 16); a0 = mfma(W[0][ks], y, a0); a1 = mfma(W[1][ks], y, a1); }
    float z[8];
#pragma unroll
    for (int r = 0; r < 8; ++r) {
      const float t = r < 4 ? a0[r & 3] : a1[r & 3];
      const float u = t * (1.0f + 0.044715f * t * t) * (-2.0f * 0.7978845608028654f * 1.4426950408889634f);
      z[r] = t * __builtin_amdgcn_rcpf(1.0f + __builtin_amdgcn_exp2f(u));
    }
    std16((u32x4){pk(z[0], z[1]), pk(z[2], z[3]), pk(z[4], z[5]), pk(z[6], z[7])}, ws, lane * 16, OFF_Z + ((s * 2 + qt) * 56 + 4 * fg + wave) * 1024);
    tm.lap(3);
    if (!wave) SEGSTAMP(64 + id, s);
    set_flag(ws, C_FZ + ((par * 2 + qt) * 56 + 4 * fg + wave) * FL, s, lane);
    tm.lap(4);
  }
  if (par == 0) tm.dump(ws, 400 + id, lane + wave);
}

constexpr int M2_SPLIT = 44;
DEV void role_m2(int id, int first, int wave, int lane, rsrc_t ws, char* smem, gptr w2, gptr b2, gptr out) {
  const int cg = id >> 1, qt = id & 1, g = lane >> 4, c = lane & 15;
  if (!wait_flags(ws, C_FG, 1, 0, lane)) return;
  u32x4 W[2][14];
  float bias[8];
#pragma unroll
  for (int t = 0; t < 2; ++t) {
#pragma unroll
    for (int k = 0; k < 14; ++k) W[t][k] = gather_frag(w2, D, 32 * (14 * wave + k), 32 * cg + 16 * t + c, g);
#pragma unroll
    for (int r = 0; r < 4; ++r) bias[4 * t + r] = bf_lo(b2[32 * cg + 16 * t + 4 * g + r]);
  }
  unsigned seen[2] = {0, 0}, last = clk() - (1u << 20);
  Tm tm; tm.start();
  int it = 0;
  for (int s = first; s < NSEG; s += s < M2_SPLIT ? 1 : 2, ++it) {
    tm.lap(3);
    if (wave == 1) {
      const int voff = lane < 56 ? C_FZ + (((s & 1) * 2 + qt) * 56 + lane) * FL : C_FH + (((s & 1) * 2 + qt) * 16 + cg) * FL;
      unsigned sn = s & 1 ? seen[1] : seen[0];
      if (!wait_paced(ws, voff, s, 57, lane, sn, last, s <= M2_SPLIT || s >= NSEG - 2 ? QUIET - 500 : 2 * QUIET + 500, s == NSEG - 1)) return;
      if (s & 1) seen[1] = sn; else seen[0] = sn;
    }
    bar();
    tm.lap(0);
    u32x4 z[14];
#pragma unroll
    for (int k = 0; k < 14; ++k) z[k] = ldd(ws, lane * 16, OFF_Z + ((s * 2 + qt) * 56 + 14 * wave + k) * 1024);
    const u32x4 hv = ldd(ws, wave ? 0x7FFFFFF0 : lane * 16, OFF_H + ((s * 2 + qt) * 14 + cg) * 1024);
    __builtin_amdgcn_sched_barrier(0);
    f32x4 a0 = {0, 0, 0, 0}, a1 = {0, 0, 0, 0};
#pragma unroll
    for (int k = 0; k < 14; ++k) { a0 = mfma(W[0][k], z[k], a0); a1 = mfma(W[1][k], z[k], a1); }
    asm volatile("" : "+v"(a0), "+v"(a1));
    tm.lap(1);
    reduce4(a0, a1, smem + (it & 1) * 8192, wave, lane);
    tm.lap(2);
    if (!wave) {
      gptr o = out + (s * 32 + qt * 16 + c) * D + cg * 32 + 4 * g;
      *(GAS u32x2*)o = (u32x2){pk(a0[0] + bias[0] + bf_lo(hv.x), a0[1] + bias[1] + bf_hi(hv.x)), pk(a0[2] + bias[2] + bf_lo(hv.y), a0[3] + bias[3] + bf_hi(hv.y))};
      *(GAS u32x2*)(o + 16) = (u32x2){pk(a1[0] + bias[4] + bf_lo(hv.z), a1[1] + bias[5] + bf_hi(hv.z)), pk(a1[2] + bias[6] + bf_lo(hv.w), a1[3] + bias[7] + bf_hi(hv.w))};
      SEGSTAMP(96 + id, s);
    }
  }
  if (!first) tm.dump(ws, 430 + id, lane + wave);
}

__global__ void __launch_bounds__(256, 1)
main_kernel(const unsigned short* __restrict__ x, const unsigned short* __restrict__ mem0, const unsigned short* __restrict__ Wq,
            const unsigned short* __restrict__ Wk, const unsigned short* __restrict__ Wv, const unsigned short* __restrict__ Wo,
            const unsigned short* __restrict__ w1, const unsigned short* __restrict__ b1, const unsigned short* __restrict__ w2,
            const unsigned short* __restrict__ b2, unsigned short* __restrict__ out, char* __restrict__ wsp) {
  extern __shared__ __attribute__((aligned(16))) char smem[];
  const int tid = threadIdx.x, lane = tid & 63, wave = __builtin_amdgcn_readfirstlane(tid >> 6), b = blockIdx.x;
  const rsrc_t ws = __builtin_amdgcn_make_buffer_rsrc(wsp, 0, WS_BYTES, 0x00020000);
  const u32x2 found = *(const __attribute__((address_space(4))) u32x2*)(wsp + OFF_CTL + C_EPOCH);
  const unsigned tag_lo = (found.x + 256u) & ~255u, tag_hi = found.y + 1u;
  const unsigned long long tag = (unsigned long long)tag_hi << 32 | tag_lo;
  unsigned wt = 0, cls = 0;
  unsigned guess = (xcc_id() & 7u) * 32u + (b >> 3), taken = 0;
  if (wave == 3) wt = warm_text(b >> 3, lane);
  if (tid == 128) wt = __builtin_amdgcn_raw_buffer_load_b32(ws, C_SLOT + guess * FL + 64, OFF_CTL, SC1);
  if (b < 224) {
    const int grp = (b & 7) * 4 + (b >> 3) / 7, jj = (b >> 3) % 7;
    if (grp < 16) fold<0>(Wk, Wq, grp, jj, ws, smem, tid, b); else fold<1>(Wv, Wo, grp - 16, jj, ws, smem, tid, b);
  } else cls = prep_rows(x, wsp, ws, smem, tid, b);
  asm volatile("" :: "v"(wt));
  K1STAMP(3);
  GAS unsigned long long* ctl = (GAS unsigned long long*)(wsp + OFF_CTL);
  if (tid == 0) taken = __hip_atomic_exchange(ctl + (C_SLOT + guess * FL) / 8, tag, RLX_AGENT) == tag;
  drain();
  bar();
  K1STAMP(4);
  Ex ex;
  ex.base = ex_base(wsp);
  ex.w0 = (unsigned)(size_t)wsp + (unsigned)ex.base;
  if (wave == 0) {
    taken = __builtin_amdgcn_readfirstlane(taken);
    if (lane < 16) std8((u32x2){tag_lo | cls | taken << 1, tag_hi}, ws, C_REC + b * FL + lane * 8, OFF_CTL);
    unsigned spins = 0;
    if (taken) {
      unsigned rank = 0;
      for (int i = 0; i < 256; ++i) {
        unsigned long long r;
        while (((r = __hip_atomic_load(ctl + (C_REC + i * FL) / 8, RLX_AGENT)) >> 8) != (tag >> 8) && ++spins < SPIN_CAP) __builtin_amdgcn_s_sleep(4);
        rank += i < b && (r & 2u);
      }
      guess = 256u;
      for (unsigned i = 0; i < 256u; ++i)
        if (__hip_atomic_load(ctl + (C_SLOT + i * FL) / 8, RLX_AGENT) != tag && rank-- == 0u) { guess = i; break; }
    }
    const int sx = guess >> 5, st = guess & 31, sy = sx & 3, i = lane - 32;
    int grp = -1, jj = 0;
    if (sx < 2 && st < 8) { if (i < 7) { grp = sy * 8 + st; jj = i; } }
    else if (sx < 4) { if (sx >= 2 && st < 14 && i < 16) { grp = 16 + i; jj = ((sy - 2) * 14 + st) >> 2; } }
    else if (st < 7) { if (i < 4) { const int gw = (sy * 7 + st) * 4 + i; grp = gw / 7; jj = gw % 7; } }
    else if (st < 14 && i < 16) { grp = 16 + i; jj = (sy * 7 + st - 7) >> 2; }
    const int src = lane < 32 ? 224 + lane : grp >= 0 ? ((grp & 3) * 7 + jj) * 8 + (grp >> 2) : -1;
    u32x2 r = {tag_lo, tag_hi};
    for (;;) {
      fence_c();
      if (src >= 0) r = __builtin_amdgcn_raw_buffer_load_b64(ws, C_REC + src * FL, OFF_CTL, SC1);
      if (__all(r.y == tag_hi && (r.x ^ tag_lo) < 256u)) break;
      if (++spins > SPIN_CAP) { guess = 256u; break; }
      __builtin_amdgcn_s_sleep(1);
    }
    const unsigned par = (unsigned)__ballot(lane < 8 && (r.x & 1u));
    if (lane == 0) *(u32x2*)smem = (u32x2){guess, par};
  }
  bar();
  const int slot = __builtin_amdgcn_readfirstlane(*(const unsigned*)smem);
  ex.par = __builtin_amdgcn_readfirstlane(*(const unsigned*)(smem + 4));
  bar();
  if (slot >= 256) return;
  const int xc = slot >> 5, t = slot & 31;
  const int y = xc & 3;
  if (xc < 4) {
    if (y < 2 && t < 8 && wave == 0) {
      asm volatile(ASM_SCA :: CHAIN_OPERANDS(y * 8 + t, TAU_S) : CHAIN_CLOBBERS);
      if (slot == 0) std8((u32x2){tag_lo, tag_hi}, ws, C_EPOCH, OFF_CTL);
      ROLE_END();
    }
    if (y < 2 && t < 8) ASM_ROLE(ASM_SCB, y * 8 + t, TAU_S);
    if (y >= 2 && t < 14) ASM_ROLE(ASM_VA, (y - 2) * 14 + t, TAU_V);
    if (t >= 18) { const int l = fresh_lane(); role_init(y * 14 + t - 18, wave, l, ws, ex, late_arg(1));
 role_m1(y * 7 + (t - 18) % 7, (t - 18) / 7, wave, l, ws, smem, late_arg(6), late_arg(7));
 }
  } else {
    if (t >= 7 && t < 14) ASM_ROLE(ASM_GUU, (y * 7 + t - 7) * 4 + wave, 0);
    if (t < 7) { asm volatile(ASM_GUG :: CHAIN_OPERANDS((y * 7 + t) * 4 + wave, 0) : CHAIN_CLOBBERS); bar(); }
    if (t >= 14 && t < 18) role_xs(y * 4 + t - 14, wave, fresh_lane(), ws);
    else if (t >= 18 && t < 25) role_xv(y * 7 + t - 18, wave, fresh_lane(), ws, smem, late_arg(0));
    else role_m2(y * 7 + (t < 7 ? t : t - 25), t < 7 ? M2_SPLIT + 1 : 0, wave, fresh_lane(), ws, smem, late_arg(8), late_arg(9), late_arg(10));
  }
  ROLE_END();
}

__device__ __attribute__((noinline, used, visibility("hidden"))) void text_end_marker() { asm volatile("s_nop 0"); }

extern "C" void kernel_launch(void* const* d_in, const int* in_sizes, int n_in, void* d_out, int out_size, void* d_ws, size_t ws_size,
                              hipStream_t stream) {
  (void)in_sizes; (void)n_in; (void)out_size; (void)ws_size;
  (void)hipFuncSetAttribute((const void*)main_kernel, hipFuncAttributeMaxDynamicSharedMemorySize, LDS_BYTES);
  typedef const unsigned short* P;
  main_kernel<<<256, 256, LDS_BYTES, stream>>>((P)d_in[0], (P)d_in[1], (P)d_in[2], (P)d_in[3], (P)d_in[4], (P)d_in[5], (P)d_in[8], (P)d_in[9],
                                               (P)d_in[10], (P)d_in[11], (unsigned short*)d_out, (char*)d_ws);
}
```
